```python
import torch

def _softmax(x, axis=-1):
    return torch.softmax(x, dim=axis)

def _gelu(x):
    return torch.nn.functional.gelu(x, approximate='tanh')

def _silu(x):
    return torch.nn.functional.silu(x)

def _sigmoid(x):
    return torch.sigmoid(x)

def _layernorm(x, axis=-1, eps=1e-05):
    m = x.mean(dim=axis, keepdim=True)
    v = x.var(dim=axis, keepdim=True, unbiased=False)
    return (x - m) / torch.sqrt(v + eps)

def _scatter_add(tgt, idx, src, axis=0):
    return tgt.clone().index_add_(axis, idx, src)

def _segment_sum(src, seg, n):
    out = torch.zeros((n,) + src.shape[1:], dtype=src.dtype)
    return out.index_add_(0, seg, src)

def _pad(x, pw):
    flat = tuple((v for pair in reversed(pw) for v in pair))
    return torch.nn.functional.pad(x, flat)
kernel_name = 'sequential_preLN__memory_aug_recurrent_mem_tr__glu_gated_reglu__prompt'

def setup_inputs():

    _w = lambda a: (lambda t: t.to(torch.bfloat16) if t.is_floating_point() else t)(torch.as_tensor(a))

    def _A_setup():
        B, S, Dm = (1, 2048, 640)
        H, Dh = (32, 128)
        M, seg = (8, 32)
        seg = min(seg, S)
        S = S // seg * seg
        return {'x': _w((lambda *s: torch.randn(*s, dtype=torch.bfloat16) * 0.05)(B, S, Dm)), 'mem0': _w((lambda *s: torch.randn(*s, dtype=torch.bfloat16) * 0.2)(B, M, Dm)), 'Wq': _w((lambda *s: torch.randn(*s, dtype=torch.bfloat16) * 1.6)(Dm, H, Dh) * Dm ** (-0.5)), 'Wk': _w((lambda *s: torch.randn(*s, dtype=torch.bfloat16) * 1.6)(Dm, H, Dh) * Dm ** (-0.5)), 'Wv': _w((lambda *s: torch.randn(*s, dtype=torch.bfloat16) * 0.2)(Dm, H, Dh) * Dm ** (-0.5)), 'Wo': _w((lambda *s: torch.randn(*s, dtype=torch.bfloat16) * 0.2)(H, Dh, Dm) * (H * Dh) ** (-0.5)), 'seg_len': seg}
    _A_in = _A_setup()

    def _B_setup():
        B, S, D, F = (1, 2048, 640, 1728)
        return {'x': _w((lambda *s: torch.randn(*s, dtype=torch.bfloat16) * 0.05)(B, S, D)), 'W_gate': _w((lambda *s: torch.randn(*s, dtype=torch.bfloat16) * 0.2)(D, F) * (2.0 / D) ** 0.5), 'W_up': _w((lambda *s: torch.randn(*s, dtype=torch.bfloat16) * 0.2)(D, F) * (2.0 / D) ** 0.5), 'W_down': _w((lambda *s: torch.randn(*s, dtype=torch.bfloat16) * 0.2)(F, D) * (1.0 / F) ** 0.5)}
    _B_in = _B_setup()
    return {'A_x': _A_in['x'], 'A_mem0': _A_in['mem0'], 'A_Wq': _A_in['Wq'], 'A_Wk': _A_in['Wk'], 'A_Wv': _A_in['Wv'], 'A_Wo': _A_in['Wo'], 'A_seg_len': _A_in['seg_len'], 'B_x': _B_in['x'], 'B_W_gate': _B_in['W_gate'], 'B_W_up': _B_in['W_up'], 'B_W_down': _B_in['W_down']}

def reference(A_x, A_mem0, A_Wq, A_Wk, A_Wv, A_Wo, A_seg_len, B_x, B_W_gate, B_W_up, B_W_down):

    def _A_ref(x, mem0, Wq, Wk, Wv, Wo, seg_len):
        B, S, Dm = x.shape
        H, Dh = (Wq.shape[1], Wq.shape[2])
        M = mem0.shape[1]
        scale = Dh ** (-0.5)
        n_seg = S // seg_len
        L = M + seg_len
        idx = torch.arange(L)
        mask = (idx[:, None] < M) | (idx[:, None] >= idx[None, :])
        mem = mem0
        outs = []
        for s in range(n_seg):
            xs = x[:, s * seg_len:(s + 1) * seg_len, :].clone()
            h = torch.concatenate([mem, xs], axis=1)
            q = torch.einsum('btd,dhe->bhte', h, Wq)
            k = torch.einsum('btd,dhe->bhte', h, Wk)
            v = torch.einsum('btd,dhe->bhte', h, Wv)
            scores = torch.einsum('bhqe,bhke->bhqk', q, k) * scale
            scores = torch.where(mask[None, None], scores, -1000000000.0)
            p = _softmax(scores, axis=-1)
            o = torch.einsum('bhqk,bhke->bhqe', p, v)
            o = torch.einsum('bhte,hed->btd', o, Wo)
            mem = o[:, :M, :].clone()
            outs.append(o[:, M:, :])
        return torch.concatenate(outs, axis=1)

    def _B_ref(x, W_gate, W_up, W_down):
        gate = torch.einsum('bsd,df->bsf', x, W_gate)
        up = torch.einsum('bsd,df->bsf', x, W_up)
        gate = gate * (lambda x, dt: x.to(dt))(gate > 0, gate.dtype)
        h = gate * up
        return torch.einsum('bsf,fd->bsd', h, W_down)
    x = A_x
    h = x + _A_ref(_layernorm(x, axis=-1), A_mem0, A_Wq, A_Wk, A_Wv, A_Wo, A_seg_len)
    h2 = h + _B_ref(_layernorm(h, axis=-1), B_W_gate, B_W_up, B_W_down)
    return h2
```

```cpp
#include <hip/hip_runtime.h>
#include <hip/hip_bf16.h>
namespace refimpl {
typedef __hip_bfloat16 bf16;
__device__ __forceinline__ float b2f(bf16 x) { return __bfloat162float(x); }
__device__ __forceinline__ bf16 f2b(float x) { return __float2bfloat16(x); }
__device__ __forceinline__ float rb(float x) { return b2f(f2b(x)); }
__global__ void r_ln(const bf16* x, bf16* y) {
  int row = blockIdx.x, t = threadIdx.x;
  float v[10]; float s = 0;
  for (int i = 0; i < 10; ++i) { v[i] = b2f(x[row * 640 + t + 64 * i]); s += v[i]; }
  for (int o = 32; o; o >>= 1) s += __shfl_xor(s, o);
  float mf = s / 640.f; float q = 0;
  for (int i = 0; i < 10; ++i) { float d = v[i] - mf; q += d * d; }
  for (int o = 32; o; o >>= 1) q += __shfl_xor(q, o);
  float m = rb(mf), var = rb(q / 640.f);
  float den = rb(sqrtf(rb(var + 1e-5f)));
  for (int i = 0; i < 10; ++i) y[row * 640 + t + 64 * i] = f2b(rb(v[i] - m) / den);
}
__global__ void r_gemm(const bf16* A, int lda, const bf16* B, int ldb, bf16* C, int ldc, int M, int N, int K) {
  __shared__ float As[16][65], Bs[16][65];
  int tx = threadIdx.x & 15, ty = threadIdx.x >> 4;
  int m0 = blockIdx.y * 64, n0 = blockIdx.x * 64;
  float acc[4][4] = {};
  for (int k0 = 0; k0 < K; k0 += 16) {
    for (int i = threadIdx.x; i < 1024; i += 256) {
      int r = i >> 4, c = i & 15;
      As[c][r] = (m0 + r < M) ? b2f(A[(size_t)(m0 + r) * lda + k0 + c]) : 0.f;
      int kr = i >> 6, nc = i & 63;
      Bs[kr][nc] = (n0 + nc < N) ? b2f(B[(size_t)(k0 + kr) * ldb + n0 + nc]) : 0.f;
    }
    __syncthreads();
    for (int k = 0; k < 16; ++k) {
      float a[4], b[4];
      for (int i = 0; i < 4; ++i) { a[i] = As[k][ty * 4 + i]; b[i] = Bs[k][tx * 4 + i]; }
      for (int i = 0; i < 4; ++i) for (int j = 0; j < 4; ++j) acc[i][j] += a[i] * b[j];
    }
    __syncthreads();
  }
  for (int i = 0; i < 4; ++i) for (int j = 0; j < 4; ++j) {
    int r = m0 + ty * 4 + i, c = n0 + tx * 4 + j;
    if (r < M && c < N) C[(size_t)r * ldc + c] = f2b(acc[i][j]);
  }
}
__global__ void r_attn(const bf16* qm, const bf16* km, const bf16* vm, const bf16* qx, const bf16* kx, const bf16* vx, bf16* o, bf16* pout) {
  __shared__ float q[40][128], k[40][128], v[40][128], p[40][40];
  int h = blockIdx.x, t = threadIdx.x;
  for (int i = t; i < 40 * 128; i += blockDim.x) {
    int r = i >> 7, c = i & 127;
    if (r < 8) { q[r][c] = b2f(qm[r * 4096 + h * 128 + c]); k[r][c] = b2f(km[r * 4096 + h * 128 + c]); v[r][c] = b2f(vm[r * 4096 + h * 128 + c]); }
    else { q[r][c] = b2f(qx[(r - 8) * 4096 + h * 128 + c]); k[r][c] = b2f(kx[(r - 8) * 4096 + h * 128 + c]); v[r][c] = b2f(vx[(r - 8) * 4096 + h * 128 + c]); }
  }
  __syncthreads();
  const float scale = 0.08838834764831845f;
  for (int i = t; i < 1600; i += blockDim.x) {
    int r = i / 40, c = i % 40; float s = 0;
    for (int e = 0; e < 128; ++e) s += q[r][e] * k[c][e];
    s = rb(rb(s) * scale);
    if (!(r < 8 || r >= c)) s = rb(-1e9f);
    p[r][c] = s;
  }
  __syncthreads();
  if (t < 40) {
    float mx = -3e38f; for (int c = 0; c < 40; ++c) mx = fmaxf(mx, p[t][c]);
    float sum = 0; for (int c = 0; c < 40; ++c) { float e = expf(p[t][c] - mx); p[t][c] = e; sum += e; }
    for (int c = 0; c < 40; ++c) { p[t][c] = rb(p[t][c] / sum); if (pout) pout[(h * 40 + t) * 40 + c] = f2b(p[t][c]); }
  }
  __syncthreads();
  for (int i = t; i < 40 * 128; i += blockDim.x) {
    int r = i >> 7, c = i & 127; float s = 0;
    for (int j = 0; j < 40; ++j) s += p[r][j] * v[j][c];
    o[r * 4096 + h * 128 + c] = f2b(s);
  }
}
__global__ void r_add(const bf16* a, const bf16* b, bf16* c, int n) { int i = blockIdx.x * 256 + threadIdx.x; if (i < n) c[i] = f2b(b2f(a[i]) + b2f(b[i])); }
__global__ void r_reglu(const bf16* g, const bf16* u, bf16* c, int n) { int i = blockIdx.x * 256 + threadIdx.x; if (i < n) { float gg = b2f(g[i]); gg = gg > 0 ? gg : 0.f * gg; c[i] = f2b(gg * b2f(u[i])); } }
__global__ void r_copy(const bf16* a, bf16* c, int n) { int i = blockIdx.x * 256 + threadIdx.x; if (i < n) c[i] = a[i]; }
struct RefBufs { bf16 *xn, *Q, *K, *V, *mems  , *qm, *km, *vm  , *o40, *oo  , *Aout, *h, *hn, *g, *u, *H, *f, *P  ; };
inline size_t ref_ws_bytes() { return (size_t)2 * (2048 * 640 * 6 + 2048 * 4096 * 3 + 65 * 8 * 640 + 3 * 64 * 8 * 4096 + 40 * 4096 + 40 * 640 + 3 * 2048 * 1728 + 64 * 32 * 1600) + 65536; }
inline RefBufs ref_carve(void* ws) {
  bf16* p = (bf16*)ws; RefBufs b;
  auto take = [&](size_t n) { bf16* r = p; p += (n + 127) / 128 * 128; return r; };
  b.xn = take(2048 * 640); b.Q = take(2048 * 4096); b.K = take(2048 * 4096); b.V = take(2048 * 4096);
  b.mems = take(65 * 8 * 640); b.qm = take(64 * 8 * 4096); b.km = take(64 * 8 * 4096); b.vm = take(64 * 8 * 4096);
  b.o40 = take(40 * 4096); b.oo = take(40 * 640); b.Aout = take(2048 * 640); b.h = take(2048 * 640); b.hn = take(2048 * 640);
  b.g = take(2048 * 1728); b.u = take(2048 * 1728); b.H = take(2048 * 1728); b.f = take(2048 * 640); b.P = take(64 * 32 * 1600);
  return b;
}
inline void ref_launch(void* const* d_in, void* d_out, void* ws, hipStream_t st) {
  const bf16 *x = (const bf16*)d_in[0], *mem0 = (const bf16*)d_in[1], *Wq = (const bf16*)d_in[2], *Wk = (const bf16*)d_in[3], *Wv = (const bf16*)d_in[4], *Wo = (const bf16*)d_in[5];
  const bf16 *Wg = (const bf16*)d_in[8], *Wu = (const bf16*)d_in[9], *Wd = (const bf16*)d_in[10];
  RefBufs b = ref_carve(ws);
  r_ln<<<2048, 64, 0, st>>>(x, b.xn);
  r_gemm<<<dim3(64, 32), 256, 0, st>>>(b.xn, 640, Wq, 4096, b.Q, 4096, 2048, 4096, 640);
  r_gemm<<<dim3(64, 32), 256, 0, st>>>(b.xn, 640, Wk, 4096, b.K, 4096, 2048, 4096, 640);
  r_gemm<<<dim3(64, 32), 256, 0, st>>>(b.xn, 640, Wv, 4096, b.V, 4096, 2048, 4096, 640);
  r_copy<<<20, 256, 0, st>>>(mem0, b.mems, 5120);
  for (int s = 0; s < 64; ++s) {
    bf16* mem = b.mems + s * 5120;
    bf16 *qm = b.qm + s * 32768, *km = b.km + s * 32768, *vm = b.vm + s * 32768;
    r_gemm<<<dim3(64, 1), 256, 0, st>>>(mem, 640, Wq, 4096, qm, 4096, 8, 4096, 640);
    r_gemm<<<dim3(64, 1), 256, 0, st>>>(mem, 640, Wk, 4096, km, 4096, 8, 4096, 640);
    r_gemm<<<dim3(64, 1), 256, 0, st>>>(mem, 640, Wv, 4096, vm, 4096, 8, 4096, 640);
    r_attn<<<32, 256, 0, st>>>(qm, km, vm, b.Q + (size_t)s * 32 * 4096, b.K + (size_t)s * 32 * 4096, b.V + (size_t)s * 32 * 4096, b.o40, b.P + (size_t)s * 32 * 1600);
    r_gemm<<<dim3(10, 1), 256, 0, st>>>(b.o40, 4096, Wo, 640, b.oo, 640, 40, 640, 4096);
    r_copy<<<20, 256, 0, st>>>(b.oo, b.mems + (s + 1) * 5120, 5120);
    r_copy<<<80, 256, 0, st>>>(b.oo + 5120, b.Aout + s * 32 * 640, 32 * 640);
  }
  r_add<<<5120, 256, 0, st>>>(x, b.Aout, b.h, 2048 * 640);
  r_ln<<<2048, 64, 0, st>>>(b.h, b.hn);
  r_gemm<<<dim3(27, 32), 256, 0, st>>>(b.hn, 640, Wg, 1728, b.g, 1728, 2048, 1728, 640);
  r_gemm<<<dim3(27, 32), 256, 0, st>>>(b.hn, 640, Wu, 1728, b.u, 1728, 2048, 1728, 640);
  r_reglu<<<2048 * 1728 / 256, 256, 0, st>>>(b.g, b.u, b.H, 2048 * 1728);
  r_gemm<<<dim3(10, 32), 256, 0, st>>>(b.H, 1728, Wd, 640, b.f, 640, 2048, 640, 1728);
  r_add<<<5120, 256, 0, st>>>(b.h, b.f, (bf16*)d_out, 2048 * 640);
}
}
extern "C" void kernel_launch(void* const* d_in, const int* in_sizes, int n_in, void* d_out, int out_size, void* d_ws, size_t ws_size, hipStream_t stream) {
  refimpl::ref_launch(d_in, d_out, d_ws, stream);
}
```

```cpp
#include <hip/hip_runtime.h>
#include <hip/hip_bf16.h>

typedef __hip_bfloat16 bf16;
typedef __bf16 bf16x8 __attribute__((ext_vector_type(8)));
typedef __bf16 bf16x16 __attribute__((ext_vector_type(16)));
typedef float f32x4 __attribute__((ext_vector_type(4)));
typedef unsigned u32x4 __attribute__((ext_vector_type(4)));
typedef unsigned u32x2 __attribute__((ext_vector_type(2)));
typedef unsigned u32x8 __attribute__((ext_vector_type(8)));
typedef short s16x4 __attribute__((ext_vector_type(4)));
typedef __attribute__((address_space(3))) s16x4* lds_s16x4_p;

constexpr int S_ = 2048, D_ = 640, NH = 32, DH = 128, HD = 4096, FF = 1728, NSEG = 64;
constexpr size_t MB = 1u << 20;
constexpr size_t WS_XCH = 0;
constexpr size_t WS_CTL = 4 * MB;
constexpr size_t WS_Q = 5 * MB;
constexpr size_t WS_K = WS_Q + 16 * MB;
constexpr size_t WS_U = WS_K + 16 * MB;
constexpr size_t WS_VO = WS_U + 25 * MB;
constexpr size_t WS_KM = WS_VO + 100 * MB;
constexpr size_t WS_P = WS_KM + 4 * MB;
constexpr size_t WS_H = WS_P + 5 * MB;
constexpr size_t WS_G = WS_H + 3 * MB;
constexpr size_t WS_END = WS_G + 7 * MB;

constexpr unsigned SENT = 0xFFFFFFFFu;
constexpr unsigned SPIN_CAP = 1u << 22;

__device__ __forceinline__ unsigned pack2(float lo, float hi) {
  typedef __bf16 b2 __attribute__((ext_vector_type(2)));
  typedef float f2 __attribute__((ext_vector_type(2)));
  return __builtin_bit_cast(unsigned, __builtin_convertvector(f2{lo, hi}, b2));
}
__device__ __forceinline__ float bflo(unsigned w) { return __uint_as_float(w << 16); }
__device__ __forceinline__ float bfhi(unsigned w) { return __uint_as_float(w & 0xffff0000u); }
__device__ __forceinline__ unsigned canon(unsigned w) { return w == SENT ? 0xFFC0FFC0u : w; }
__device__ __forceinline__ void glds16(const void* base, unsigned voff, unsigned lds) {
  asm volatile("s_mov_b32 m0, %2\n\ts_nop 0\n\tglobal_load_lds_dwordx4 %0, %1" ::"v"(voff), "s"(base), "s"(lds) : "m0");
}
__device__ __forceinline__ unsigned lds_addr(const void* p) { return (unsigned)(size_t)(const __attribute__((address_space(3))) char*)p; }
__device__ __forceinline__ u32x2 tr_read(const char* p) {
  s16x4 r = __builtin_amdgcn_ds_read_tr16_b64_v4i16((lds_s16x4_p)(p));
  return __builtin_bit_cast(u32x2, r);
}
#define WAIT_V(n) asm volatile("s_waitcnt vmcnt(" #n ")" ::: "memory")
#define WAIT_L0() asm volatile("s_waitcnt lgkmcnt(0)" ::: "memory")
#define BAR() asm volatile("s_barrier" ::: "memory")
__device__ __forceinline__ f32x4 mfma16(u32x4 a, u32x4 b, f32x4 c) {
  return __builtin_amdgcn_mfma_f32_16x16x32_bf16(__builtin_bit_cast(bf16x8, a), __builtin_bit_cast(bf16x8, b), c, 0, 0, 0);
}
__device__ __forceinline__ f32x4 smf(u32x4 a, u32x8 b, f32x4 c, int idx) {
  return __builtin_amdgcn_smfmac_f32_16x16x64_bf16(__builtin_bit_cast(bf16x8, a), __builtin_bit_cast(bf16x16, b), c, idx, 0, 0);
}
__device__ __forceinline__ unsigned xcc_id() { return (unsigned)__builtin_amdgcn_s_getreg(20 | (3 << 11)) & 0xfu; }

__device__ __forceinline__ int lds_byte(int r, int c) {
  int st = (r >> 4) * 2 + (c >> 5), ob = (r & 15) * 64 + (c & 31) * 2;
  return st * 1024 + (ob ^ (((ob >> 9) & 1) << 5));
}
__device__ __forceinline__ void stage_rc(int b, int& R, int& C) {
  int st = b >> 10, sb = b & 1023, swz = sb ^ (((sb >> 9) & 1) << 5);
  R = (st >> 1) * 16 + (swz >> 6); C = (st & 1) * 32 + ((swz & 63) >> 1);
}
__device__ __forceinline__ void stageB_rc(int p, int& row, int& ch) {
  row = ((p >> 7) << 3) | ((p >> 2) & 7);
  ch = (((p >> 5) & 3) << 2) | ((p & 3) ^ ((row >> 2) & 3));
}
__device__ __forceinline__ int trB_off(int lane, int c, int ks, int t) {
  int g = lane >> 4, q = (lane & 15) >> 2, p = lane & 3;
  return 2048 * (4 * ks + g) + 512 * (c >> 1) + 64 * (4 * t + q) + 16 * ((2 * (c & 1) + (p >> 1)) ^ ((2 * g + t) & 3)) + 8 * (p & 1);
}

template <bool SW>
__device__ __forceinline__ void mma_tile(const char* sa, const char* sb, int lane, int wm, int c0, int c1, f32x4 (&acc)[2][2]) {
  const int fr = lane & 15, fq = lane >> 4;
#pragma unroll
  for (int ks = 0; ks < 2; ++ks) {
    u32x4 a[2], b[2];
#pragma unroll
    for (int m = 0; m < 2; ++m) a[m] = *(const u32x4*)(sa + lds_byte(wm * 32 + m * 16 + fr, ks * 32 + fq * 8));
#pragma unroll
    for (int n = 0; n < 2; ++n) {
      int c = n ? c1 : c0;
      u32x2 lo = tr_read(sb + trB_off(lane, c, ks, 0)), hi = tr_read(sb + trB_off(lane, c, ks, 1));
      b[n] = u32x4{lo.x, lo.y, hi.x, hi.y};
    }
#pragma unroll
    for (int m = 0; m < 2; ++m)
#pragma unroll
      for (int n = 0; n < 2; ++n) acc[m][n] = SW ? mfma16(b[n], a[m], acc[m][n]) : mfma16(a[m], b[n], acc[m][n]);
  }
}

__device__ __forceinline__ void ln_rows_to_lds(const bf16* src, char* sa, int tid) {
  const int row = tid >> 3, j = tid & 7;
  u32x4 v[10];
#pragma unroll
  for (int i = 0; i < 10; ++i) v[i] = *(const u32x4*)(src + (size_t)row * D_ + (j + 8 * i) * 8);
  float s = 0.f;
#pragma unroll
  for (int i = 0; i < 10; ++i)
#pragma unroll
    for (int e = 0; e < 4; ++e) s += bflo(v[i][e]) + bfhi(v[i][e]);
  s += __shfl_xor(s, 1); s += __shfl_xor(s, 2); s += __shfl_xor(s, 4);
  const float mean = s * (1.f / 640.f);
  float q = 0.f;
#pragma unroll
  for (int i = 0; i < 10; ++i)
#pragma unroll
    for (int e = 0; e < 4; ++e) { float a = bflo(v[i][e]) - mean, b = bfhi(v[i][e]) - mean; q += a * a + b * b; }
  q += __shfl_xor(q, 1); q += __shfl_xor(q, 2); q += __shfl_xor(q, 4);
  const float rstd = 1.f / sqrtf(q * (1.f / 640.f) + 1e-5f);
#pragma unroll
  for (int i = 0; i < 10; ++i) {
    u32x4 o;
#pragma unroll
    for (int e = 0; e < 4; ++e) o[e] = pack2((bflo(v[i][e]) - mean) * rstd, (bfhi(v[i][e]) - mean) * rstd);
    *(u32x4*)(sa + i * 8192 + lds_byte(row, j * 8)) = o;
  }
}

constexpr int RING = 3;

__device__ __forceinline__ void task_kq(const bf16* x, const bf16* W, bf16* out, int rb, int h0, int nh, char* sm, int tid) {
  const int lane = tid & 63, wid = __builtin_amdgcn_readfirstlane(tid >> 6), wm = wid >> 2, wn = wid & 3;
  const unsigned lw = lds_addr(sm) + wid * 1024;
  char* sa = sm; char* ring = sm + 81920;
  int r0, c0, r1, c1; stageB_rc(tid, r0, c0); stageB_rc(tid + 512, r1, c1);
  const unsigned o0 = (r0 * HD + c0 * 8) * 2, o1 = (r1 * HD + c1 * 8) * 2;
  const int T = nh * 10;
  auto issue = [&](int t) {
    const bf16* b = W + (size_t)((t % 10) * 64) * HD + (h0 + t / 10) * DH;
    const unsigned d = lw + 81920 + (t % RING) * 16384;
    glds16(b, o0, d); glds16(b, o1, d + 8192);
  };
  issue(0); issue(1);
  ln_rows_to_lds(x + (size_t)rb * 64 * D_, sa, tid);
  f32x4 acc[2][2] = {};
  for (int t = 0; t < T; ++t) {
    if (t + 1 < T) { WAIT_V(2); } else { WAIT_V(0); }
    WAIT_L0(); BAR();
    if (t + 2 < T) issue(t + 2);
    mma_tile<true>(sa + (t % 10) * 8192, ring + (t % RING) * 16384, lane, wm, 2 * wn, 2 * wn + 1, acc);
    if (t % 10 == 9) {
      const int h = h0 + t / 10;
#pragma unroll
      for (int m = 0; m < 2; ++m)
#pragma unroll
        for (int n = 0; n < 2; ++n) {
          u32x2 o = {pack2(acc[m][n][0], acc[m][n][1]), pack2(acc[m][n][2], acc[m][n][3])};
          *(u32x2*)(out + (size_t)(rb * 64 + wm * 32 + m * 16 + (lane & 15)) * HD + h * DH + (2 * wn + n) * 16 + (lane >> 4) * 4) = o;
          acc[m][n] = f32x4{0, 0, 0, 0};
        }
    }
  }
  WAIT_L0(); BAR();
}

__device__ __forceinline__ void task_vvo(const bf16* x, const bf16* Wv, const bf16* Wo, bf16* VO, int rb, int h0, int nh, char* sm, int tid) {
  const int lane = tid & 63, wid = __builtin_amdgcn_readfirstlane(tid >> 6), wm = wid >> 2, wn = wid & 3;
  const unsigned lw = lds_addr(sm) + wid * 1024;
  char* sa = sm; char* sv = sm + 81920; char* ring = sm + 98304;
  int r0, c0, r1, c1; stageB_rc(tid, r0, c0); stageB_rc(tid + 512, r1, c1);
  const int T = nh * 20;
  auto issue = [&](int t) {
    const int h = h0 + t / 20, u = t % 20;
    const unsigned d = lw + 98304 + (t % RING) * 16384;
    if (u < 10) {
      const bf16* b = Wv + (size_t)(u * 64) * HD + h * DH;
      glds16(b, (r0 * HD + c0 * 8) * 2, d); glds16(b, (r1 * HD + c1 * 8) * 2, d + 8192);
    } else {
      const int v = u - 10;
      const bf16* b = Wo + (size_t)(h * DH + (v & 1) * 64) * D_ + (v >> 1) * 128;
      glds16(b, (r0 * D_ + c0 * 8) * 2, d); glds16(b, (r1 * D_ + c1 * 8) * 2, d + 8192);
    }
  };
  issue(0); issue(1);
  ln_rows_to_lds(x + (size_t)rb * 64 * D_, sa, tid);
  f32x4 acc[2][2] = {};
  for (int t = 0; t < T; ++t) {
    if (t + 1 < T) { WAIT_V(2); } else { WAIT_V(0); }
    WAIT_L0(); BAR();
    if (t + 2 < T) issue(t + 2);
    const int h = h0 + t / 20, u = t % 20;
    const char* sb = ring + (t % RING) * 16384;
    if (u < 10) {
      mma_tile<true>(sa + u * 8192, sb, lane, wm, 2 * wn, 2 * wn + 1, acc);
      if (u == 9) {
#pragma unroll
        for (int m = 0; m < 2; ++m)
#pragma unroll
          for (int n = 0; n < 2; ++n) {
            u32x2 o = {pack2(acc[m][n][0], acc[m][n][1]), pack2(acc[m][n][2], acc[m][n][3])};
            const int e = (2 * wn + n) * 16 + (lane >> 4) * 4;
            *(u32x2*)(sv + (e >> 6) * 8192 + lds_byte(wm * 32 + m * 16 + (lane & 15), e & 63)) = o;
            acc[m][n] = f32x4{0, 0, 0, 0};
          }
      }
    } else {
      const int v = u - 10;
      mma_tile<false>(sv + (v & 1) * 8192, sb, lane, wm, 2 * wn, 2 * wn + 1, acc);
      if (v & 1) {
        const int seg = rb * 2 + wm, O = 5 * h + 1 + (lane >> 4);
#pragma unroll
        for (int n = 0; n < 2; ++n) {
          const int col = (v >> 1) * 128 + (2 * wn + n) * 16 + (lane & 15);
          u32x4 o = {pack2(acc[0][n][0], acc[0][n][1]), pack2(acc[0][n][2], acc[0][n][3]), pack2(acc[1][n][0], acc[1][n][1]), pack2(acc[1][n][2], acc[1][n][3])};
          size_t off = ((((size_t)seg * 80 + (col >> 3)) * 20 + (O >> 3)) * 64 + (O & 3) * 16 + ((O >> 2) & 1) * 8 + (col & 7)) * 8;
          *(u32x4*)(VO + off) = o;
          acc[0][n] = f32x4{0, 0, 0, 0}; acc[1][n] = f32x4{0, 0, 0, 0};
        }
      }
    }
  }
  WAIT_L0(); BAR();
}

__device__ __forceinline__ void task_u(const bf16* Wv, const bf16* Wo, bf16* U, int h, int db, char* sm, int tid) {
  const int lane = tid & 63, wid = __builtin_amdgcn_readfirstlane(tid >> 6), wm = wid >> 2, wn = wid & 3;
  const unsigned lw = lds_addr(sm) + wid * 1024;
  char* sv = sm + 81920; char* ring = sm + 98304;
  int r0, c0, r1, c1; stageB_rc(tid, r0, c0); stageB_rc(tid + 512, r1, c1);
  { int R, C; stage_rc(tid * 16, R, C);
    const bf16* a = Wv + (size_t)(db * 64) * HD + h * DH;
    glds16(a, (R * HD + C) * 2, lw + 81920); glds16(a + 64, (R * HD + C) * 2, lw + 81920 + 8192); }
  auto issue = [&](int v) {
    const unsigned d = lw + 98304 + (v % RING) * 16384;
    const bf16* b = Wo + (size_t)(h * DH + (v & 1) * 64) * D_ + (v >> 1) * 128;
    glds16(b, (r0 * D_ + c0 * 8) * 2, d); glds16(b, (r1 * D_ + c1 * 8) * 2, d + 8192);
  };
  issue(0); issue(1);
  f32x4 acc[2][2] = {};
  for (int v = 0; v < 10; ++v) {
    if (v + 1 < 10) { WAIT_V(2); } else { WAIT_V(0); }
    WAIT_L0(); BAR();
    if (v + 2 < 10) issue(v + 2);
    mma_tile<true>(sv + (v & 1) * 8192, ring + (v % RING) * 16384, lane, wm, 2 * wn, 2 * wn + 1, acc);
    if (v & 1) {
#pragma unroll
      for (int m = 0; m < 2; ++m)
#pragma unroll
        for (int n = 0; n < 2; ++n) {
          const int d1 = db * 64 + wm * 32 + m * 16 + (lane & 15);
          const int d2 = (v >> 1) * 128 + (2 * wn + n) * 16 + (lane >> 4) * 4;
          u32x2 o = {pack2(acc[m][n][0], acc[m][n][1]), pack2(acc[m][n][2], acc[m][n][3])};
          *(u32x2*)(U + (((size_t)(d2 >> 3) * NH + h) * D_ + d1) * 8 + (d2 & 7)) = o;
          acc[m][n] = f32x4{0, 0, 0, 0};
        }
    }
  }
  WAIT_L0(); BAR();
}

__device__ __forceinline__ void wave_t1(const bf16* Q, const bf16* K, const bf16* KM, bf16* P, int seg, int h, int lane) {
  const int fr = lane & 15, fq = lane >> 4;
  u32x4 qf[2][4], kf[3][4];
#pragma unroll
  for (int t = 0; t < 4; ++t) {
    kf[0][t] = *(const u32x4*)(KM + ((size_t)(seg * NH + h) * 8 + (fr & 7)) * DH + t * 32 + fq * 8);
#pragma unroll
    for (int i = 0; i < 2; ++i) {
      qf[i][t] = *(const u32x4*)(Q + (size_t)(seg * 32 + i * 16 + fr) * HD + h * DH + t * 32 + fq * 8);
      kf[i + 1][t] = *(const u32x4*)(K + (size_t)(seg * 32 + i * 16 + fr) * HD + h * DH + t * 32 + fq * 8);
    }
  }
  const float cs = 0.08838834764831845f * 1.4426950408889634f;
#pragma unroll
  for (int i = 0; i < 2; ++i) {
    f32x4 sc[3] = {};
#pragma unroll
    for (int t = 0; t < 4; ++t)
#pragma unroll
      for (int k = 0; k < 3; ++k) sc[k] = mfma16(kf[k][t], qf[i][t], sc[k]);
    const int qi = i * 16 + fr;
    float mx = -INFINITY;
#pragma unroll
    for (int r = 0; r < 4; ++r) {
      if (fq >= 2) sc[0][r] = -INFINITY;
      if (fq * 4 + r > qi) sc[1][r] = -INFINITY;
      if (16 + fq * 4 + r > qi) sc[2][r] = -INFINITY;
      mx = fmaxf(mx, fmaxf(sc[0][r], fmaxf(sc[1][r], sc[2][r])));
    }
    mx = fmaxf(mx, __shfl_xor(mx, 16)); mx = fmaxf(mx, __shfl_xor(mx, 32));
    float sum = 0.f;
#pragma unroll
    for (int k = 0; k < 3; ++k)
#pragma unroll
      for (int r = 0; r < 4; ++r) { sc[k][r] = __builtin_amdgcn_exp2f((sc[k][r] - mx) * cs); sum += sc[k][r]; }
    sum += __shfl_xor(sum, 16); sum += __shfl_xor(sum, 32);
    const float inv = 1.f / sum;
    bf16* prow = P + ((size_t)(seg * 32 + qi) * 160 + 5 * h) * 8;
    u32x4 o = {pack2(sc[1][0] * inv, sc[1][1] * inv), pack2(sc[1][2] * inv, sc[1][3] * inv), pack2(sc[2][0] * inv, sc[2][1] * inv), pack2(sc[2][2] * inv, sc[2][3] * inv)};
    *(u32x4*)(prow + (1 + fq) * 8) = o;
    if (fq < 2) { u32x2 o2 = {pack2(sc[0][0] * inv, sc[0][1] * inv), pack2(sc[0][2] * inv, sc[0][3] * inv)}; *(u32x2*)(prow + fq * 4) = o2; }
  }
}

__device__ __forceinline__ void task_t2(const bf16* x, const bf16* P, const bf16* VO, bf16* Hb, int seg, int cs, char* sm, int tid) {
  const int lane = tid & 63, wid = tid >> 6, fr = lane & 15, fq = lane >> 4;
  f32x4 acc[2][4] = {};
  u32x4 a[5][2], b[5][4];
#pragma unroll
  for (int k = 0; k < 5; ++k) {
    const int O = (wid * 5 + k) * 4 + fq;
#pragma unroll
    for (int m = 0; m < 2; ++m) a[k][m] = *(const u32x4*)(P + ((size_t)(seg * 32 + m * 16 + fr) * 160 + O) * 8);
#pragma unroll
    for (int n = 0; n < 4; ++n) {
      const int col = cs * 64 + n * 16 + fr;
      b[k][n] = *(const u32x4*)(VO + ((((size_t)seg * 80 + (col >> 3)) * 20 + (O >> 3)) * 64 + (O & 3) * 16 + ((O >> 2) & 1) * 8 + (col & 7)) * 8);
    }
  }
#pragma unroll
  for (int k = 0; k < 5; ++k)
#pragma unroll
    for (int m = 0; m < 2; ++m)
#pragma unroll
      for (int n = 0; n < 4; ++n) acc[m][n] = mfma16(b[k][n], a[k][m], acc[m][n]);
  float* red = (float*)sm;
#pragma unroll
  for (int m = 0; m < 2; ++m)
#pragma unroll
    for (int n = 0; n < 4; ++n) *(f32x4*)(red + (wid * 32 + m * 16 + fr) * 68 + n * 16 + fq * 4) = acc[m][n];
  __syncthreads();
  { const int row = tid >> 4, c4 = (tid & 15) * 4;
    f32x4 s = *(const f32x4*)(red + row * 68 + c4);
#pragma unroll
    for (int w = 1; w < 8; ++w) s += *(const f32x4*)(red + (w * 32 + row) * 68 + c4);
    const size_t go = (size_t)(seg * 32 + row) * D_ + cs * 64 + c4;
    u32x2 xv = *(const u32x2*)(x + go);
    unsigned a0 = pack2(s[0], s[1]), a1 = pack2(s[2], s[3]);
    u32x2 o = {pack2(bflo(xv.x) + bflo(a0), bfhi(xv.x) + bfhi(a0)), pack2(bflo(xv.y) + bflo(a1), bfhi(xv.y) + bfhi(a1))};
    *(u32x2*)(Hb + go) = o;
  }
  __syncthreads();
}

__device__ __forceinline__ void task_f1(const bf16* Hb, const bf16* Wg, const bf16* Wu, bf16* G, int rb, int f0, int nt, char* sm, int tid) {
  const int lane = tid & 63, wid = __builtin_amdgcn_readfirstlane(tid >> 6), wm = wid >> 2, wn = wid & 3;
  const unsigned lw = lds_addr(sm) + wid * 1024;
  char* sa = sm; char* ring = sm + 81920;
  int r0, c0, r1, c1; stageB_rc(tid, r0, c0); stageB_rc(tid + 512, r1, c1);
  const bf16* pw = (wid & 1) ? Wu : Wg;
  const unsigned o0 = (r0 * FF + (c0 & 7) * 8) * 2, o1 = (r1 * FF + (c1 & 7) * 8) * 2;
  const int T = nt * 10;
  auto issue = [&](int t) {
    const size_t o = (size_t)((t % 10) * 64) * FF + (f0 + t / 10) * 64;
    const unsigned d = lw + 81920 + (t % RING) * 16384;
    glds16(pw + o, o0, d); glds16(pw + o, o1, d + 8192);
  };
  issue(0); issue(1);
  ln_rows_to_lds(Hb + (size_t)rb * 64 * D_, sa, tid);
  f32x4 acc[2][2] = {};
  for (int t = 0; t < T; ++t) {
    if (t + 1 < T) { WAIT_V(2); } else { WAIT_V(0); }
    WAIT_L0(); BAR();
    if (t + 2 < T) issue(t + 2);
    mma_tile<true>(sa + (t % 10) * 8192, ring + (t % RING) * 16384, lane, wm, wn, wn + 4, acc);
    if (t % 10 == 9) {
      const int f = (f0 + t / 10) * 64 + wn * 16 + (lane >> 4) * 4;
#pragma unroll
      for (int m = 0; m < 2; ++m) {
        unsigned g0 = pack2(acc[m][0][0], acc[m][0][1]), g1 = pack2(acc[m][0][2], acc[m][0][3]);
        unsigned u0 = pack2(acc[m][1][0], acc[m][1][1]), u1 = pack2(acc[m][1][2], acc[m][1][3]);
        auto rl = [](float g) { return g > 0.f ? g : 0.f * g; };
        u32x2 o = {pack2(rl(bflo(g0)) * bflo(u0), rl(bfhi(g0)) * bfhi(u0)), pack2(rl(bflo(g1)) * bflo(u1), rl(bfhi(g1)) * bfhi(u1))};
        *(u32x2*)(G + (size_t)(rb * 64 + wm * 32 + m * 16 + (lane & 15)) * FF + f) = o;
        acc[m][0] = f32x4{0, 0, 0, 0}; acc[m][1] = f32x4{0, 0, 0, 0};
      }
    }
  }
  WAIT_L0(); BAR();
}

__device__ __forceinline__ void task_f2(const bf16* G, const bf16* Wd, const bf16* Hb, bf16* out, int rb, int cs, char* sm, int tid) {
  const int lane = tid & 63, wid = __builtin_amdgcn_readfirstlane(tid >> 6), wm = wid >> 2, wn = wid & 3;
  const unsigned lw = lds_addr(sm) + wid * 1024;
  int r0, c0, r1, c1; stageB_rc(tid, r0, c0); stageB_rc(tid + 512, r1, c1);
  int R, C; stage_rc(tid * 16, R, C);
  const bf16* pa = G + (size_t)(rb * 64) * FF;
  const bf16* pb = Wd + cs * 128;
  const unsigned oa = (R * FF + C) * 2, o0 = (r0 * D_ + c0 * 8) * 2, o1 = (r1 * D_ + c1 * 8) * 2;
  constexpr int R4 = 4;
  auto issue = [&](int t) {
    const unsigned d = lw + (t % R4) * 24576;
    glds16(pa + t * 64, oa, d);
    glds16(pb + (size_t)t * 64 * D_, o0, d + 8192); glds16(pb + (size_t)t * 64 * D_, o1, d + 16384);
  };
  issue(0); issue(1); issue(2);
  f32x4 acc[2][2] = {};
  for (int t = 0; t < 27; ++t) {
    if (t + 2 < 27) { WAIT_V(6); } else if (t + 1 < 27) { WAIT_V(3); } else { WAIT_V(0); }
    WAIT_L0(); BAR();
    if (t + 3 < 27) issue(t + 3);
    const char* d = sm + (t % R4) * 24576;
    mma_tile<true>(d, d + 8192, lane, wm, 2 * wn, 2 * wn + 1, acc);
  }
#pragma unroll
  for (int m = 0; m < 2; ++m)
#pragma unroll
    for (int n = 0; n < 2; ++n) {
      const size_t go = (size_t)(rb * 64 + wm * 32 + m * 16 + (lane & 15)) * D_ + cs * 128 + (2 * wn + n) * 16 + (lane >> 4) * 4;
      u32x2 hv = *(const u32x2*)(Hb + go);
      unsigned a0 = pack2(acc[m][n][0], acc[m][n][1]), a1 = pack2(acc[m][n][2], acc[m][n][3]);
      u32x2 o = {pack2(bflo(hv.x) + bflo(a0), bfhi(hv.x) + bfhi(a0)), pack2(bflo(hv.y) + bflo(a1), bfhi(hv.y) + bfhi(a1))};
      *(u32x2*)(out + go) = o;
    }
  WAIT_L0(); BAR();
}

__device__ __forceinline__ unsigned xoff(int step, unsigned o, unsigned par) {
  return (unsigned)step * 65536u + ((o >> 13) << 14) + (o & 8191u) + (((par >> (step >> 5)) & 1u) << 13);
}

__global__ void __launch_bounds__(512) k_prologue(const bf16* mem0, const bf16* Wv, const bf16* Wo, char* ws) {
  extern __shared__ __attribute__((aligned(16))) char sm[];
  const int tid = threadIdx.x, bid = blockIdx.x;
  if (bid == 0) {
    if (tid < 64) {
      const __amdgpu_buffer_rsrc_t r = __builtin_amdgcn_make_buffer_rsrc(ws + WS_XCH, 0, 4 << 20, 0x00020000);
      unsigned tab = 0;
      for (int g = 0; g < 2; ++g) {
        unsigned best[2] = {~0u, ~0u};
        for (int k = 0; k < 8; ++k)
          for (int p = 0; p < 2; ++p) {
            unsigned off = (g << 21) + (k * 4 + 1) * 65536 + 3 * 16384 + p * 8192 + 6144 + 512;
            asm volatile("s_waitcnt vmcnt(0)" ::: "memory");
            unsigned long long t0 = __builtin_amdgcn_s_memtime();
            unsigned v = __builtin_amdgcn_raw_buffer_load_b32(r, off, 0, 16);
            asm volatile("s_waitcnt vmcnt(0)" ::"v"(v) : "memory");
            unsigned long long t1 = __builtin_amdgcn_s_memtime();
            unsigned d = (unsigned)(t1 - t0); if (d < best[p]) best[p] = d;
          }
        unsigned nearp = best[1] < best[0] ? 1u : 0u;
        if (xcc_id() >= 4) nearp ^= 1u;
        tab |= nearp << g;
      }
      if (tid < 16) ((unsigned*)(ws + WS_CTL))[tid] = tid == 0 ? tab : 0u;
    }
    return;
  }
  if (bid <= 64) {
    const int step = bid - 1;
    for (int i = tid; i < 4096; i += 512) {
      const unsigned po = i * 16;
      const unsigned lo = ((po >> 14) << 13) + (po & 8191u);
      if (lo >= 30720u) continue;
      u32x4 v = {SENT, SENT, SENT, SENT};
      if (step == 0 && lo < 10240u) {
        const int b = lo >> 7, row = (lo >> 4) & 7;
        v = *(const u32x4*)(mem0 + row * D_ + b * 8);
        v.x = canon(v.x); v.y = canon(v.y); v.z = canon(v.z); v.w = canon(v.w);
      }
      *(u32x4*)(ws + WS_XCH + (size_t)step * 65536 + po) = v;
    }
    return;
  }
  const int t = bid - 65;
  task_u(Wv, Wo, (bf16*)(ws + WS_U), t / 10, t % 10, sm, tid);
}

__global__ void __launch_bounds__(512) k_proj(const bf16* x, const bf16* Wq, const bf16* Wk, const bf16* Wv, const bf16* Wo, char* ws) {
  extern __shared__ __attribute__((aligned(16))) char sm[];
  const int tid = threadIdx.x, bid = blockIdx.x;
  const int rb = bid >> 5, j = bid & 31;
  if (j < 16) task_vvo(x, Wv, Wo, (bf16*)(ws + WS_VO), rb, j * 2, 2, sm, tid);
  else if (j < 24) task_kq(x, Wk, (bf16*)(ws + WS_K), rb, (j - 16) * 4, 4, sm, tid);
  else task_kq(x, Wq, (bf16*)(ws + WS_Q), rb, (j - 24) * 4, 4, sm, tid);
}

template <int NL>
__device__ __forceinline__ bool poll_frags(__amdgpu_buffer_rsrc_t r, const unsigned (&off)[NL], u32x4 (&v)[NL], int n) {
  unsigned spins = 0;
  for (;;) {
    asm volatile("" ::: "memory");
#pragma unroll
    for (int i = 0; i < NL; ++i) if (i < n) v[i] = __builtin_amdgcn_raw_buffer_load_b128(r, off[i], 0, 16);
    bool bad = false;
#pragma unroll
    for (int i = 0; i < NL; ++i) if (i < n) bad |= (v[i].x == SENT) | (v[i].y == SENT) | (v[i].z == SENT) | (v[i].w == SENT);
    if (!__any(bad)) return true;
    if (++spins > SPIN_CAP) return false;
  }
}

template <class FB, class FO>
__device__ __forceinline__ void load_wfrags(FB base, FO voff, u32x8 (&w)[2][10], char* sm, int tid) {
  const int lane = tid & 63, wid = __builtin_amdgcn_readfirstlane(tid >> 6);
  const int bq = lane >> 4, q = (lane & 15) >> 2, p = lane & 3;
  const unsigned lw = lds_addr(sm) + wid * 1024;
#pragma unroll
  for (int rnd = 0; rnd < 3; ++rnd) {
    const int nf = rnd < 2 ? 4 : 2;
    for (int i = 0; i < nf * 4; ++i) {
      const int ci = (i & 3) * 512 + tid, half = (i >> 1) & 1, k = (ci >> 4) & 63;
      const int cc = half * 16 + ((ci & 15) ^ (((k & 3) << 1) | (((k >> 2) & 1) << 3)));
      glds16(base(half), voff((rnd * 4 + (i >> 2)) * 64 + k, cc), lw + (i >> 2) * 32768 + (i & 3) * 8192);
    }
    WAIT_V(0); BAR();
#pragma unroll
    for (int ff = 0; ff < 4; ++ff) {
      if (ff < nf) {
#pragma unroll
        for (int t = 0; t < 2; ++t) {
          u32x2 qd[4];
#pragma unroll
          for (int z = 0; z < 4; ++z) {
            const int k = 16 * (bq >> 1) + 4 * (bq & 1) + (z & 1) * 8 + (z >> 1) * 32 + q;
            const int cc = (2 * wid + t) * 2 + (p >> 1);
            qd[z] = tr_read(sm + ff * 32768 + ((cc >> 4) * 1024 + k * 16 + ((cc & 15) ^ (((k & 3) << 1) | (((k >> 2) & 1) << 3)))) * 16 + 8 * (p & 1));
          }
          w[t][rnd * 4 + ff] = u32x8{qd[0].x, qd[1].x, qd[0].y, qd[1].y, qd[2].x, qd[3].x, qd[2].y, qd[3].y};
        }
      }
    }
    WAIT_L0(); BAR();
  }
}

__global__ void __launch_bounds__(512, 2) k_rec(const bf16* Wq, const bf16* Wk, char* ws) {
  extern __shared__ __attribute__((aligned(16))) char sm[];
  const int tid = threadIdx.x, lane = tid & 63;
  const int wid = __builtin_amdgcn_readfirstlane(tid >> 6);
  unsigned* ctl = (unsigned*)(ws + WS_CTL);
  if (tid == 0) {
    unsigned role = 1000u;
    if (xcc_id() < 4) role = __hip_atomic_fetch_add(ctl + 1, 1u, __ATOMIC_RELAXED, __HIP_MEMORY_SCOPE_AGENT);
    __hip_atomic_fetch_add(ctl + 2, 1u, __ATOMIC_RELAXED, __HIP_MEMORY_SCOPE_AGENT);
    if (role >= 112u) {
      unsigned spins = 0;
      while (__hip_atomic_load(ctl + 2, __ATOMIC_RELAXED, __HIP_MEMORY_SCOPE_AGENT) < gridDim.x && ++spins < SPIN_CAP) __builtin_amdgcn_s_sleep(2);
      if (__hip_atomic_load(ctl + 1, __ATOMIC_RELAXED, __HIP_MEMORY_SCOPE_AGENT) < 112u) role = __hip_atomic_fetch_add(ctl + 1, 1u, __ATOMIC_RELAXED, __HIP_MEMORY_SCOPE_AGENT);
    }
    *(unsigned*)sm = role;
  }
  __syncthreads();
  const int role = __builtin_amdgcn_readfirstlane(*(unsigned*)sm);
  __syncthreads();
  if (role >= 112) return;
  const unsigned par = ctl[0];
  const __amdgpu_buffer_rsrc_t xr = __builtin_amdgcn_make_buffer_rsrc(ws + WS_XCH, 0, 4 << 20, 0x00020000);
  const int fr = lane & 15, fq = lane >> 4;
  const int sidx = fr < 8 ? 0x4444 : 0xEEEE;
  u32x8 w[2][10];
  char* memA = sm;
  const int nmem = wid < 2 ? 2 : 1;
  if (role < 32) {
    const int h = role;
    load_wfrags([&](int half) { return (half ? Wk : Wq) + h * DH; }, [&](int k, int cc) { return (unsigned)(k * HD + (cc & 15) * 8) * 2u; }, w, sm, tid);
    char* qk = sm + 10240;
    const bf16* K = (const bf16*)(ws + WS_K);
    bf16* KM = (bf16*)(ws + WS_KM);
    const float cs = 0.08838834764831845f * 1.4426950408889634f;
    for (int s = 0; s < NSEG; ++s) {
      u32x4 kf[2][4];
      if (wid == 0 && s < NSEG - 1) {
#pragma unroll
        for (int i = 0; i < 2; ++i)
#pragma unroll
          for (int t = 0; t < 4; ++t) kf[i][t] = *(const u32x4*)(K + (size_t)(s * 32 + i * 16 + fr) * HD + h * DH + t * 32 + fq * 8);
      }
      { unsigned off[2] = {xoff(s, wid * 1024 + lane * 16, par), xoff(s, (8 + wid) * 1024 + lane * 16, par)};
        u32x4 v[2];
        poll_frags<2>(xr, off, v, nmem);
        *(u32x4*)(memA + wid * 1024 + lane * 16) = v[0];
        if (wid < 2) *(u32x4*)(memA + (8 + wid) * 1024 + lane * 16) = v[1]; }
      __syncthreads();
      f32x4 a0 = {}, a1 = {};
#pragma unroll
      for (int f = 0; f < 10; ++f) {
        u32x4 a = *(const u32x4*)(memA + f * 1024 + lane * 16);
        a0 = smf(a, w[0][f], a0, sidx); a1 = smf(a, w[1][f], a1, sidx);
      }
      { const int col = (2 * wid + (lane >> 5)) * 16 + fr;
        char* dst = qk + (col >> 7) * 2176 + (fq & 1) * 4 * 272 + (col & 127) * 2;
#pragma unroll
        for (int r = 0; r < 4; ++r) {
          auto sw = __builtin_amdgcn_permlane32_swap(__float_as_uint(a0[r]), __float_as_uint(a1[r]), false, false);
          float v = __uint_as_float(sw[0]) + __uint_as_float(sw[1]);
          *(unsigned short*)(dst + r * 272) = (unsigned short)(pack2(v, v) & 0xffffu);
        } }
      __syncthreads();
      if (wid == 0) {
        if (s < NSEG - 1) {
          f32x4 sc[3] = {};
#pragma unroll
          for (int t = 0; t < 4; ++t) {
            u32x4 qf = *(const u32x4*)(qk + (fr & 7) * 272 + t * 64 + fq * 16);
            u32x4 km = *(const u32x4*)(qk + 2176 + (fr & 7) * 272 + t * 64 + fq * 16);
            sc[0] = mfma16(km, qf, sc[0]); sc[1] = mfma16(kf[0][t], qf, sc[1]); sc[2] = mfma16(kf[1][t], qf, sc[2]);
          }
          float mx = -INFINITY;
#pragma unroll
          for (int r = 0; r < 4; ++r) { if (fq >= 2) sc[0][r] = -INFINITY; mx = fmaxf(mx, fmaxf(sc[0][r], fmaxf(sc[1][r], sc[2][r]))); }
          mx = fmaxf(mx, __shfl_xor(mx, 16)); mx = fmaxf(mx, __shfl_xor(mx, 32));
          float sum = 0.f;
#pragma unroll
          for (int k = 0; k < 3; ++k)
#pragma unroll
            for (int r = 0; r < 4; ++r) { sc[k][r] = __builtin_amdgcn_exp2f((sc[k][r] - mx) * cs); sum += sc[k][r]; }
          sum += __shfl_xor(sum, 16); sum += __shfl_xor(sum, 32);
          const float inv = 1.f / sum;
          if (fr < 8) {
            const int O = 5 * h + 1 + fq, Om = 5 * h;
            u32x4 o = {canon(pack2(sc[1][0] * inv, sc[1][1] * inv)), canon(pack2(sc[1][2] * inv, sc[1][3] * inv)), canon(pack2(sc[2][0] * inv, sc[2][1] * inv)), canon(pack2(sc[2][2] * inv, sc[2][3] * inv))};
            __builtin_amdgcn_raw_buffer_store_b128(o, xr, xoff(s, 10240 + (O >> 3) * 1024 + ((O & 3) * 16 + ((O >> 2) & 1) * 8 + fr) * 16, par), 0, 16);
            if (fq < 2) {
              u32x2 o2 = {canon(pack2(sc[0][0] * inv, sc[0][1] * inv)), canon(pack2(sc[0][2] * inv, sc[0][3] * inv))};
              __builtin_amdgcn_raw_buffer_store_b64(o2, xr, xoff(s, 10240 + (Om >> 3) * 1024 + ((Om & 3) * 16 + ((Om >> 2) & 1) * 8 + fr) * 16 + fq * 8, par), 0, 16);
            }
          }
        }
      } else if (wid == 4 || wid == 5) {
        const int i = (wid - 4) * 64 + lane;
        u32x4 v = *(const u32x4*)(qk + 2176 + (i >> 4) * 272 + (i & 15) * 16);
        *(u32x4*)(KM + ((size_t)(s * NH + h) * 8 + (i >> 4)) * DH + (i & 15) * 8) = v;
      }
    }
  } else {
    const int b = role - 32;
    const bf16* U = (const bf16*)(ws + WS_U) + (size_t)b * NH * D_ * 8;
    load_wfrags([&](int half) { return U; }, [&](int k, int cc) { return (unsigned)(cc * D_ + k) * 16u; }, w, sm, tid);
    char* mu = sm + 10240;
    float* red = (float*)(sm + 16384);
    bf16* VO = (bf16*)(ws + WS_VO);
    const int ng = wid < 4 ? 3 : 2;
    for (int s = 0; s < NSEG; ++s) {
      bf16* vo = VO + ((size_t)s * 80 + b) * 20 * 64 * 8;
      u32x4 vf[3];
      if (s < NSEG - 1) {
#pragma unroll
        for (int i = 0; i < 3; ++i) if (i < ng) vf[i] = *(const u32x4*)(vo + ((wid + 8 * i) * 64 + lane) * 8);
      }
      { unsigned off[2] = {xoff(s, wid * 1024 + lane * 16, par), xoff(s, (8 + wid) * 1024 + lane * 16, par)};
        u32x4 v[2];
        poll_frags<2>(xr, off, v, nmem);
        *(u32x4*)(memA + wid * 1024 + lane * 16) = v[0];
        if (wid < 2) *(u32x4*)(memA + (8 + wid) * 1024 + lane * 16) = v[1]; }
      __syncthreads();
      f32x4 a0 = {}, a1 = {};
#pragma unroll
      for (int f = 0; f < 10; ++f) {
        u32x4 a = *(const u32x4*)(memA + f * 1024 + lane * 16);
        a0 = smf(a, w[0][f], a0, sidx); a1 = smf(a, w[1][f], a1, sidx);
      }
      { float v[4];
#pragma unroll
        for (int r = 0; r < 4; ++r) {
          auto sw = __builtin_amdgcn_permlane32_swap(__float_as_uint(a0[r]), __float_as_uint(a1[r]), false, false);
          v[r] = __uint_as_float(sw[0]) + __uint_as_float(sw[1]);
        }
        const int col = (2 * wid + (lane >> 5)) * 16 + fr;
        *(u32x2*)(mu + col * 16 + (fq & 1) * 8) = u32x2{pack2(v[0], v[1]), pack2(v[2], v[3])}; }
      __syncthreads();
      if (tid < 256) {
        const int hh = tid >> 3, c = tid & 7, O = 5 * hh;
        *(u32x4*)(vo + ((O >> 3) * 64 + (O & 3) * 16 + ((O >> 2) & 1) * 8 + c) * 8) = *(const u32x4*)(mu + tid * 16);
      }
      if (s < NSEG - 1) {
        unsigned off[3];
#pragma unroll
        for (int i = 0; i < 3; ++i) {
          const int g = wid + 8 * i, O = 8 * g + 4 * (fr >> 3) + fq;
          off[i] = xoff(s, 10240 + (i < ng ? g : wid) * 1024 + lane * 16, par);
          if (i < ng && O % 5 == 0) vf[i] = *(const u32x4*)(mu + ((O / 5) * 8 + (fr & 7)) * 16);
        }
        u32x4 pv[3];
        poll_frags<3>(xr, off, pv, ng);
        f32x4 acc = {};
#pragma unroll
        for (int i = 0; i < 3; ++i) if (i < ng) acc = mfma16(pv[i], vf[i], acc);
        if ((fr >> 3) == (fq >> 1)) {
#pragma unroll
          for (int r = 0; r < 4; ++r) red[(((fq & 1) * 4 + r) * 8 + (fr & 7)) * 20 + wid * 2 + (fr >> 3)] = acc[r];
        }
        __syncthreads();
        if (wid == 0) {
          const f32x4* rp = (const f32x4*)(red + lane * 20);
          f32x4 t0 = rp[0] + rp[1], t1 = rp[2] + rp[3]; t0 += t1;
          float v = (t0[0] + t0[1]) + (t0[2] + t0[3]);
          float nb = __shfl_down(v, 1);
          if (!(lane & 1)) __builtin_amdgcn_raw_buffer_store_b32(canon(pack2(v, nb)), xr, xoff(s + 1, b * 128 + lane * 2, par), 0, 16);
        }
      }
    }
  }
}

__global__ void __launch_bounds__(512) k_t1(char* ws) {
  const int w = blockIdx.x * 8 + (threadIdx.x >> 6);
  wave_t1((const bf16*)(ws + WS_Q), (const bf16*)(ws + WS_K), (const bf16*)(ws + WS_KM), (bf16*)(ws + WS_P), w >> 5, w & 31, threadIdx.x & 63);
}
__global__ void __launch_bounds__(512) k_t2(const bf16* x, char* ws) {
  extern __shared__ __attribute__((aligned(16))) char sm[];
  task_t2(x, (const bf16*)(ws + WS_P), (const bf16*)(ws + WS_VO), (bf16*)(ws + WS_H), blockIdx.x / 10, blockIdx.x % 10, sm, threadIdx.x);
}
__global__ void __launch_bounds__(512) k_f1(const bf16* Wg, const bf16* Wu, char* ws) {
  extern __shared__ __attribute__((aligned(16))) char sm[];
  task_f1((const bf16*)(ws + WS_H), Wg, Wu, (bf16*)(ws + WS_G), blockIdx.x / 9, (blockIdx.x % 9) * 3, 3, sm, threadIdx.x);
}
__global__ void __launch_bounds__(512) k_f2(const bf16* Wd, char* ws, bf16* out) {
  extern __shared__ __attribute__((aligned(16))) char sm[];
  task_f2((const bf16*)(ws + WS_G), Wd, (const bf16*)(ws + WS_H), out, blockIdx.x / 5, blockIdx.x % 5, sm, threadIdx.x);
}

extern "C" void kernel_launch(void* const* d_in, const int* in_sizes, int n_in, void* d_out, int out_size, void* d_ws, size_t ws_size, hipStream_t stream) {
  const bf16 *x = (const bf16*)d_in[0], *mem0 = (const bf16*)d_in[1], *Wq = (const bf16*)d_in[2], *Wk = (const bf16*)d_in[3], *Wv = (const bf16*)d_in[4], *Wo = (const bf16*)d_in[5];
  const bf16 *Wg = (const bf16*)d_in[8], *Wu = (const bf16*)d_in[9], *Wd = (const bf16*)d_in[10];
  char* ws = (char*)(((size_t)d_ws + (2 * MB - 1)) & ~(size_t)(2 * MB - 1));
  {
    (void)hipFuncSetAttribute((const void*)k_prologue, hipFuncAttributeMaxDynamicSharedMemorySize, 147456);
    (void)hipFuncSetAttribute((const void*)k_proj, hipFuncAttributeMaxDynamicSharedMemorySize, 147456);
    (void)hipFuncSetAttribute((const void*)k_rec, hipFuncAttributeMaxDynamicSharedMemorySize, 131072);
    (void)hipFuncSetAttribute((const void*)k_t2, hipFuncAttributeMaxDynamicSharedMemorySize, 69632);
    (void)hipFuncSetAttribute((const void*)k_f1, hipFuncAttributeMaxDynamicSharedMemorySize, 131072);
    (void)hipFuncSetAttribute((const void*)k_f2, hipFuncAttributeMaxDynamicSharedMemorySize, 98304);
  }
  k_prologue<<<65 + 320, 512, 147456, stream>>>(mem0, Wv, Wo, ws);
  k_proj<<<1024, 512, 147456, stream>>>(x, Wq, Wk, Wv, Wo, ws);
  k_rec<<<256, 512, 131072, stream>>>(Wq, Wk, ws);
  k_t1<<<256, 512, 0, stream>>>(ws);
  k_t2<<<640, 512, 69632, stream>>>(x, ws);
  k_f1<<<288, 512, 131072, stream>>>(Wg, Wu, ws);
  k_f2<<<160, 512, 98304, stream>>>(Wd, ws, (bf16*)d_out);
}
```

```cpp
#include <hip/hip_runtime.h>
#include <hip/hip_bf16.h>

typedef __hip_bfloat16 bf16;
typedef __bf16 bf16x8 __attribute__((ext_vector_type(8)));
typedef __bf16 bf16x16 __attribute__((ext_vector_type(16)));
typedef float f32x4 __attribute__((ext_vector_type(4)));
typedef unsigned u32x4 __attribute__((ext_vector_type(4)));
typedef unsigned u32x2 __attribute__((ext_vector_type(2)));
typedef unsigned u32x8 __attribute__((ext_vector_type(8)));
typedef short s16x4 __attribute__((ext_vector_type(4)));
typedef __attribute__((address_space(3))) s16x4* lds_s16x4_p;

constexpr int S_ = 2048, D_ = 640, NH = 32, DH = 128, HD = 4096, FF = 1728, NSEG = 64;
constexpr size_t MB = 1u << 20;
#define NC 1
constexpr size_t WS_XCH = 0;
constexpr size_t WS_LP = 4 * MB;
constexpr size_t WS_CTL = 16 * MB;
constexpr size_t WS_Q = 17 * MB;
constexpr size_t WS_K = WS_Q + 16 * MB;
constexpr size_t WS_U = WS_K + 16 * MB;
constexpr size_t WS_VO = WS_U + 25 * MB;
constexpr size_t WS_KM = WS_VO + 100 * MB;
constexpr size_t WS_P = WS_KM + 4 * MB;
constexpr size_t WS_H = WS_P + 5 * MB;
constexpr size_t WS_G = WS_H + 3 * MB;
constexpr size_t WS_XN = WS_G + 7 * MB;
constexpr size_t WS_ST = WS_XN + 3 * MB;
constexpr size_t WS_CS = WS_ST + 1 * MB;
constexpr size_t WS_END = WS_CS + 1 * MB;

constexpr unsigned SENT = 0xFFFFFFFFu;
constexpr unsigned SPIN_CAP = 1u << 22;

__device__ __forceinline__ unsigned pack2(float lo, float hi) {
  typedef __bf16 b2 __attribute__((ext_vector_type(2)));
  typedef float f2 __attribute__((ext_vector_type(2)));
  return __builtin_bit_cast(unsigned, __builtin_convertvector(f2{lo, hi}, b2));
}
__device__ __forceinline__ u32x4 pack8(f32x4 a, f32x4 b) { return u32x4{pack2(a[0], a[1]), pack2(a[2], a[3]), pack2(b[0], b[1]), pack2(b[2], b[3])}; }
__device__ __forceinline__ float bflo(unsigned w) { return __uint_as_float(w << 16); }
__device__ __forceinline__ float bfhi(unsigned w) { return __uint_as_float(w & 0xffff0000u); }
__device__ __forceinline__ unsigned canon(unsigned w) { return min(w, 0xFFFFFFFEu); }
__device__ __forceinline__ void glds16(const void* base, unsigned voff, unsigned lds) {
  asm volatile("s_mov_b32 m0, %2\n\ts_nop 0\n\tglobal_load_lds_dwordx4 %0, %1" ::"v"(voff), "s"(base), "s"(lds) : "m0");
}
__device__ __forceinline__ unsigned lds_addr(const void* p) { return (unsigned)(size_t)(const __attribute__((address_space(3))) char*)p; }
__device__ __forceinline__ u32x2 tr_read(const char* p) {
  s16x4 r = __builtin_amdgcn_ds_read_tr16_b64_v4i16((lds_s16x4_p)(p));
  return __builtin_bit_cast(u32x2, r);
}
#define WAIT_V(n) asm volatile("s_waitcnt vmcnt(" #n ")" ::: "memory")
#define WAIT_L0() asm volatile("s_waitcnt lgkmcnt(0)" ::: "memory")
#define BAR() asm volatile("s_barrier" ::: "memory")
#define LBAR() asm volatile("s_waitcnt lgkmcnt(0)\n\ts_barrier" ::: "memory")
__device__ __forceinline__ f32x4 mfma16(u32x4 a, u32x4 b, f32x4 c) {
  return __builtin_amdgcn_mfma_f32_16x16x32_bf16(__builtin_bit_cast(bf16x8, a), __builtin_bit_cast(bf16x8, b), c, 0, 0, 0);
}
__device__ __forceinline__ f32x4 smf(u32x4 a, u32x8 b, f32x4 c, int idx) {
  return __builtin_amdgcn_smfmac_f32_16x16x64_bf16(__builtin_bit_cast(bf16x8, a), __builtin_bit_cast(bf16x16, b), c, idx, 0, 0);
}
__device__ __forceinline__ unsigned xcc_id() { return (unsigned)__builtin_amdgcn_s_getreg(20 | (3 << 11)) & 0xfu; }

__device__ __forceinline__ int bswz(int row) { return (((row >> 2) ^ (row >> 3)) & 1) << 1; }
__device__ __forceinline__ void stageB_rc(int p, int& row, int& ch) {
  row = ((p >> 7) << 3) | ((p >> 2) & 7);
  ch = (((p >> 5) & 3) << 2) | ((p & 3) ^ bswz(row));
}
template <bool PERM>
__device__ __forceinline__ unsigned trB_base(int lane, int j) {
  const int g = lane >> 4, q = (lane & 15) >> 2, p = lane & 3;
  return PERM ? 2048 * (g >> 1) + 64 * (4 * (g & 1) + q) + 8 * p + 32 * ((j ^ g ^ (g >> 1)) & 1) : 2048 * g + 64 * q + 8 * p + 32 * ((j ^ g) & 1);
}
__device__ __forceinline__ u32x2 tr_read_a(unsigned a) {
  s16x4 r = __builtin_amdgcn_ds_read_tr16_b64_v4i16((lds_s16x4_p)(size_t)a);
  return __builtin_bit_cast(u32x2, r);
}

template <bool SW, bool PERM, int CSTR>
__device__ __forceinline__ void mma_tile(const u32x4& a00, const u32x4& a10, const u32x4& a01, const u32x4& a11, unsigned tb0, unsigned tb1, int so, f32x4 (&acc)[2][4]) {
#pragma unroll
  for (int ks = 0; ks < 2; ++ks) {
    u32x4 b[4];
#pragma unroll
    for (int n = 0; n < 4; ++n) {
      const int o = so + 8192 * ks + CSTR * (n >> 1);
      u32x2 lo = tr_read_a(((n & 1) ? tb1 : tb0) + o), hi = tr_read_a((PERM ? ((n & 1) ? tb1 : tb0) + 4096 : ((n & 1) ? tb0 : tb1) + 256) + o);
      b[n] = u32x4{lo.x, lo.y, hi.x, hi.y};
    }
    const u32x4 a0 = ks ? a01 : a00, a1 = ks ? a11 : a10;
#pragma unroll
    for (int n = 0; n < 4; ++n) {
      acc[0][n] = SW ? mfma16(b[n], a0, acc[0][n]) : mfma16(a0, b[n], acc[0][n]);
      acc[1][n] = SW ? mfma16(b[n], a1, acc[1][n]) : mfma16(a1, b[n], acc[1][n]);
    }
  }
}

__device__ __forceinline__ void ln_tile(const bf16* src  , bf16* dst, int lane) {
  const int fr = lane & 15, fq = lane >> 4;
  u32x4 af[20];
#pragma unroll
  for (int s = 0; s < 20; ++s) af[s] = *(const u32x4*)(src + (size_t)fr * D_ + s * 32 + fq * 8);
  float s = 0.f;
#pragma unroll
  for (int i = 0; i < 20; ++i)
#pragma unroll
    for (int e = 0; e < 4; ++e) s += bflo(af[i][e]) + bfhi(af[i][e]);
  s += __shfl_xor(s, 16); s += __shfl_xor(s, 32);
  const float mean = s * (1.f / 640.f);
  float q = 0.f;
#pragma unroll
  for (int i = 0; i < 20; ++i)
#pragma unroll
    for (int e = 0; e < 4; ++e) { float a = bflo(af[i][e]) - mean, b = bfhi(af[i][e]) - mean; q += a * a + b * b; }
  q += __shfl_xor(q, 16); q += __shfl_xor(q, 32);
  const float rstd = 1.f / sqrtf(q * (1.f / 640.f) + 1e-5f);
#pragma unroll
  for (int i = 0; i < 20; ++i) {
    u32x4 o;
#pragma unroll
    for (int e = 0; e < 4; ++e) o[e] = pack2((bflo(af[i][e]) - mean) * rstd, (bfhi(af[i][e]) - mean) * rstd);
    *(u32x4*)(dst + i * 512 + lane * 8) = o;
  }
}
__device__ __forceinline__ void load_af(const bf16* src, u32x4 (&af)[2][20], int lane) {
#pragma unroll
  for (int s = 0; s < 20; ++s)
#pragma unroll
    for (int m = 0; m < 2; ++m) af[m][s] = *(const u32x4*)(src + (m * 20 + s) * 512 + lane * 8);
}

constexpr int RING = 5;
#define ZERO4 f32x4{0.f, 0.f, 0.f, 0.f}
__device__ __forceinline__ void ring_wait(int t, int T) {
  const int rem = T - 1 - t;
  if (rem >= 3) { WAIT_V(6); } else if (rem == 2) { WAIT_V(4); } else if (rem == 1) { WAIT_V(2); } else { WAIT_V(0); }
  LBAR();
}

__device__ __forceinline__ void task_kq(const bf16* x, const bf16* W, bf16* out, int rb, int h0, int nh, char* sm, int tid) {
  const int lane = tid & 63, wid = __builtin_amdgcn_readfirstlane(tid >> 6), wm = wid >> 1, wn = wid & 1;
  const unsigned lw = lds_addr(sm) + wid * 1024;
  const unsigned tb0 = lds_addr(sm) + wn * 1024 + trB_base<false>(lane, 0), tb1 = lds_addr(sm) + wn * 1024 + trB_base<false>(lane, 1);
  int r0, c0, r1, c1; stageB_rc(tid, r0, c0); stageB_rc(tid + 512, r1, c1);
  const unsigned o0 = (r0 * HD + c0 * 8) * 2, o1 = (r1 * HD + c1 * 8) * 2;
  const int T = nh * 10;
  auto issue = [&](int hh, int u) {
    const bf16* b = W + (size_t)(u * 64) * HD + (h0 + hh) * DH;
    const unsigned d = lw + (u % RING) * 16384;
    glds16(b, o0, d); glds16(b, o1, d + 8192);
  };
#pragma unroll
  for (int u = 0; u < RING - 1; ++u) issue(0, u);
  u32x4 af[2][20];
  load_af(x + (size_t)(rb * 8 + wm * 2) * 10240, af, lane);
  for (int hh = 0; hh < nh; ++hh) {
    f32x4 acc[2][4] = {};
#pragma unroll
    for (int u = 0; u < 10; ++u) {
      ring_wait(hh * 10 + u, T);
      if (u + RING - 1 < 10) issue(hh, u + RING - 1); else if (hh + 1 < nh) issue(hh + 1, u + RING - 1 - 10);
      mma_tile<true, false, 512>(af[0][2 * u], af[1][2 * u], af[0][2 * u + 1], af[1][2 * u + 1], tb0, tb1, (u % RING) * 16384, acc);
    }
    bf16* o = out + ((size_t)((rb * 4 + wm) * NH + h0 + hh) * 8 + 2 * wn) * 512 + lane * 8;
#pragma unroll
    for (int m = 0; m < 2; ++m)
#pragma unroll
      for (int t = 0; t < 2; ++t) *(u32x4*)(o + (m * 4 + t) * 512) = pack8(acc[m][2 * t], acc[m][2 * t + 1]);
  }
  LBAR();
}

template <int NHH>
__device__ __forceinline__ void task_vvo(const bf16* x, const bf16* Wv, const bf16* Wo, bf16* VO, int rb, int h0, char* sm, int tid) {
  const int lane = tid & 63, wid = __builtin_amdgcn_readfirstlane(tid >> 6), wm = wid >> 1, wn = wid & 1;
  const unsigned lw = lds_addr(sm) + wid * 1024;
  const unsigned tb0 = lds_addr(sm) + wn * 1024 + trB_base<false>(lane, 0), tb1 = lds_addr(sm) + wn * 1024 + trB_base<false>(lane, 1);
  const unsigned tp0 = lds_addr(sm) + wn * 1024 + trB_base<true>(lane, 0), tp1 = lds_addr(sm) + wn * 1024 + trB_base<true>(lane, 1);
  char* vbuf = sm + RING * 16384;
  int r0, c0, r1, c1; stageB_rc(tid, r0, c0); stageB_rc(tid + 512, r1, c1);
  const unsigned o0 = (r0 * HD + c0 * 8) * 2, o1 = (r1 * HD + c1 * 8) * 2, p0 = (r0 * D_ + c0 * 8) * 2, p1 = (r1 * D_ + c1 * 8) * 2;
  constexpr int T = NHH * 20;
  auto issue = [&](int t) {
    const unsigned d = lw + (t % RING) * 16384;
    if (t < NHH * 10) {
      const bf16* b = Wv + (size_t)((t % 10) * 64) * HD + (h0 + t / 10) * DH;
      glds16(b, o0, d); glds16(b, o1, d + 8192);
    } else {
      const int v = (t - NHH * 10) % 10, h = h0 + (t - NHH * 10) / 10;
      const bf16* b = Wo + (size_t)(h * DH + (v & 1) * 64) * D_ + (v >> 1) * 128;
      glds16(b, p0, d); glds16(b, p1, d + 8192);
    }
  };
#pragma unroll
  for (int u = 0; u < RING - 1; ++u) issue(u);
  {
    u32x4 af[2][20];
    load_af(x + (size_t)(rb * 8 + wm * 2) * 10240, af, lane);
#pragma unroll
    for (int hh = 0; hh < NHH; ++hh) {
      f32x4 acc[2][4] = {};
#pragma unroll
      for (int u = 0; u < 10; ++u) {
        ring_wait(hh * 10 + u, T);
        issue(hh * 10 + u + RING - 1);
        mma_tile<true, false, 512>(af[0][2 * u], af[1][2 * u], af[0][2 * u + 1], af[1][2 * u + 1], tb0, tb1, (u % RING) * 16384, acc);
      }
#pragma unroll
      for (int m = 0; m < 2; ++m)
#pragma unroll
        for (int t = 0; t < 2; ++t) *(u32x4*)(vbuf + (((hh * 4 + wm) * 2 + m) * 4 + 2 * wn + t) * 1024 + lane * 16) = pack8(acc[m][2 * t], acc[m][2 * t + 1]);
    }
  }
#pragma unroll
  for (int hh = 0; hh < NHH; ++hh) {
    const int h = h0 + hh;
    const int seg = rb * 4 + wm, O = 5 * h + 1 + (lane >> 4);
    bf16* vo = VO + (((size_t)seg * 80 * 20 + (O >> 3)) * 64 + (O & 3) * 16 + ((O >> 2) & 1) * 8 + (lane & 7)) * 8 + (size_t)((lane >> 3) & 1) * (20 * 64 * 8);
    u32x4 vf[2][4];
#pragma unroll
    for (int nt = 0; nt < 5; ++nt) {
      f32x4 acc[2][4] = {};
#pragma unroll
      for (int kh = 0; kh < 2; ++kh) {
        const int u = NHH * 10 + hh * 10 + nt * 2 + kh;
        ring_wait(u, T);
        if (u + RING - 1 < T) issue(u + RING - 1);
        if (nt == 0 && kh == 0) {
#pragma unroll
          for (int m = 0; m < 2; ++m)
#pragma unroll
            for (int t = 0; t < 4; ++t) vf[m][t] = *(const u32x4*)(vbuf + (((hh * 4 + wm) * 2 + m) * 4 + t) * 1024 + lane * 16);
        }
        mma_tile<false, true, 512>(vf[0][2 * kh], vf[1][2 * kh], vf[0][2 * kh + 1], vf[1][2 * kh + 1], tp0, tp1, (u % RING) * 16384, acc);
      }
#pragma unroll
      for (int n = 0; n < 4; ++n)
        *(u32x4*)(vo + (size_t)(nt * 16 + wn * 8 + n * 2) * (20 * 64 * 8)) = pack8(acc[0][n], acc[1][n]);
    }
  }
  LBAR();
}

__device__ __forceinline__ void task_u(const bf16* Wv, const bf16* Wo, bf16* U, int h, int db, char* sm, int tid) {
  const int lane = tid & 63, wid = __builtin_amdgcn_readfirstlane(tid >> 6), wm = wid >> 1, wn = wid & 1;
  const unsigned lw = lds_addr(sm) + wid * 1024;
  const unsigned tb0 = lds_addr(sm) + wn * 1024 + trB_base<false>(lane, 0), tb1 = lds_addr(sm) + wn * 1024 + trB_base<false>(lane, 1);
  int r0, c0, r1, c1; stageB_rc(tid, r0, c0); stageB_rc(tid + 512, r1, c1);
  const unsigned p0 = (r0 * D_ + c0 * 8) * 2, p1 = (r1 * D_ + c1 * 8) * 2;
  auto issue = [&](int v) {
    const unsigned d = lw + (v % RING) * 16384;
    const bf16* b = Wo + (size_t)(h * DH + (v & 1) * 64) * D_ + (v >> 1) * 128;
    glds16(b, p0, d); glds16(b, p1, d + 8192);
  };
#pragma unroll
  for (int u = 0; u < RING - 1; ++u) issue(u);
  u32x4 vf[2][4];
#pragma unroll
  for (int m = 0; m < 2; ++m)
#pragma unroll
    for (int s = 0; s < 4; ++s) vf[m][s] = *(const u32x4*)(Wv + (size_t)(db * 128 + wm * 32 + m * 16 + (lane & 15)) * HD + h * DH + s * 32 + (lane >> 4) * 8);
#pragma unroll
  for (int nt = 0; nt < 5; ++nt) {
    f32x4 acc[2][4] = {};
#pragma unroll
    for (int kh = 0; kh < 2; ++kh) {
      const int v = nt * 2 + kh;
      ring_wait(v, 10);
      if (v + RING - 1 < 10) issue(v + RING - 1);
      mma_tile<true, false, 512>(vf[0][2 * kh], vf[1][2 * kh], vf[0][2 * kh + 1], vf[1][2 * kh + 1], tb0, tb1, (v % RING) * 16384, acc);
    }
#pragma unroll
    for (int m = 0; m < 2; ++m)
#pragma unroll
      for (int n = 0; n < 4; ++n) {
        const int d1 = db * 128 + wm * 32 + m * 16 + (lane & 15);
        const int d2 = nt * 128 + wn * 64 + n * 16 + (lane >> 4) * 4;
        u32x2 o = {pack2(acc[m][n][0], acc[m][n][1]), pack2(acc[m][n][2], acc[m][n][3])};
        *(u32x2*)(U + (((size_t)(d2 >> 3) * NH + h) * D_ + d1) * 8 + (d2 & 7)) = o;
      }
  }
  LBAR();
}

__device__ __forceinline__ void wave_t1(const bf16* Q, const bf16* K, const bf16* KM, bf16* P, int seg, int h, int lane) {
  const int fr = lane & 15, fq = lane >> 4;
  u32x4 qf[2][4], kf[3][4];
  const size_t fb = (size_t)(seg * NH + h) * 4096 + lane * 8;
#pragma unroll
  for (int t = 0; t < 4; ++t) {
    kf[0][t] = *(const u32x4*)(KM + (size_t)(seg * NH + h) * 1024 + ((t * 4 + fq) * 8 + (fr & 7)) * 8);
#pragma unroll
    for (int i = 0; i < 2; ++i) {
      qf[i][t] = *(const u32x4*)(Q + fb + (i * 4 + t) * 512);
      kf[i + 1][t] = *(const u32x4*)(K + fb + (i * 4 + t) * 512);
    }
  }
  const float cs = 0.08838834764831845f * 1.4426950408889634f;
#pragma unroll
  for (int i = 0; i < 2; ++i) {
    f32x4 sc[3] = {};
#pragma unroll
    for (int t = 0; t < 4; ++t)
#pragma unroll
      for (int k = 0; k < 3; ++k) sc[k] = mfma16(kf[k][t], qf[i][t], sc[k]);
    const int qi = i * 16 + fr;
    float mx = -INFINITY;
#pragma unroll
    for (int r = 0; r < 4; ++r) {
      if (fq >= 2) sc[0][r] = -INFINITY;
      if (fq * 4 + r > qi) sc[1][r] = -INFINITY;
      if (16 + fq * 4 + r > qi) sc[2][r] = -INFINITY;
      mx = fmaxf(mx, fmaxf(sc[0][r], fmaxf(sc[1][r], sc[2][r])));
    }
    mx = fmaxf(mx, __shfl_xor(mx, 16)); mx = fmaxf(mx, __shfl_xor(mx, 32));
    float sum = 0.f;
#pragma unroll
    for (int k = 0; k < 3; ++k)
#pragma unroll
      for (int r = 0; r < 4; ++r) { sc[k][r] = __builtin_amdgcn_exp2f((sc[k][r] - mx) * cs); sum += sc[k][r]; }
    sum += __shfl_xor(sum, 16); sum += __shfl_xor(sum, 32);
    const float inv = 1.f / sum;
    bf16* prow = P + ((size_t)(seg * 32 + qi) * 160 + 5 * h) * 8;
    *(u32x4*)(prow + (1 + fq) * 8) = pack8(sc[1] * inv, sc[2] * inv);
    if (fq < 2) { u32x2 o2 = {pack2(sc[0][0] * inv, sc[0][1] * inv), pack2(sc[0][2] * inv, sc[0][3] * inv)}; *(u32x2*)(prow + fq * 4) = o2; }
  }
}

__device__ __forceinline__ void task_t2(const bf16* x, const bf16* P, const bf16* VO, bf16* Hb, float* ST, int seg, int cs, char* sm, int tid) {
  const int lane = tid & 63, wid = tid >> 6, fr = lane & 15, fq = lane >> 4;
  f32x4 acc[2][4] = {};
  u32x4 a[5][2], b[5][4];
#pragma unroll
  for (int k = 0; k < 5; ++k) {
    const int O = (wid * 5 + k) * 4 + fq;
#pragma unroll
    for (int m = 0; m < 2; ++m) a[k][m] = *(const u32x4*)(P + ((size_t)(seg * 32 + m * 16 + fr) * 160 + O) * 8);
#pragma unroll
    for (int n = 0; n < 4; ++n) {
      const int col = cs * 64 + n * 16 + fr;
      b[k][n] = *(const u32x4*)(VO + ((((size_t)seg * 80 + (col >> 3)) * 20 + (O >> 3)) * 64 + (O & 3) * 16 + ((O >> 2) & 1) * 8 + (col & 7)) * 8);
    }
  }
#pragma unroll
  for (int k = 0; k < 5; ++k)
#pragma unroll
    for (int m = 0; m < 2; ++m)
#pragma unroll
      for (int n = 0; n < 4; ++n) acc[m][n] = mfma16(b[k][n], a[k][m], acc[m][n]);
  float* red = (float*)sm;
#pragma unroll
  for (int m = 0; m < 2; ++m)
#pragma unroll
    for (int n = 0; n < 4; ++n) *(f32x4*)(red + (wid * 32 + m * 16 + fr) * 68 + n * 16 + fq * 4) = acc[m][n];
  LBAR();
  { const int row = tid >> 4, c4 = (tid & 15) * 4;
    f32x4 s = *(const f32x4*)(red + row * 68 + c4);
#pragma unroll
    for (int w = 1; w < 8; ++w) s += *(const f32x4*)(red + (w * 32 + row) * 68 + c4);
    const size_t go = (size_t)(seg * 32 + row) * D_ + cs * 64 + c4;
    u32x2 xv = *(const u32x2*)(x + go);
    unsigned a0 = pack2(s[0], s[1]), a1 = pack2(s[2], s[3]);
    u32x2 o = {pack2(bflo(xv.x) + bflo(a0), bfhi(xv.x) + bfhi(a0)), pack2(bflo(xv.y) + bflo(a1), bfhi(xv.y) + bfhi(a1))};
    const int col = cs * 64 + c4, grow = seg * 32 + row;
    *(u32x2*)(Hb + (((size_t)(grow >> 4) * 20 + (col >> 5)) * 64 + ((col >> 3) & 3) * 16 + (grow & 15)) * 8 + (col & 7)) = o;
    const float h0 = bflo(o.x), h1 = bfhi(o.x), h2 = bflo(o.y), h3 = bfhi(o.y);
    float sm4 = (h0 + h1) + (h2 + h3);
#pragma unroll
    for (int d = 1; d < 16; d <<= 1) sm4 += __shfl_xor(sm4, d);
    const float mu = sm4 * (1.f / 64.f);
    float m2 = (h0 - mu) * (h0 - mu) + (h1 - mu) * (h1 - mu) + (h2 - mu) * (h2 - mu) + (h3 - mu) * (h3 - mu);
#pragma unroll
    for (int d = 1; d < 16; d <<= 1) m2 += __shfl_xor(m2, d);
    if ((tid & 15) == 0) *(float2*)(ST + ((size_t)grow * 10 + cs) * 2) = float2{sm4, m2};
  }
  LBAR();
}

__device__ __forceinline__ void task_f1(const bf16* Hb, const float* ST, const float* CS, const bf16* Wg, const bf16* Wu, bf16* G, int rb, int f0, int nt, char* sm, int tid) {
  const int lane = tid & 63, wid = __builtin_amdgcn_readfirstlane(tid >> 6), wm = wid >> 1, wn = wid & 1;
  const unsigned lw = lds_addr(sm) + wid * 1024;
  const unsigned tb0 = lds_addr(sm) + wn * 512 + trB_base<false>(lane, 0), tb1 = lds_addr(sm) + wn * 512 + trB_base<false>(lane, 1);
  int r0, c0, r1, c1; stageB_rc(tid, r0, c0); stageB_rc(tid + 512, r1, c1);
  const bf16* pw = (wid & 1) ? Wu : Wg;
  const unsigned o0 = (r0 * FF + (c0 & 7) * 8) * 2, o1 = (r1 * FF + (c1 & 7) * 8) * 2;
  const int T = nt * 10;
  auto issue = [&](int ff, int u) {
    const size_t o = (size_t)(u * 64) * FF + (f0 + ff) * 64;
    const unsigned d = lw + (u % RING) * 16384;
    glds16(pw + o, o0, d); glds16(pw + o, o1, d + 8192);
  };
#pragma unroll
  for (int u = 0; u < RING - 1; ++u) issue(0, u);
  u32x4 af[2][20];
  load_af(Hb + (size_t)(rb * 8 + wm * 2) * 10240, af, lane);
  float mean[2], rstd[2];
#pragma unroll
  for (int m = 0; m < 2; ++m) {
    const f32x4* sp = (const f32x4*)(ST + (size_t)(rb * 128 + wm * 32 + m * 16 + (lane & 15)) * 20);
    f32x4 p[5];
#pragma unroll
    for (int i = 0; i < 5; ++i) p[i] = sp[i];
    float s = 0.f;
#pragma unroll
    for (int i = 0; i < 5; ++i) s += p[i][0] + p[i][2];
    mean[m] = s * (1.f / 640.f);
    float q = 0.f;
#pragma unroll
    for (int i = 0; i < 5; ++i) {
      const float d0 = p[i][0] * (1.f / 64.f) - mean[m], d1 = p[i][2] * (1.f / 64.f) - mean[m];
      q += p[i][1] + p[i][3] + 64.f * (d0 * d0 + d1 * d1);
    }
    rstd[m] = 1.f / sqrtf(q * (1.f / 640.f) + 1e-5f);
  }
  for (int ff = 0; ff < nt; ++ff) {
    f32x4 acc[2][4] = {};
#pragma unroll
    for (int u = 0; u < 10; ++u) {
      ring_wait(ff * 10 + u, T);
      if (u + RING - 1 < 10) issue(ff, u + RING - 1); else if (ff + 1 < nt) issue(ff + 1, u + RING - 1 - 10);
      mma_tile<true, false, 1024>(af[0][2 * u], af[1][2 * u], af[0][2 * u + 1], af[1][2 * u + 1], tb0, tb1, (u % RING) * 16384, acc);
    }
    f32x4 cs[4];
#pragma unroll
    for (int n = 0; n < 4; ++n) cs[n] = *(const f32x4*)(CS + (n >> 1) * FF + (f0 + ff) * 64 + (2 * wn + (n & 1)) * 16 + (lane >> 4) * 4);
#pragma unroll
    for (int m = 0; m < 2; ++m) {
#pragma unroll
      for (int n = 0; n < 4; ++n) acc[m][n] = (acc[m][n] - mean[m] * cs[n]) * rstd[m];
      u32x4 g = pack8(acc[m][0], acc[m][1]), u = pack8(acc[m][2], acc[m][3]), o;
      auto rl = [](float v) { return v > 0.f ? v : 0.f * v; };
#pragma unroll
      for (int e = 0; e < 4; ++e) o[e] = pack2(rl(bflo(g[e])) * bflo(u[e]), rl(bfhi(g[e])) * bfhi(u[e]));
      *(u32x4*)(G + ((size_t)(rb * 8 + wm * 2 + m) * 54 + (f0 + ff) * 2 + wn) * 512 + lane * 8) = o;
    }
  }
  LBAR();
}

__device__ __forceinline__ void task_f2(const bf16* G, const bf16* Wd, const bf16* Hb, bf16* out, int rb, int cs, char* sm, int tid) {
  const int lane = tid & 63, wid = __builtin_amdgcn_readfirstlane(tid >> 6), wm = wid >> 1, wn = wid & 1;
  const unsigned lw = lds_addr(sm) + wid * 1024;
  const unsigned tp0 = lds_addr(sm) + wn * 1024 + trB_base<true>(lane, 0), tp1 = lds_addr(sm) + wn * 1024 + trB_base<true>(lane, 1);
  int r0, c0, r1, c1; stageB_rc(tid, r0, c0); stageB_rc(tid + 512, r1, c1);
  const bf16* pa = G + (size_t)(rb * 8 + wid) * 54 * 512;
  const bf16* pb = Wd + cs * 128;
  const unsigned o0 = (r0 * D_ + c0 * 8) * 2, o1 = (r1 * D_ + c1 * 8) * 2;
  constexpr int R4 = 4;
  auto issue = [&](int t) {
    const unsigned d = lds_addr(sm) + (t % R4) * 32768;
    glds16(pa + t * 1024, lane * 16, d + wid * 2048); glds16(pa + t * 1024 + 512, lane * 16, d + wid * 2048 + 1024);
    glds16(pb + (size_t)t * 64 * D_, o0, d + 16384 + wid * 1024); glds16(pb + (size_t)t * 64 * D_, o1, d + 24576 + wid * 1024);
  };
  (void)lw;
  issue(0); issue(1); issue(2);
  f32x4 acc[2][4] = {};
  for (int t = 0; t < 27; ++t) {
    if (t + 2 < 27) { WAIT_V(8); } else if (t + 1 < 27) { WAIT_V(4); } else { WAIT_V(0); }
    LBAR();
    if (t + 3 < 27) issue(t + 3);
    const char* d = sm + (t % R4) * 32768;
    const u32x4 a00 = *(const u32x4*)(d + (wm * 2) * 2048 + lane * 16), a01 = *(const u32x4*)(d + (wm * 2) * 2048 + 1024 + lane * 16);
    const u32x4 a10 = *(const u32x4*)(d + (wm * 2 + 1) * 2048 + lane * 16), a11 = *(const u32x4*)(d + (wm * 2 + 1) * 2048 + 1024 + lane * 16);
    mma_tile<true, true, 512>(a00, a10, a01, a11, tp0 + (t % R4) * 32768, tp1 + (t % R4) * 32768, 16384, acc);
  }
#pragma unroll
  for (int m = 0; m < 2; ++m)
#pragma unroll
    for (int n = 0; n < 4; ++n) {
      const size_t go = (size_t)(rb * 128 + wm * 32 + m * 16 + (lane & 15)) * D_ + cs * 128 + wn * 64 + n * 16 + (lane >> 4) * 4;
      const int col = cs * 128 + wn * 64 + n * 16 + (lane >> 4) * 4;
      u32x2 hv = *(const u32x2*)(Hb + (((size_t)(rb * 8 + wm * 2 + m) * 20 + (col >> 5)) * 64 + ((col >> 3) & 3) * 16 + (lane & 15)) * 8 + (col & 7));
      unsigned a0 = pack2(acc[m][n][0], acc[m][n][1]), a1 = pack2(acc[m][n][2], acc[m][n][3]);
      u32x2 o = {pack2(bflo(hv.x) + bflo(a0), bfhi(hv.x) + bfhi(a0)), pack2(bflo(hv.y) + bflo(a1), bfhi(hv.y) + bfhi(a1))};
      *(u32x2*)(out + go) = o;
    }
  LBAR();
}

__device__ __forceinline__ unsigned xoff(int step, unsigned o, unsigned par) {
  return (unsigned)step * 65536u + ((o >> 13) << 14) + (o & 8191u) + (((par >> (step >> 5)) & 1u) << 13);
}

__global__ void __launch_bounds__(512, 2) k_prologue(const bf16* x, const bf16* mem0, const bf16* Wv, const bf16* Wo, const bf16* Wg, const bf16* Wu, char* ws) {
  extern __shared__ __attribute__((aligned(16))) char sm[];
  const int tid = threadIdx.x, bid = blockIdx.x;
  if (bid == 0) {
    if (tid < 64) {
      const __amdgpu_buffer_rsrc_t r = __builtin_amdgcn_make_buffer_rsrc(ws + WS_XCH, 0, NC << 22, 0x00020000);
      unsigned tab = 0;
      for (int g = 0; g < 2 * NC; ++g) {
        unsigned best[2] = {~0u, ~0u};
        for (int k = 0; k < 8; ++k)
          for (int p = 0; p < 2; ++p) {
            unsigned off = (g << 21) + (k * 4 + 1) * 65536 + 3 * 16384 + p * 8192 + 6144 + 512;
            asm volatile("s_waitcnt vmcnt(0)" ::: "memory");
            unsigned long long t0 = __builtin_amdgcn_s_memtime();
            unsigned v = __builtin_amdgcn_raw_buffer_load_b32(r, off, 0, 16);
            asm volatile("s_waitcnt vmcnt(0)" ::"v"(v) : "memory");
            unsigned long long t1 = __builtin_amdgcn_s_memtime();
            unsigned d = (unsigned)(t1 - t0); if (d < best[p]) best[p] = d;
          }
        unsigned nearp = best[1] < best[0] ? 1u : 0u;
        if (xcc_id() >= 4) nearp ^= 1u;
        tab |= nearp << g;
      }
      if (tid < 16) ((unsigned*)(ws + WS_CTL))[tid] = tid == 0 ? tab : 0u;
    }
    return;
  }
  if (bid <= 16) {
    for (int i = tid; i < 4 * NC * 4096; i += 512) {
      const int step = (bid - 1) * 4 * NC + (i >> 12);
      const unsigned po = (i & 4095) * 16;
      const unsigned lo = ((po >> 14) << 13) + (po & 8191u);
      if (lo >= 30720u) continue;
      u32x4 v = {SENT, SENT, SENT, SENT};
      if (step < NC && lo < 10240u) {
        const int b = lo >> 7, row = (lo >> 4) & 7;
        v = *(const u32x4*)(mem0 + row * D_ + b * 8);
        v.x = canon(v.x); v.y = canon(v.y); v.z = canon(v.z); v.w = canon(v.w);
      }
      *(u32x4*)(ws + WS_XCH + (size_t)step * 65536 + po) = v;
    }
    return;
  }
  if (bid <= 48) {
    if (tid < 256) { const int rt = (bid - 17) * 4 + (tid >> 6); ln_tile(x + (size_t)rt * 16 * D_, (bf16*)(ws + WS_XN) + (size_t)rt * 10240, tid & 63); }
    return;
  }
  if (bid <= 75) {
    const int f0 = (bid - 49) * 64, ch = tid & 7, rg = tid >> 3;
    float* red = (float*)sm;
#pragma unroll
    for (int w = 0; w < 2; ++w) {
      const bf16* W = w ? Wu : Wg;
      float a[8] = {};
#pragma unroll
      for (int i = 0; i < 10; ++i) {
        u32x4 v = *(const u32x4*)(W + (size_t)(rg * 10 + i) * FF + f0 + ch * 8);
#pragma unroll
        for (int e = 0; e < 4; ++e) { a[2 * e] += bflo(v[e]); a[2 * e + 1] += bfhi(v[e]); }
      }
      *(f32x4*)(red + (w * 64 + rg) * 64 + ch * 8) = f32x4{a[0], a[1], a[2], a[3]};
      *(f32x4*)(red + (w * 64 + rg) * 64 + ch * 8 + 4) = f32x4{a[4], a[5], a[6], a[7]};
    }
    LBAR();
    if (tid < 128) {
      float t = 0.f;
      for (int i = 0; i < 64; ++i) t += red[((tid >> 6) * 64 + i) * 64 + (tid & 63)];
      ((float*)(ws + WS_CS))[(tid >> 6) * FF + f0 + (tid & 63)] = t;
    }
    return;
  }
  const int t = bid - 76;
  task_u(Wv, Wo, (bf16*)(ws + WS_U), t / 5, t % 5, sm, tid);
}

__global__ void __launch_bounds__(512, 2) k_proj(const bf16* Wq, const bf16* Wk, const bf16* Wv, const bf16* Wo, char* ws) {
  extern __shared__ __attribute__((aligned(16))) char sm[];
  const int tid = threadIdx.x, bid = blockIdx.x;
  const bf16* x = (const bf16*)(ws + WS_XN);
  const int rb = bid >> 5, j = bid & 31;
  if (j < 16) task_vvo<2>(x, Wv, Wo, (bf16*)(ws + WS_VO), rb, j * 2, sm, tid);
  else if (j < 24) task_kq(x, Wk, (bf16*)(ws + WS_K), rb, (j - 16) * 4, 4, sm, tid);
  else task_kq(x, Wq, (bf16*)(ws + WS_Q), rb, (j - 24) * 4, 4, sm, tid);
}

#define TS(i)
#define TSDECL
#define TSOUT(s)
#define STAG 6
#define SLA 30
#define SLM 20
#define SLR 8
#define STL 6
#define DL 1
#define SLV 6
#define VPOS 2
#define SLB 16
__device__ __forceinline__ bool frag_bad(u32x4 v) { return max(max(v.x, v.y), max(v.z, v.w)) == SENT; }
__device__ __forceinline__ void glds16c(const void* base, unsigned voff, unsigned lds) {
  asm volatile("s_mov_b32 m0, %2\n\ts_nop 0\n\tglobal_load_lds_dwordx4 %0, %1 sc1" ::"v"(voff), "s"(base), "s"(lds) : "m0");
}
__device__ __forceinline__ void glds16n(const void* base, unsigned voff, unsigned lds) {
  asm volatile("s_mov_b32 m0, %2\n\ts_nop 0\n\tglobal_load_lds_dwordx4 %0, %1 nt" ::"v"(voff), "s"(base), "s"(lds) : "m0");
}
template <int N> __device__ __forceinline__ void wait_v() { asm volatile("s_waitcnt vmcnt(%0)" ::"n"(N) : "memory"); }
template <int NL, int DEPTH, int SG, bool LOCAL = false>
__device__ __forceinline__ void poll_lds(const char* xb, const unsigned (&off)[NL], char* const (&dst)[NL], int lane, u32x4 (&v)[NL]) {
  unsigned spins = 0;
#pragma unroll
  for (int d = 0; d < DEPTH - 1; ++d) {
#pragma unroll
    for (int i = 0; i < NL; ++i) { if (LOCAL) glds16n(xb, off[i], lds_addr(dst[i])); else glds16c(xb, off[i], lds_addr(dst[i])); }
    __builtin_amdgcn_s_sleep(SG);
  }
  for (;;) {
    if (LOCAL && (spins & 15) != 15) {
#pragma unroll
      for (int i = 0; i < NL; ++i) glds16n(xb, off[i], lds_addr(dst[i]));
    } else {
#pragma unroll
      for (int i = 0; i < NL; ++i) glds16c(xb, off[i], lds_addr(dst[i]));
    }
    wait_v<NL * (DEPTH - 1)>();
    bool bad = false;
#pragma unroll
    for (int i = 0; i < NL; ++i) v[i] = *(const u32x4*)(dst[i] + lane * 16);
#pragma unroll
    for (int i = 0; i < NL; ++i) bad |= frag_bad(v[i]);
    if (!__any(bad) || ++spins > SPIN_CAP) break;
  }
}
template <int NL>
__device__ __forceinline__ void poll_lds1(const char* xb, const unsigned (&off)[NL], char* const (&dst)[NL], int lane, u32x4 (&v)[NL]) {
  unsigned spins = 0, pend = (1u << NL) - 1u;
  for (;;) {
#pragma unroll
    for (int i = 0; i < NL; ++i) if (pend & (1u << i)) glds16c(xb, off[i], lds_addr(dst[i]));
    WAIT_V(0);
#pragma unroll
    for (int i = 0; i < NL; ++i) v[i] = *(const u32x4*)(dst[i] + lane * 16);
    pend = 0;
#pragma unroll
    for (int i = 0; i < NL; ++i) if (__any(frag_bad(v[i]))) pend |= 1u << i;
    if (!pend || ++spins > SPIN_CAP) break;
  }
}
__device__ __forceinline__ unsigned dpp_ror8(unsigned v) { return __builtin_amdgcn_mov_dpp(v, 0x128, 0xf, 0xf, true); }
__device__ __forceinline__ float red_max_q(float v) {
  auto a = __builtin_amdgcn_permlane16_swap(__float_as_uint(v), __float_as_uint(v), false, false);
  v = fmaxf(__uint_as_float(a[0]), __uint_as_float(a[1]));
  auto b = __builtin_amdgcn_permlane32_swap(__float_as_uint(v), __float_as_uint(v), false, false);
  return fmaxf(__uint_as_float(b[0]), __uint_as_float(b[1]));
}
__device__ __forceinline__ float red_sum_q(float v) {
  auto a = __builtin_amdgcn_permlane16_swap(__float_as_uint(v), __float_as_uint(v), false, false);
  v = __uint_as_float(a[0]) + __uint_as_float(a[1]);
  auto b = __builtin_amdgcn_permlane32_swap(__float_as_uint(v), __float_as_uint(v), false, false);
  return __uint_as_float(b[0]) + __uint_as_float(b[1]);
}

template <class FB, class FO>
__device__ __forceinline__ void load_wfrags(FB base, FO voff, u32x8 (&w)[2][10], char* sm, int tid) {
  const int lane = tid & 63, wid = __builtin_amdgcn_readfirstlane(tid >> 6);
  const int bq = lane >> 4, q = (lane & 15) >> 2, p = lane & 3;
  const unsigned lw = lds_addr(sm) + wid * 1024;
#pragma unroll
  for (int rnd = 0; rnd < 3; ++rnd) {
    const int nf = rnd < 2 ? 4 : 2;
    for (int i = 0; i < nf * 4; ++i) {
      const int ci = (i & 3) * 512 + tid, half = (i >> 1) & 1, k = (ci >> 4) & 63;
      const int cc = half * 16 + ((ci & 15) ^ (((k & 3) << 1) | (((k >> 2) & 1) << 3)));
      glds16(base(half), voff((rnd * 4 + (i >> 2)) * 64 + k, cc), lw + (i >> 2) * 32768 + (i & 3) * 8192);
    }
    WAIT_V(0); BAR();
#pragma unroll
    for (int ff = 0; ff < 4; ++ff) {
      if (ff < nf) {
#pragma unroll
        for (int t = 0; t < 2; ++t) {
          u32x2 qd[4];
#pragma unroll
          for (int z = 0; z < 4; ++z) {
            const int k = 16 * (bq >> 1) + 4 * (bq & 1) + (z & 1) * 8 + (z >> 1) * 32 + q;
            const int cc = (2 * wid + t) * 2 + (p >> 1);
            qd[z] = tr_read(sm + ff * 32768 + ((cc >> 4) * 1024 + k * 16 + ((cc & 15) ^ (((k & 3) << 1) | (((k >> 2) & 1) << 3)))) * 16 + 8 * (p & 1));
          }
          w[t][rnd * 4 + ff] = u32x8{qd[0].x, qd[1].x, qd[0].y, qd[1].y, qd[2].x, qd[3].x, qd[2].y, qd[3].y};
        }
      }
    }
    LBAR();
  }
}

template <bool FAST>
__device__ __forceinline__ void poll_mem(const char* xb, int s, unsigned par, char* memA, char* other, int wid, int lane) {
  if (wid < 3) {
    unsigned off[2] = {xoff(s, wid * 1024 + lane * 16, par), xoff(s, (7 + wid) * 1024 + lane * 16, par)};
    char* const dst[2] = {memA + wid * 1024, memA + (7 + wid) * 1024};
    u32x4 v[2];
    if (FAST) {
      poll_lds<2, 2, STAG>(xb, off, dst, lane, v);
      *(u32x4*)(other + wid * 1024 + lane * 16) = u32x4{SENT, SENT, SENT, SENT};
      *(u32x4*)(other + (7 + wid) * 1024 + lane * 16) = u32x4{SENT, SENT, SENT, SENT};
    } else poll_lds1<2>(xb, off, dst, lane, v);
  } else if (wid < 7) {
    unsigned off[1] = {xoff(s, wid * 1024 + lane * 16, par)};
    char* const dst[1] = {memA + wid * 1024};
    u32x4 v[1];
    if (FAST) {
      poll_lds<1, 2, STAG>(xb, off, dst, lane, v);
      *(u32x4*)(other + wid * 1024 + lane * 16) = u32x4{SENT, SENT, SENT, SENT};
    } else poll_lds1<1>(xb, off, dst, lane, v);
  }
}
template <bool SPIN>
__device__ __forceinline__ f32x4 mem_times_w(const char* memA, const u32x8 (&w)[2][10], int lane) {
  const int sidx = (lane & 15) < 8 ? 0x4444 : 0xEEEE;
  u32x4 a[10];
  constexpr int WIN = SPIN ? 6 : 10;
#pragma unroll
  for (int f = 0; f < WIN; ++f) a[f] = *(const u32x4*)(memA + f * 1024 + lane * 16);
  f32x4 a0 = ZERO4, a1 = ZERO4;
#pragma unroll
  for (int f = 0; f < 10; ++f) {
    if (SPIN) {
      unsigned spins = 0;
      while (__any(frag_bad(a[f])) && ++spins < SPIN_CAP) { asm volatile("" ::: "memory"); a[f] = *(const u32x4*)(memA + f * 1024 + lane * 16); }
    }
    a0 = smf(a[f], w[0][f], a0, sidx); a1 = smf(a[f], w[1][f], a1, sidx);
    if (SPIN && f + WIN < 10) { asm volatile("" ::: "memory"); a[f + WIN] = *(const u32x4*)(memA + (f + WIN) * 1024 + lane * 16); }
  }
  f32x4 v;
#pragma unroll
  for (int r = 0; r < 4; ++r) {
    auto sw = __builtin_amdgcn_permlane32_swap(__float_as_uint(a0[r]), __float_as_uint(a1[r]), false, false);
    v[r] = __uint_as_float(sw[0]) + __uint_as_float(sw[1]);
  }
  return v;
}

__global__ void __launch_bounds__(512, 2) k_rec(const bf16* Wq, const bf16* Wk, char* ws) {
  extern __shared__ __attribute__((aligned(16))) char sm[];
  const int tid = threadIdx.x, lane = tid & 63;
  const int wid = __builtin_amdgcn_readfirstlane(tid >> 6);
  unsigned* ctl = (unsigned*)(ws + WS_CTL);
  if (tid == 0) {
    unsigned slot = 1000u;
    const unsigned xc = xcc_id();
    if (xc < 4) { const unsigned t = __hip_atomic_fetch_add(ctl + 8 + xc, 1u, __ATOMIC_RELAXED, __HIP_MEMORY_SCOPE_AGENT); if (t < 28u) slot = xc * 28 + t; }
    __hip_atomic_fetch_add(ctl + 2, 1u, __ATOMIC_RELAXED, __HIP_MEMORY_SCOPE_AGENT);
    if (slot >= 112u) {
      unsigned spins = 0;
      while (__hip_atomic_load(ctl + 2, __ATOMIC_RELAXED, __HIP_MEMORY_SCOPE_AGENT) < gridDim.x && ++spins < SPIN_CAP) __builtin_amdgcn_s_sleep(2);
      unsigned c[4], miss = 0;
      for (int x = 0; x < 4; ++x) { c[x] = min(__hip_atomic_load(ctl + 8 + x, __ATOMIC_RELAXED, __HIP_MEMORY_SCOPE_AGENT), 28u); miss += 28u - c[x]; }
      if (miss) {
        unsigned j = __hip_atomic_fetch_add(ctl + 3, 1u, __ATOMIC_RELAXED, __HIP_MEMORY_SCOPE_AGENT);
        for (int x = 0; x < 4; ++x) { if (slot >= 112u && j < 28u - c[x]) slot = x * 28 + c[x] + j; else j -= 28u - c[x]; }
      }
    }
    *(unsigned*)sm = slot;
  }
  __syncthreads();
  const int slot = __builtin_amdgcn_readfirstlane(*(unsigned*)sm);
  __syncthreads();
  if (slot >= 112) return;
  const int grp = slot / 28, tsl = slot % 28;
  const int role = tsl < 8 ? grp * 8 + tsl : 32 + grp * 20 + (tsl - 8);
  const unsigned par = ctl[0];
  const int cpy = role % NC;
  const __amdgpu_buffer_rsrc_t xr = __builtin_amdgcn_make_buffer_rsrc(ws + WS_XCH, 0, NC << 22, 0x00020000);
  const int fr = lane & 15, fq = lane >> 4;
  u32x8 w[2][10];
  char* memA = sm;
  if (role < 32) {
    const int h = role;
    load_wfrags([&](int half) { return (half ? Wk : Wq) + h * DH; }, [&](int k, int cc) { return (unsigned)(k * HD + (cc & 15) * 8) * 2u; }, w, sm, tid);
    char* qk = sm + 10240;
    char* kring = sm + 16384;
    char* memB = sm + 40960;
    const bf16* K = (const bf16*)(ws + WS_K) + (size_t)h * 4096;
    bf16* KM = (bf16*)(ws + WS_KM);
    const float cs = 0.08838834764831845f * 1.4426950408889634f;
    auto kdma = [&](int s) {
      const bf16* src = K + (size_t)s * NH * 4096;
      const unsigned d = lds_addr(kring) + (s % 3) * 8192;
#pragma unroll
      for (int i = 0; i < 8; ++i) glds16(src + i * 512, lane * 16, d + i * 1024);
    };
    if (wid == 7) { kdma(0); kdma(1); WAIT_V(8); }
    for (int i = tid; i < 640; i += 512) *(u32x4*)(memA + i * 16) = u32x4{SENT, SENT, SENT, SENT};
    LBAR();
    u32x4 kf[2][4];
    if (wid == 0) {
#pragma unroll
      for (int t = 0; t < 4; ++t) { kf[0][t] = *(const u32x4*)(kring + lane * 16 + t * 1024); kf[1][t] = *(const u32x4*)(kring + 4096 + lane * 16 + t * 1024); }
    }
    const int Ost = fr < 8 ? 5 * h + 1 + fq : 5 * h;
    const unsigned pst = 10240 + (Ost >> 3) * 1024 + ((Ost & 3) * 16 + ((Ost >> 2) & 1) * 8 + (fr & 7)) * 16;
    for (int s = 0; s < NSEG; ++s) {
      TSDECL; TS(0);
      char* mcur = (s & 1) ? memB : memA;
      poll_mem<true>(ws + WS_XCH, s, par, mcur, (s & 1) ? memA : memB, wid, lane);
      if (wid == 7) { WAIT_V(0); }
      TS(1);
      LBAR();
      TS(2);
      { f32x4 v = mem_times_w<false>(mcur, w, lane);
        const int col = (2 * wid + (lane >> 5)) * 16 + fr, e = col & 127;
        char* dst = qk + (col >> 7) * 2176 + (fq & 1) * 4 * 272 + (e >> 5) * 64 + ((e >> 2) & 3) * 16 + ((e >> 4) & 1) * 8 + (e & 3) * 2;
#pragma unroll
        for (int r = 0; r < 4; ++r) *(unsigned short*)(dst + r * 272) = (unsigned short)(pack2(v[r], v[r]) & 0xffffu); }
      TS(3);
      LBAR();
      TS(4);
      if (wid == 0) {
        if (s < NSEG - 1) {
          u32x4 qf[4], km[4];
#pragma unroll
          for (int t = 0; t < 4; ++t) {
            qf[t] = *(const u32x4*)(qk + (fr & 7) * 272 + t * 64 + fq * 16);
            km[t] = *(const u32x4*)(qk + 2176 + (fr & 7) * 272 + t * 64 + fq * 16);
          }
          asm volatile("" : "+v"(qf[0]), "+v"(qf[1]), "+v"(qf[2]), "+v"(qf[3]), "+v"(km[0]), "+v"(km[1]), "+v"(km[2]), "+v"(km[3]));
          f32x4 sc[3] = {};
#pragma unroll
          for (int t = 0; t < 4; ++t) { sc[1] = mfma16(kf[0][t], qf[t], sc[1]); sc[2] = mfma16(kf[1][t], qf[t], sc[2]); sc[0] = mfma16(km[t], qf[t], sc[0]); }
          const bool hi = fr >= 8;
          float e[6];
#pragma unroll
          for (int r = 0; r < 4; ++r) e[r] = hi ? sc[2][r] : sc[1][r];
          e[4] = hi ? sc[0][2] : sc[0][0]; e[5] = hi ? sc[0][3] : sc[0][1];
          if (fq >= 2) { e[4] = -INFINITY; e[5] = -INFINITY; }
          float mx = fmaxf(fmaxf(fmaxf(e[0], e[1]), fmaxf(e[2], e[3])), fmaxf(e[4], e[5]));
          mx = fmaxf(mx, __uint_as_float(dpp_ror8(__float_as_uint(mx))));
          mx = red_max_q(mx) * cs;
          float sum = 0.f;
#pragma unroll
          for (int r = 0; r < 6; ++r) { e[r] = __builtin_amdgcn_exp2f(__builtin_fmaf(e[r], cs, -mx)); sum += e[r]; }
          sum += __uint_as_float(dpp_ror8(__float_as_uint(sum)));
          sum = red_sum_q(sum);
          const float inv = __builtin_amdgcn_rcpf(sum);
          const unsigned p0 = canon(pack2(e[0] * inv, e[1] * inv)), p1 = canon(pack2(e[2] * inv, e[3] * inv)), pm = canon(pack2(e[4] * inv, e[5] * inv));
          const unsigned q0 = dpp_ror8(p0), q1 = dpp_ror8(p1), qm = dpp_ror8(pm);
          auto s0 = __builtin_amdgcn_permlane16_swap(qm, qm, false, false);
          auto s1 = __builtin_amdgcn_permlane16_swap(pm, pm, false, false);
          const u32x4 o = hi ? u32x4{qm, pm, s0[1], s1[1]} : u32x4{p0, p1, q0, q1};
          if (fr < 8 || fq == 0) __builtin_amdgcn_raw_buffer_store_b128(o, xr, xoff(s, pst, par), 0, 16);
          const char* kt = kring + ((s + 1) % 3) * 8192 + lane * 16;
#pragma unroll
          for (int t = 0; t < 4; ++t) { kf[0][t] = *(const u32x4*)(kt + t * 1024); kf[1][t] = *(const u32x4*)(kt + 4096 + t * 1024); }
        }
      } else if (wid == 7) {
#pragma unroll
        for (int j = 0; j < 2; ++j) {
          const int i = j * 64 + lane;
          u32x4 v = *(const u32x4*)(qk + 2176 + (i & 7) * 272 + (i >> 3) * 16);
          *(u32x4*)(KM + (size_t)(s * NH + h) * 1024 + i * 8) = v;
        }
        if (s + 2 < NSEG) kdma(s + 2);
      }
      TS(5);
      TSOUT(s);
      if (wid < 7) __builtin_amdgcn_s_sleep(SLA);
    }
    WAIT_V(0);
  } else {
    const int b = role - 32;
    const bf16* U = (const bf16*)(ws + WS_U) + (size_t)b * NH * D_ * 8;
    load_wfrags([&](int half) { return U; }, [&](int k, int cc) { return (unsigned)(cc * D_ + k) * 16u; }, w, sm, tid);
    float* red = (float*)(sm + 10240);
    char* vring = sm + 16384;
    char* pbuf = sm + 16384 + 61440;
    bf16* VO = (bf16*)(ws + WS_VO) + (size_t)b * 20 * 512;
    const int ng = wid < 6 ? 3 : 2;
    auto vdma = [&](int s) {
      const bf16* src = VO + (size_t)s * 80 * 20 * 512;
      const unsigned d = lds_addr(vring) + (s % 3) * 20480;
#pragma unroll
      for (int i = 0; i < 20; ++i) glds16(src + i * 512, lane * 16, d + i * 1024);
    };
    if (wid == 7) { vdma(0); vdma(1); }
    if (tid < 128) red[(tid >> 1) * 20 + 14 + (tid & 1)] = 0.f;
    for (int s = 0; s < NSEG; ++s) {
      char* vt = vring + (s % 3) * 20480;
      TSDECL; TS(0);
      if (SLM) __builtin_amdgcn_s_sleep(SLM);
      poll_mem<false>(ws + WS_XCH, s, par, memA, memA, wid, lane);
      if (wid == 7) {
        if (VPOS == 2) { __builtin_amdgcn_s_sleep(SLV); vdma(s + 2 < NSEG ? s + 2 : s + 2 - 3); wait_v<40>(); } else { WAIT_V(20); }
      }
      TS(1);
      LBAR();
      TS(2);
      { f32x4 v = mem_times_w<false>(memA, w, lane);
        const int O = 5 * ((2 * wid + (lane >> 5)) * 2 + (fr >> 3));
        *(u32x2*)(vt + (O >> 3) * 1024 + ((O & 3) * 16 + ((O >> 2) & 1) * 8 + (fr & 7)) * 16 + (fq & 1) * 8) = u32x2{pack2(v[0], v[1]), pack2(v[2], v[3])}; }
      TS(3);
      LBAR();
      TS(4);
      if (wid == 7) {
#pragma unroll
        for (int j = 0; j < 4; ++j) {
          const int i = j * 64 + lane, O = 5 * (i >> 3);
          const int off = (O >> 3) * 1024 + ((O & 3) * 16 + ((O >> 2) & 1) * 8 + (i & 7)) * 16;
          *(u32x4*)((char*)(VO + (size_t)s * 80 * 20 * 512) + off) = *(const u32x4*)(vt + off);
        }
        if (VPOS == 0) { WAIT_L0(); vdma(s + 2 < NSEG ? s + 2 : s + 2 - 3); }
      } else if (s < NSEG - 1) {
        unsigned off[3]; u32x4 vf[3];
#pragma unroll
        for (int i = 0; i < 3; ++i) {
          const int g = i < ng ? wid + 7 * i : wid;
          off[i] = xoff(s, 10240 + g * 1024 + lane * 16, par);
          vf[i] = *(const u32x4*)(vt + g * 1024 + lane * 16);
        }
        __builtin_amdgcn_s_sleep(SLB);
        char* const dst[3] = {pbuf + wid * 3072, pbuf + wid * 3072 + 1024, pbuf + wid * 3072 + 2048};
        u32x4 pv[3];
        if (DL == 1) poll_lds1<3>(ws + WS_XCH, off, dst, lane, pv); else poll_lds<3, 2, STL>(ws + WS_XCH, off, dst, lane, pv);
        TS(5);
        f32x4 acc = ZERO4;
#pragma unroll
        for (int i = 0; i < 3; ++i) if (i < ng) acc = mfma16(pv[i], vf[i], acc);
        if ((fr >> 3) == (fq >> 1)) {
#pragma unroll
          for (int r = 0; r < 4; ++r) red[(((fq & 1) * 4 + r) * 8 + (fr & 7)) * 20 + wid * 2 + (fr >> 3)] = acc[r];
        }
      }
      if (s < NSEG - 1) {
        LBAR();
        TS(6);
        if (VPOS == 1 && wid == 7) { __builtin_amdgcn_s_sleep(SLV); vdma(s + 2 < NSEG ? s + 2 : s + 2 - 3); }
        if (wid == 0) {
          const f32x4* rp = (const f32x4*)(red + lane * 20);
          f32x4 t0 = rp[0] + rp[1], t1 = rp[2] + rp[3]; t0 += t1;
          float v = (t0[0] + t0[1]) + (t0[2] + t0[3]);
          const float nb = __uint_as_float(__builtin_amdgcn_mov_dpp(__float_as_uint(v), 0xB1, 0xf, 0xf, true));
          u32x4 o; o.x = canon(pack2(v, nb));
          o.y = __builtin_amdgcn_mov_dpp(o.x, 0x4E, 0xf, 0xf, true);
          o.z = __builtin_amdgcn_mov_dpp(o.x, 0x104, 0xf, 0xf, true);
          o.w = __builtin_amdgcn_mov_dpp(o.y, 0x104, 0xf, 0xf, true);
          if (!(lane & 7)) {
#pragma unroll
            for (int c = 0; c < NC; ++c) __builtin_amdgcn_raw_buffer_store_b128(o, xr, xoff((s + 1) * NC + c, b * 128 + lane * 2, par), 0, 16);
          }
        }
      }
      TS(7); TSOUT(s);
    }
    WAIT_V(0);
  }
}

__global__ void __launch_bounds__(512) k_t1(char* ws) {
  const int w = blockIdx.x * 8 + (threadIdx.x >> 6);
  wave_t1((const bf16*)(ws + WS_Q), (const bf16*)(ws + WS_K), (const bf16*)(ws + WS_KM), (bf16*)(ws + WS_P), w >> 5, w & 31, threadIdx.x & 63);
}
__global__ void __launch_bounds__(512) k_t2(const bf16* x, char* ws) {
  extern __shared__ __attribute__((aligned(16))) char sm[];
  task_t2(x, (const bf16*)(ws + WS_P), (const bf16*)(ws + WS_VO), (bf16*)(ws + WS_H), (float*)(ws + WS_ST), blockIdx.x / 10, blockIdx.x % 10, sm, threadIdx.x);
}
__global__ void __launch_bounds__(512, 2) k_f1(const bf16* Wg, const bf16* Wu, char* ws) {
  extern __shared__ __attribute__((aligned(16))) char sm[];
  const int rb = blockIdx.x / 14, j = blockIdx.x % 14;
  task_f1((const bf16*)(ws + WS_H), (const float*)(ws + WS_ST), (const float*)(ws + WS_CS), Wg, Wu, (bf16*)(ws + WS_G), rb, j * 2, j < 13 ? 2 : 1, sm, threadIdx.x);
}
__global__ void __launch_bounds__(512, 2) k_f2(const bf16* Wd, char* ws, bf16* out) {
  extern __shared__ __attribute__((aligned(16))) char sm[];
  task_f2((const bf16*)(ws + WS_G), Wd, (const bf16*)(ws + WS_H), out, blockIdx.x / 5, blockIdx.x % 5, sm, threadIdx.x);
}

extern "C" void kernel_launch(void* const* d_in, const int* in_sizes, int n_in, void* d_out, int out_size, void* d_ws, size_t ws_size, hipStream_t stream) {
  const bf16 *x = (const bf16*)d_in[0], *mem0 = (const bf16*)d_in[1], *Wq = (const bf16*)d_in[2], *Wk = (const bf16*)d_in[3], *Wv = (const bf16*)d_in[4], *Wo = (const bf16*)d_in[5];
  const bf16 *Wg = (const bf16*)d_in[8], *Wu = (const bf16*)d_in[9], *Wd = (const bf16*)d_in[10];
  char* ws = (char*)(((size_t)d_ws + (2 * MB - 1)) & ~(size_t)(2 * MB - 1));
  (void)hipFuncSetAttribute((const void*)k_prologue, hipFuncAttributeMaxDynamicSharedMemorySize, 131072);
  (void)hipFuncSetAttribute((const void*)k_proj, hipFuncAttributeMaxDynamicSharedMemorySize, 147456);
  (void)hipFuncSetAttribute((const void*)k_rec, hipFuncAttributeMaxDynamicSharedMemorySize, 131072);
  (void)hipFuncSetAttribute((const void*)k_t2, hipFuncAttributeMaxDynamicSharedMemorySize, 69632);
  (void)hipFuncSetAttribute((const void*)k_f1, hipFuncAttributeMaxDynamicSharedMemorySize, 131072);
  (void)hipFuncSetAttribute((const void*)k_f2, hipFuncAttributeMaxDynamicSharedMemorySize, 131072);
  k_prologue<<<76 + 160, 512, 131072, stream>>>(x, mem0, Wv, Wo, Wg, Wu, ws);
  k_proj<<<512, 512, 147456, stream>>>(Wq, Wk, Wv, Wo, ws);
  k_rec<<<256, 512, 131072, stream>>>(Wq, Wk, ws);
  k_t1<<<256, 512, 0, stream>>>(ws);
  k_t2<<<640, 512, 69632, stream>>>(x, ws);
  k_f1<<<224, 512, 131072, stream>>>(Wg, Wu, ws);
  k_f2<<<80, 512, 131072, stream>>>(Wd, ws, (bf16*)d_out);
}
```

```cpp
#define NO_KM
#include <hip/hip_runtime.h>
#include <hip/hip_bf16.h>

typedef __hip_bfloat16 bf16;
typedef __bf16 bf16x8 __attribute__((ext_vector_type(8)));
typedef __bf16 bf16x16 __attribute__((ext_vector_type(16)));
typedef float f32x4 __attribute__((ext_vector_type(4)));
typedef unsigned u32x4 __attribute__((ext_vector_type(4)));
typedef unsigned u32x2 __attribute__((ext_vector_type(2)));
typedef unsigned u32x8 __attribute__((ext_vector_type(8)));
typedef short s16x4 __attribute__((ext_vector_type(4)));
typedef __attribute__((address_space(3))) s16x4* lds_s16x4_p;

constexpr int S_ = 2048, D_ = 640, NH = 32, DH = 128, HD = 4096, FF = 1728, NSEG = 64;
constexpr size_t MB = 1u << 20;
#define NC 1
constexpr size_t WS_XCH = 0;
constexpr size_t WS_LP = 4 * MB;
constexpr size_t WS_CTL = 16 * MB;
constexpr size_t WS_Q = 17 * MB;
constexpr size_t WS_K = WS_Q + 16 * MB;
constexpr size_t WS_U = WS_K + 16 * MB;
constexpr size_t WS_VO = WS_U + 25 * MB;
constexpr size_t WS_KM = WS_VO + 100 * MB;
constexpr size_t WS_P = WS_KM + 4 * MB;
constexpr size_t WS_H = WS_P + 5 * MB;
constexpr size_t WS_G = WS_H + 3 * MB;
constexpr size_t WS_XN = WS_G + 7 * MB;
constexpr size_t WS_ST = WS_XN + 3 * MB;
constexpr size_t WS_CS = WS_ST + 1 * MB;
constexpr size_t WS_END = WS_CS + 1 * MB;

#define SLD 8
constexpr unsigned C_QH = WS_CTL + 256 * 1, C_PROG = WS_CTL + 256 * 6, C_FIN = WS_CTL + 256 * 7, C_K = WS_CTL + 256 * 8, C_V = WS_CTL + 256 * 24,
                   C_Q = WS_CTL + 256 * 40, C_T2 = WS_CTL + 256 * 56, C_F1 = WS_CTL + 256 * 72, C_T1 = WS_CTL + 256 * 88,
                   C_T2S = WS_CTL + 256 * (152 - 48), C_F1S = WS_CTL + 256 * (168 - 48), C_SEG = WS_CTL + 256 * (184 - 48), C_END = WS_CTL + 256 * 200;
constexpr unsigned SENT = 0xFFFFFFFFu;
constexpr unsigned SPIN_CAP = 1u << 18;

__device__ __forceinline__ unsigned pack2(float lo, float hi) {
  typedef __bf16 b2 __attribute__((ext_vector_type(2)));
  typedef float f2 __attribute__((ext_vector_type(2)));
  return __builtin_bit_cast(unsigned, __builtin_convertvector(f2{lo, hi}, b2));
}
__device__ __forceinline__ u32x4 pack8(f32x4 a, f32x4 b) { return u32x4{pack2(a[0], a[1]), pack2(a[2], a[3]), pack2(b[0], b[1]), pack2(b[2], b[3])}; }
__device__ __forceinline__ float bflo(unsigned w) { return __uint_as_float(w << 16); }
__device__ __forceinline__ float bfhi(unsigned w) { return __uint_as_float(w & 0xffff0000u); }
__device__ __forceinline__ unsigned canon(unsigned w) { return min(w, 0xFFFFFFFEu); }
__device__ __forceinline__ void glds16(const void* base, unsigned voff, unsigned lds) {
  asm volatile("s_mov_b32 m0, %2\n\ts_nop 0\n\tglobal_load_lds_dwordx4 %0, %1" ::"v"(voff), "s"(base), "s"(__builtin_amdgcn_readfirstlane(lds)) : "m0");
}
__device__ __forceinline__ void glds16c(const void* base, unsigned voff, unsigned lds) {
  asm volatile("s_mov_b32 m0, %2\n\ts_nop 0\n\tglobal_load_lds_dwordx4 %0, %1 sc1" ::"v"(voff), "s"(base), "s"(__builtin_amdgcn_readfirstlane(lds)) : "m0");
}
typedef __amdgpu_buffer_rsrc_t rsrc_t;
__device__ __forceinline__ u32x4 ldc(rsrc_t r, unsigned base, unsigned off) { return __builtin_amdgcn_raw_buffer_load_b128(r, off, base, 16); }
__device__ __forceinline__ u32x2 ldc2(rsrc_t r, unsigned base, unsigned off) { return __builtin_amdgcn_raw_buffer_load_b64(r, off, base, 16); }
__device__ __forceinline__ void stc(rsrc_t r, unsigned base, unsigned off, u32x4 v) { __builtin_amdgcn_raw_buffer_store_b128(v, r, off, base, 16); }
__device__ __forceinline__ void stc2(rsrc_t r, unsigned base, unsigned off, u32x2 v) { __builtin_amdgcn_raw_buffer_store_b64(v, r, off, base, 16); }
template <int N> __device__ __forceinline__ void wait_v() { asm volatile("s_waitcnt vmcnt(%0)" ::"n"(N) : "memory"); }
__device__ __forceinline__ unsigned lds_addr(const void* p) { return (unsigned)(size_t)(const __attribute__((address_space(3))) char*)p; }
__device__ __forceinline__ u32x2 tr_read(const char* p) {
  s16x4 r = __builtin_amdgcn_ds_read_tr16_b64_v4i16((lds_s16x4_p)(p));
  return __builtin_bit_cast(u32x2, r);
}
#define WAIT_V(n) asm volatile("s_waitcnt vmcnt(" #n ")" ::: "memory")
#define WAIT_L0() asm volatile("s_waitcnt lgkmcnt(0)" ::: "memory")
#define BAR() asm volatile("s_barrier" ::: "memory")
#define LBAR() asm volatile("s_waitcnt lgkmcnt(0)\n\ts_barrier" ::: "memory")
__device__ __forceinline__ f32x4 mfma16(u32x4 a, u32x4 b, f32x4 c) {
  return __builtin_amdgcn_mfma_f32_16x16x32_bf16(__builtin_bit_cast(bf16x8, a), __builtin_bit_cast(bf16x8, b), c, 0, 0, 0);
}
__device__ __forceinline__ f32x4 smf(u32x4 a, u32x8 b, f32x4 c, int idx) {
  return __builtin_amdgcn_smfmac_f32_16x16x64_bf16(__builtin_bit_cast(bf16x8, a), __builtin_bit_cast(bf16x16, b), c, idx, 0, 0);
}
__device__ __forceinline__ unsigned xcc_id() { return (unsigned)__builtin_amdgcn_s_getreg(20 | (3 << 11)) & 0xfu; }

__device__ __forceinline__ unsigned dpp_ror8(unsigned v) { return __builtin_amdgcn_mov_dpp(v, 0x128, 0xf, 0xf, true); }
__device__ __forceinline__ float red_max_q(float v) {
  auto a = __builtin_amdgcn_permlane16_swap(__float_as_uint(v), __float_as_uint(v), false, false);
  v = fmaxf(__uint_as_float(a[0]), __uint_as_float(a[1]));
  auto b = __builtin_amdgcn_permlane32_swap(__float_as_uint(v), __float_as_uint(v), false, false);
  return fmaxf(__uint_as_float(b[0]), __uint_as_float(b[1]));
}
__device__ __forceinline__ float red_sum_q(float v) {
  auto a = __builtin_amdgcn_permlane16_swap(__float_as_uint(v), __float_as_uint(v), false, false);
  v = __uint_as_float(a[0]) + __uint_as_float(a[1]);
  auto b = __builtin_amdgcn_permlane32_swap(__float_as_uint(v), __float_as_uint(v), false, false);
  return __uint_as_float(b[0]) + __uint_as_float(b[1]);
}

__device__ __forceinline__ float red_sum8(float v) {
  v += __uint_as_float(__builtin_amdgcn_mov_dpp(__float_as_uint(v), 0xB1, 0xf, 0xf, true));
  v += __uint_as_float(__builtin_amdgcn_mov_dpp(__float_as_uint(v), 0x4E, 0xf, 0xf, true));
  return v + __uint_as_float(__builtin_amdgcn_mov_dpp(__float_as_uint(v), 0x141, 0xf, 0xf, true));
}
__device__ __forceinline__ int bswz(int row) { return (((row >> 2) ^ (row >> 3)) & 1) << 1; }
__device__ __forceinline__ void stageB_rc(int p, int& row, int& ch) {
  row = ((p >> 7) << 3) | ((p >> 2) & 7);
  ch = (((p >> 5) & 3) << 2) | ((p & 3) ^ bswz(row));
}
template <bool PERM>
__device__ __forceinline__ unsigned trB_base(int lane, int j) {
  const int g = lane >> 4, q = (lane & 15) >> 2, p = lane & 3;
  return PERM ? 2048 * (g >> 1) + 64 * (4 * (g & 1) + q) + 8 * p + 32 * ((j ^ g ^ (g >> 1)) & 1) : 2048 * g + 64 * q + 8 * p + 32 * ((j ^ g) & 1);
}
__device__ __forceinline__ u32x2 tr_read_a(unsigned a) {
  s16x4 r = __builtin_amdgcn_ds_read_tr16_b64_v4i16((lds_s16x4_p)(size_t)a);
  return __builtin_bit_cast(u32x2, r);
}

template <bool SW, bool PERM, int CSTR>
__device__ __forceinline__ void mma_tile(const u32x4& a00, const u32x4& a10, const u32x4& a01, const u32x4& a11, unsigned tb0, unsigned tb1, int so, f32x4 (&acc)[2][4]) {
#pragma unroll
  for (int ks = 0; ks < 2; ++ks) {
    u32x4 b[4];
#pragma unroll
    for (int n = 0; n < 4; ++n) {
      const int o = so + 8192 * ks + CSTR * (n >> 1);
      u32x2 lo = tr_read_a(((n & 1) ? tb1 : tb0) + o), hi = tr_read_a((PERM ? ((n & 1) ? tb1 : tb0) + 4096 : ((n & 1) ? tb0 : tb1) + 256) + o);
      b[n] = u32x4{lo.x, lo.y, hi.x, hi.y};
    }
    const u32x4 a0 = ks ? a01 : a00, a1 = ks ? a11 : a10;
#pragma unroll
    for (int n = 0; n < 4; ++n) {
      acc[0][n] = SW ? mfma16(b[n], a0, acc[0][n]) : mfma16(a0, b[n], acc[0][n]);
      acc[1][n] = SW ? mfma16(b[n], a1, acc[1][n]) : mfma16(a1, b[n], acc[1][n]);
    }
  }
}

__device__ __forceinline__ void ln_tile(const bf16* src  , bf16* dst, int lane) {
  const int fr = lane & 15, fq = lane >> 4;
  u32x4 af[20];
#pragma unroll
  for (int s = 0; s < 20; ++s) af[s] = *(const u32x4*)(src + (size_t)fr * D_ + s * 32 + fq * 8);
  float s = 0.f;
#pragma unroll
  for (int i = 0; i < 20; ++i)
#pragma unroll
    for (int e = 0; e < 4; ++e) s += bflo(af[i][e]) + bfhi(af[i][e]);
  s += __shfl_xor(s, 16); s += __shfl_xor(s, 32);
  const float mean = s * (1.f / 640.f);
  float q = 0.f;
#pragma unroll
  for (int i = 0; i < 20; ++i)
#pragma unroll
    for (int e = 0; e < 4; ++e) { float a = bflo(af[i][e]) - mean, b = bfhi(af[i][e]) - mean; q += a * a + b * b; }
  q += __shfl_xor(q, 16); q += __shfl_xor(q, 32);
  const float rstd = 1.f / sqrtf(q * (1.f / 640.f) + 1e-5f);
#pragma unroll
  for (int i = 0; i < 20; ++i) {
    u32x4 o;
#pragma unroll
    for (int e = 0; e < 4; ++e) o[e] = pack2((bflo(af[i][e]) - mean) * rstd, (bfhi(af[i][e]) - mean) * rstd);
    *(u32x4*)(dst + i * 512 + lane * 8) = o;
  }
}
__device__ __forceinline__ void load_af(const bf16* src, u32x4 (&af)[2][20], int lane) {
#pragma unroll
  for (int s = 0; s < 20; ++s)
#pragma unroll
    for (int m = 0; m < 2; ++m) af[m][s] = *(const u32x4*)(src + (m * 20 + s) * 512 + lane * 8);
}

__device__ __forceinline__ void load_afc(rsrc_t r, unsigned off, u32x4 (&af)[2][20], int lane) {
#pragma unroll
  for (int s = 0; s < 20; ++s)
#pragma unroll
    for (int m = 0; m < 2; ++m) af[m][s] = ldc(r, off, (m * 20 + s) * 1024 + lane * 16);
}
__device__ __forceinline__ void dep_poll(rsrc_t r, unsigned off, unsigned target) {
  const unsigned t0 = (unsigned)__builtin_amdgcn_s_memrealtime();
  while ((unsigned)__builtin_amdgcn_readfirstlane(__builtin_amdgcn_raw_buffer_load_b32(r, 0, off, 16)) < target) {
    if ((unsigned)__builtin_amdgcn_s_memrealtime() - t0 > 5000000u) break;
    __builtin_amdgcn_s_sleep(SLD);
  }
}
struct Dep { unsigned o0, t0, o1, t1; };
__device__ __forceinline__ void dep_wait(rsrc_t r, const Dep& d, int wid) {
  if (wid == 0) { dep_poll(r, d.o0, d.t0); if (d.t1) dep_poll(r, d.o1, d.t1); }
  BAR();
}
__device__ __forceinline__ bool dep_first(rsrc_t r, const Dep& d, int wid, unsigned* bc) {
  if (wid == 0) {
    const unsigned a = __builtin_amdgcn_raw_buffer_load_b32(r, 0, d.o0, 16), b = __builtin_amdgcn_raw_buffer_load_b32(r, 0, d.o1, 16);
    if ((unsigned)__builtin_amdgcn_readfirstlane(a) < d.t0) dep_poll(r, d.o0, d.t0);
    bc[1] = b;
  }
  LBAR();
  return (unsigned)__builtin_amdgcn_readfirstlane(bc[1]) >= d.t1;
}
__device__ __forceinline__ void dep_second(rsrc_t r, const Dep& d, int wid) {
  if (wid == 0) dep_poll(r, d.o1, d.t1);
  BAR();
}
constexpr int RING = 5;
#define ZERO4 f32x4{0.f, 0.f, 0.f, 0.f}
__device__ __forceinline__ void ring_wait(int t, int T) {
  const int rem = T - 1 - t;
  if (rem >= 3) { WAIT_V(6); } else if (rem == 2) { WAIT_V(4); } else if (rem == 1) { WAIT_V(2); } else { WAIT_V(0); }
  LBAR();
}

__device__ __forceinline__ void task_kq(const bf16* x, const bf16* W, rsrc_t wr, unsigned out, int rb, int h0, int nh, char* sm, int tid) {
  const int lane = tid & 63, wid = __builtin_amdgcn_readfirstlane(tid >> 6), wm = wid >> 1, wn = wid & 1;
  const unsigned lw = lds_addr(sm) + wid * 1024;
  const unsigned tb0 = lds_addr(sm) + wn * 1024 + trB_base<false>(lane, 0), tb1 = lds_addr(sm) + wn * 1024 + trB_base<false>(lane, 1);
  int r0, c0, r1, c1; stageB_rc(tid, r0, c0); stageB_rc(tid + 512, r1, c1);
  const unsigned o0 = (r0 * HD + c0 * 8) * 2, o1 = (r1 * HD + c1 * 8) * 2;
  const int T = nh * 10;
  auto issue = [&](int hh, int u) {
    const bf16* b = W + (size_t)(u * 64) * HD + (h0 + hh) * DH;
    const unsigned d = lw + (u % RING) * 16384;
    glds16(b, o0, d); glds16(b, o1, d + 8192);
  };
#pragma unroll
  for (int u = 0; u < RING - 1; ++u) issue(0, u);
  u32x4 af[2][20];
  load_af(x + (size_t)(rb * 8 + wm * 2) * 10240, af, lane);
  for (int hh = 0; hh < nh; ++hh) {
    f32x4 acc[2][4] = {};
#pragma unroll
    for (int u = 0; u < 10; ++u) {
      if (u < 4) { WAIT_V(10); LBAR(); } else ring_wait(hh * 10 + u, T);
      if (u + RING - 1 < 10) issue(hh, u + RING - 1); else if (hh + 1 < nh) issue(hh + 1, u + RING - 1 - 10);
      mma_tile<true, false, 512>(af[0][2 * u], af[1][2 * u], af[0][2 * u + 1], af[1][2 * u + 1], tb0, tb1, (u % RING) * 16384, acc);
    }
    const unsigned o = out + (((rb * 4 + wm) * NH + h0 + hh) * 8 + 2 * wn) * 1024;
#pragma unroll
    for (int m = 0; m < 2; ++m)
#pragma unroll
      for (int t = 0; t < 2; ++t) stc(wr, o, (m * 4 + t) * 1024 + lane * 16, pack8(acc[m][2 * t], acc[m][2 * t + 1]));
  }
}

constexpr int vvo_allow(int u, int T) {
  int n = 0;
  for (int j = u + 1; j <= u + 3 && j < T; ++j) n += 2;
  for (int j = u - 4; j < u; ++j) if (j >= T / 2 && (j & 1)) n += 4;
  return n;
}
template <int NHH>
__device__ __forceinline__ void task_vvo(const bf16* x, const bf16* Wv, const bf16* Wo, rsrc_t wr, int rb, int h0, char* sm, int tid) {
  const int lane = tid & 63, wid = __builtin_amdgcn_readfirstlane(tid >> 6), wm = wid >> 1, wn = wid & 1;
  const unsigned lw = lds_addr(sm) + wid * 1024;
  const unsigned tb0 = lds_addr(sm) + wn * 1024 + trB_base<false>(lane, 0), tb1 = lds_addr(sm) + wn * 1024 + trB_base<false>(lane, 1);
  const unsigned tp0 = lds_addr(sm) + wn * 1024 + trB_base<true>(lane, 0), tp1 = lds_addr(sm) + wn * 1024 + trB_base<true>(lane, 1);
  char* vbuf = sm + RING * 16384;
  int r0, c0, r1, c1; stageB_rc(tid, r0, c0); stageB_rc(tid + 512, r1, c1);
  const unsigned o0 = (r0 * HD + c0 * 8) * 2, o1 = (r1 * HD + c1 * 8) * 2, p0 = (r0 * D_ + c0 * 8) * 2, p1 = (r1 * D_ + c1 * 8) * 2;
  constexpr int T = NHH * 20;
  auto issue = [&](int t) {
    const unsigned d = lw + (t % RING) * 16384;
    if (t < NHH * 10) {
      const bf16* b = Wv + (size_t)((t % 10) * 64) * HD + (h0 + t / 10) * DH;
      glds16(b, o0, d); glds16(b, o1, d + 8192);
    } else {
      const int v = (t - NHH * 10) % 10, h = h0 + (t - NHH * 10) / 10;
      const bf16* b = Wo + (size_t)(h * DH + (v & 1) * 64) * D_ + (v >> 1) * 128;
      glds16(b, p0, d); glds16(b, p1, d + 8192);
    }
  };
#pragma unroll
  for (int u = 0; u < RING - 1; ++u) issue(u);
  {
    u32x4 af[2][20];
    load_af(x + (size_t)(rb * 8 + wm * 2) * 10240, af, lane);
#pragma unroll
    for (int hh = 0; hh < NHH; ++hh) {
      f32x4 acc[2][4] = {};
#pragma unroll
      for (int u = 0; u < 10; ++u) {
        ring_wait(hh * 10 + u, T);
        issue(hh * 10 + u + RING - 1);
        mma_tile<true, false, 512>(af[0][2 * u], af[1][2 * u], af[0][2 * u + 1], af[1][2 * u + 1], tb0, tb1, (u % RING) * 16384, acc);
      }
#pragma unroll
      for (int m = 0; m < 2; ++m)
#pragma unroll
        for (int t = 0; t < 2; ++t) *(u32x4*)(vbuf + (((hh * 4 + wm) * 2 + m) * 4 + 2 * wn + t) * 1024 + lane * 16) = pack8(acc[m][2 * t], acc[m][2 * t + 1]);
    }
  }
#pragma unroll
  for (int hh = 0; hh < NHH; ++hh) {
    const int h = h0 + hh;
    const int seg = rb * 4 + wm, O = 5 * h + 1 + (lane >> 4);
    const unsigned vo = ((seg * 80 * 20 + (O >> 3)) * 64 + (O & 3) * 16 + ((O >> 2) & 1) * 8 + (lane & 7)) * 16 + ((lane >> 3) & 1) * 20480;
    u32x4 vf[2][4];
#pragma unroll
    for (int nt = 0; nt < 5; ++nt) {
      f32x4 acc[2][4] = {};
#pragma unroll
      for (int kh = 0; kh < 2; ++kh) {
        const int u = NHH * 10 + hh * 10 + nt * 2 + kh;
        switch (vvo_allow(u, T)) {
          case 14: WAIT_V(14); break; case 12: WAIT_V(12); break; case 10: WAIT_V(10); break; case 8: WAIT_V(8); break;
          default: WAIT_V(6); break;
        }
        LBAR();
        if (u + RING - 1 < T) issue(u + RING - 1);
        if (nt == 0 && kh == 0) {
#pragma unroll
          for (int m = 0; m < 2; ++m)
#pragma unroll
            for (int t = 0; t < 4; ++t) vf[m][t] = *(const u32x4*)(vbuf + (((hh * 4 + wm) * 2 + m) * 4 + t) * 1024 + lane * 16);
        }
        mma_tile<false, true, 512>(vf[0][2 * kh], vf[1][2 * kh], vf[0][2 * kh + 1], vf[1][2 * kh + 1], tp0, tp1, (u % RING) * 16384, acc);
      }
#pragma unroll
      for (int n = 0; n < 4; ++n)
        stc(wr, WS_VO + (nt * 16 + wn * 8 + n * 2) * 20480, vo, pack8(acc[0][n], acc[1][n]));
    }
  }
}

__device__ __forceinline__ void task_u(const bf16* Wv, const bf16* Wo, bf16* U, int h, int db, char* sm, int tid) {
  const int lane = tid & 63, wid = __builtin_amdgcn_readfirstlane(tid >> 6), wm = wid >> 1, wn = wid & 1;
  const unsigned lw = lds_addr(sm) + wid * 1024;
  const unsigned tb0 = lds_addr(sm) + wn * 1024 + trB_base<false>(lane, 0), tb1 = lds_addr(sm) + wn * 1024 + trB_base<false>(lane, 1);
  int r0, c0, r1, c1; stageB_rc(tid, r0, c0); stageB_rc(tid + 512, r1, c1);
  const unsigned p0 = (r0 * D_ + c0 * 8) * 2, p1 = (r1 * D_ + c1 * 8) * 2;
  auto issue = [&](int v) {
    const unsigned d = lw + (v % RING) * 16384;
    const bf16* b = Wo + (size_t)(h * DH + (v & 1) * 64) * D_ + (v >> 1) * 128;
    glds16(b, p0, d); glds16(b, p1, d + 8192);
  };
#pragma unroll
  for (int u = 0; u < RING - 1; ++u) issue(u);
  u32x4 vf[2][4];
#pragma unroll
  for (int m = 0; m < 2; ++m)
#pragma unroll
    for (int s = 0; s < 4; ++s) vf[m][s] = *(const u32x4*)(Wv + (size_t)(db * 128 + wm * 32 + m * 16 + (lane & 15)) * HD + h * DH + s * 32 + (lane >> 4) * 8);
#pragma unroll
  for (int nt = 0; nt < 5; ++nt) {
    f32x4 acc[2][4] = {};
#pragma unroll
    for (int kh = 0; kh < 2; ++kh) {
      const int v = nt * 2 + kh;
      ring_wait(v, 10);
      if (v + RING - 1 < 10) issue(v + RING - 1);
      mma_tile<true, false, 512>(vf[0][2 * kh], vf[1][2 * kh], vf[0][2 * kh + 1], vf[1][2 * kh + 1], tb0, tb1, (v % RING) * 16384, acc);
    }
#pragma unroll
    for (int m = 0; m < 2; ++m)
#pragma unroll
      for (int t = 0; t < 2; ++t) {
        const int d1 = db * 128 + wm * 32 + m * 16 + (lane & 15);
        const int c8 = nt * 16 + wn * 8 + t * 4 + ((lane >> 4) & 1) * 2 + (lane >> 5);
        auto s0 = __builtin_amdgcn_permlane16_swap(pack2(acc[m][2 * t][0], acc[m][2 * t][1]), pack2(acc[m][2 * t + 1][0], acc[m][2 * t + 1][1]), false, false);
        auto s1 = __builtin_amdgcn_permlane16_swap(pack2(acc[m][2 * t][2], acc[m][2 * t][3]), pack2(acc[m][2 * t + 1][2], acc[m][2 * t + 1][3]), false, false);
        *(u32x4*)(U + (((size_t)c8 * NH + h) * D_ + d1) * 8) = u32x4{s0[0], s1[0], s0[1], s1[1]};
      }
  }
  LBAR();
}

__device__ __forceinline__ void wave_t1(rsrc_t wr, const char* ql, const u32x4 (&kf)[2][4], const u32x4 (&km)[4], int seg, int h, int lane) {
  const int fr = lane & 15, fq = lane >> 4;
  const float cs = 0.08838834764831845f * 1.4426950408889634f;
#pragma unroll
  for (int i = 0; i < 2; ++i) {
    f32x4 sc[3] = {};
#pragma unroll
    for (int t = 0; t < 4; ++t) {
      const u32x4 qf = *(const u32x4*)(ql + (i * 4 + t) * 1024 + lane * 16);
      sc[0] = mfma16(km[t], qf, sc[0]); sc[1] = mfma16(kf[0][t], qf, sc[1]); sc[2] = mfma16(kf[1][t], qf, sc[2]);
    }
    const int qi = i * 16 + fr;
    float mx = -INFINITY;
#pragma unroll
    for (int r = 0; r < 4; ++r) {
      if (fq >= 2) sc[0][r] = -INFINITY;
      if (fq * 4 + r > qi) sc[1][r] = -INFINITY;
      if (16 + fq * 4 + r > qi) sc[2][r] = -INFINITY;
      mx = fmaxf(mx, fmaxf(sc[0][r], fmaxf(sc[1][r], sc[2][r])));
    }
    mx = red_max_q(mx);
    float sum = 0.f;
#pragma unroll
    for (int k = 0; k < 3; ++k)
#pragma unroll
      for (int r = 0; r < 4; ++r) { sc[k][r] = __builtin_amdgcn_exp2f((sc[k][r] - mx) * cs); sum += sc[k][r]; }
    sum = red_sum_q(sum);
    const float inv = 1.f / sum;
    const unsigned prow = ((seg * 2 + i) * 160 + 5 * h) * 256 + fr * 16;
    stc(wr, WS_P, prow + (1 + fq) * 256, pack8(sc[1] * inv, sc[2] * inv));
    if (fq < 2) { u32x2 o2 = {pack2(sc[0][0] * inv, sc[0][1] * inv), pack2(sc[0][2] * inv, sc[0][3] * inv)}; stc2(wr, WS_P, prow + fq * 8, o2); }
    __builtin_amdgcn_sched_barrier(0);
  }
}

__device__ __forceinline__ void task_t2(const bf16* x, rsrc_t wr, const Dep& dep, unsigned* bc, int seg, int cs, char* sm, int tid) {
  const int lane = tid & 63, wid = __builtin_amdgcn_readfirstlane(tid >> 6), fr = lane & 15, fq = lane >> 4;
  const bool rdy = dep_first(wr, dep, wid, bc);
  f32x4 acc[2][4] = {};
  u32x4 a[5][2], b[5][4];
#pragma unroll
  for (int k = 0; k < 5; ++k) {
    const int O = (wid * 5 + k) * 4 + fq;
#pragma unroll
    for (int n = 0; n < 4; ++n) {
      const int col = cs * 64 + n * 16 + fr;
      b[k][n] = ldc(wr, WS_VO, (((seg * 80 + (col >> 3)) * 20 + (O >> 3)) * 64 + (O & 3) * 16 + ((O >> 2) & 1) * 8 + (col & 7)) * 16);
    }
  }
  if (!rdy) {
    dep_second(wr, dep, wid);
#pragma unroll
    for (int k = 0; k < 5; ++k) {
      const int O = (wid * 5 + k) * 4 + fq;
      if (O % 5 == 0) {
#pragma unroll
        for (int n = 0; n < 4; ++n) {
          const int col = cs * 64 + n * 16 + fr;
          b[k][n] = ldc(wr, WS_VO, (((seg * 80 + (col >> 3)) * 20 + (O >> 3)) * 64 + (O & 3) * 16 + ((O >> 2) & 1) * 8 + (col & 7)) * 16);
        }
      }
    }
  }
#pragma unroll
  for (int k = 0; k < 5; ++k)
#pragma unroll
    for (int m = 0; m < 2; ++m) a[k][m] = ldc(wr, WS_P, ((seg * 2 + m) * 40 + wid * 5 + k) * 1024 + lane * 16);
#pragma unroll
  for (int k = 0; k < 5; ++k)
#pragma unroll
    for (int m = 0; m < 2; ++m)
#pragma unroll
      for (int n = 0; n < 4; ++n) acc[m][n] = mfma16(b[k][n], a[k][m], acc[m][n]);
  float* red = (float*)sm;
#pragma unroll
  for (int m = 0; m < 2; ++m)
#pragma unroll
    for (int n = 0; n < 4; ++n) *(f32x4*)(red + (wid * 32 + m * 16 + fr) * 68 + n * 16 + fq * 4) = acc[m][n];
  LBAR();
  if (tid < 256) {
    const int row = tid >> 3, c8 = (tid & 7) * 8;
    const int col = cs * 64 + c8, grow = seg * 32 + row;
    const u32x4 xv = *(const u32x4*)(x + (size_t)grow * D_ + col);
    f32x4 s0 = *(const f32x4*)(red + row * 68 + c8), s1 = *(const f32x4*)(red + row * 68 + c8 + 4);
#pragma unroll
    for (int w = 1; w < 8; ++w) { s0 += *(const f32x4*)(red + (w * 32 + row) * 68 + c8); s1 += *(const f32x4*)(red + (w * 32 + row) * 68 + c8 + 4); }
    const u32x4 av = pack8(s0, s1);
    u32x4 o;
#pragma unroll
    for (int e = 0; e < 4; ++e) o[e] = pack2(bflo(xv[e]) + bflo(av[e]), bfhi(xv[e]) + bfhi(av[e]));
    stc(wr, WS_H, ((((grow >> 4) * 20 + (col >> 5)) * 4 + ((col >> 3) & 3)) * 16 + (grow & 15)) * 16, o);
    float sm8 = 0.f;
#pragma unroll
    for (int e = 0; e < 4; ++e) sm8 += bflo(o[e]) + bfhi(o[e]);
    sm8 = red_sum8(sm8);
    const float mu = sm8 * (1.f / 64.f);
    float m2 = 0.f;
#pragma unroll
    for (int e = 0; e < 4; ++e) { const float a = bflo(o[e]) - mu, b = bfhi(o[e]) - mu; m2 += a * a + b * b; }
    m2 = red_sum8(m2);
    if ((tid & 7) == 0) stc2(wr, WS_ST, (grow * 10 + cs) * 8, u32x2{__float_as_uint(sm8), __float_as_uint(m2)});
  }
}

__device__ __forceinline__ void task_f1(rsrc_t wr, const Dep& dep, const float* CS, const bf16* Wg, const bf16* Wu, int rb, int f0, int nt, char* sm, int tid) {
  const int lane = tid & 63, wid = __builtin_amdgcn_readfirstlane(tid >> 6), wm = wid >> 1, wn = wid & 1;
  const unsigned lw = lds_addr(sm) + wid * 1024;
  const unsigned tb0 = lds_addr(sm) + wn * 512 + trB_base<false>(lane, 0), tb1 = lds_addr(sm) + wn * 512 + trB_base<false>(lane, 1);
  int r0, c0, r1, c1; stageB_rc(tid, r0, c0); stageB_rc(tid + 512, r1, c1);
  const bf16* pw = (wid & 1) ? Wu : Wg;
  const unsigned o0 = (r0 * FF + (c0 & 7) * 8) * 2, o1 = (r1 * FF + (c1 & 7) * 8) * 2;
  const int T = nt * 10;
  auto issue = [&](int ff, int u) {
    const size_t o = (size_t)(u * 64) * FF + (f0 + ff) * 64;
    const unsigned d = lw + (u % RING) * 16384;
    glds16(pw + o, o0, d); glds16(pw + o, o1, d + 8192);
  };
#pragma unroll
  for (int u = 0; u < RING - 1; ++u) issue(0, u);
  u32x4 af[2][20];
  float* lcs = (float*)(sm + RING * 16384);
  if (tid < nt * 128) lcs[tid] = CS[((tid >> 6) & 1) * FF + (f0 + (tid >> 7)) * 64 + (tid & 63)];
  dep_wait(wr, dep, wid);
  load_afc(wr, (unsigned)WS_H + (rb * 8 + wm * 2) * 20480, af, lane);
  float mean[2], rstd[2];
#pragma unroll
  for (int m = 0; m < 2; ++m) {
    const unsigned sp = (rb * 128 + wm * 32 + m * 16 + (lane & 15)) * 80;
    f32x4 p[5];
#pragma unroll
    for (int i = 0; i < 5; ++i) p[i] = __builtin_bit_cast(f32x4, ldc(wr, WS_ST, sp + i * 16));
    float s = 0.f;
#pragma unroll
    for (int i = 0; i < 5; ++i) s += p[i][0] + p[i][2];
    mean[m] = s * (1.f / 640.f);
    float q = 0.f;
#pragma unroll
    for (int i = 0; i < 5; ++i) {
      const float d0 = p[i][0] * (1.f / 64.f) - mean[m], d1 = p[i][2] * (1.f / 64.f) - mean[m];
      q += p[i][1] + p[i][3] + 64.f * (d0 * d0 + d1 * d1);
    }
    rstd[m] = 1.f / sqrtf(q * (1.f / 640.f) + 1e-5f);
  }
  for (int ff = 0; ff < nt; ++ff) {
    f32x4 acc[2][4] = {};
#pragma unroll
    for (int u = 0; u < 10; ++u) {
      if (u < 4) { WAIT_V(8); LBAR(); } else ring_wait(ff * 10 + u, T);
      if (u + RING - 1 < 10) issue(ff, u + RING - 1); else if (ff + 1 < nt) issue(ff + 1, u + RING - 1 - 10);
      mma_tile<true, false, 1024>(af[0][2 * u], af[1][2 * u], af[0][2 * u + 1], af[1][2 * u + 1], tb0, tb1, (u % RING) * 16384, acc);
    }
    f32x4 cs[4];
#pragma unroll
    for (int n = 0; n < 4; ++n) cs[n] = *(const f32x4*)(lcs + ff * 128 + (n >> 1) * 64 + (2 * wn + (n & 1)) * 16 + (lane >> 4) * 4);
#pragma unroll
    for (int m = 0; m < 2; ++m) {
#pragma unroll
      for (int n = 0; n < 4; ++n) acc[m][n] = (acc[m][n] - mean[m] * cs[n]) * rstd[m];
      u32x4 g = pack8(acc[m][0], acc[m][1]), u = pack8(acc[m][2], acc[m][3]), o;
      auto rl = [](float v) { return v > 0.f ? v : 0.f * v; };
#pragma unroll
      for (int e = 0; e < 4; ++e) o[e] = pack2(rl(bflo(g[e])) * bflo(u[e]), rl(bfhi(g[e])) * bfhi(u[e]));
      stc(wr, WS_G, ((rb * 8 + wm * 2 + m) * 54 + (f0 + ff) * 2 + wn) * 1024 + lane * 16, o);
    }
  }
}

__device__ __forceinline__ void task_f2(const bf16* G, const bf16* Wd, rsrc_t wr, bf16* out, int rb, int cs, char* sm, int tid) {
  const int lane = tid & 63, wid = __builtin_amdgcn_readfirstlane(tid >> 6), wm = wid >> 1, wn = wid & 1;
  const unsigned lw = lds_addr(sm) + wid * 1024;
  const unsigned tp0 = lds_addr(sm) + wn * 1024 + trB_base<true>(lane, 0), tp1 = lds_addr(sm) + wn * 1024 + trB_base<true>(lane, 1);
  int r0, c0, r1, c1; stageB_rc(tid, r0, c0); stageB_rc(tid + 512, r1, c1);
  const bf16* pa = G + (size_t)(rb * 8 + wid) * 54 * 512;
  const bf16* pb = Wd + cs * 128;
  const unsigned o0 = (r0 * D_ + c0 * 8) * 2, o1 = (r1 * D_ + c1 * 8) * 2;
  constexpr int R4 = 4;
  auto issue = [&](int t) {
    const unsigned d = lds_addr(sm) + (t % R4) * 32768;
    glds16c(pa + t * 1024, lane * 16, d + wid * 2048); glds16c(pa + t * 1024 + 512, lane * 16, d + wid * 2048 + 1024);
    glds16(pb + (size_t)t * 64 * D_, o0, d + 16384 + wid * 1024); glds16(pb + (size_t)t * 64 * D_, o1, d + 24576 + wid * 1024);
  };
  (void)lw;
  issue(0); issue(1); issue(2);
  f32x4 acc[2][4] = {};
  for (int t = 0; t < 27; ++t) {
    if (t + 2 < 27) { WAIT_V(8); } else if (t + 1 < 27) { WAIT_V(4); } else { WAIT_V(0); }
    LBAR();
    if (t + 3 < 27) issue(t + 3);
    const char* d = sm + (t % R4) * 32768;
    const u32x4 a00 = *(const u32x4*)(d + (wm * 2) * 2048 + lane * 16), a01 = *(const u32x4*)(d + (wm * 2) * 2048 + 1024 + lane * 16);
    const u32x4 a10 = *(const u32x4*)(d + (wm * 2 + 1) * 2048 + lane * 16), a11 = *(const u32x4*)(d + (wm * 2 + 1) * 2048 + 1024 + lane * 16);
    mma_tile<true, true, 512>(a00, a10, a01, a11, tp0 + (t % R4) * 32768, tp1 + (t % R4) * 32768, 16384, acc);
  }
#pragma unroll
  for (int m = 0; m < 2; ++m)
#pragma unroll
    for (int n = 0; n < 4; ++n) {
      const size_t go = (size_t)(rb * 128 + wm * 32 + m * 16 + (lane & 15)) * D_ + cs * 128 + wn * 64 + n * 16 + (lane >> 4) * 4;
      const int col = cs * 128 + wn * 64 + n * 16 + (lane >> 4) * 4;
      u32x2 hv = ldc2(wr, WS_H, ((((rb * 8 + wm * 2 + m) * 20 + (col >> 5)) * 64 + ((col >> 3) & 3) * 16 + (lane & 15)) * 8 + (col & 7)) * 2);
      unsigned a0 = pack2(acc[m][n][0], acc[m][n][1]), a1 = pack2(acc[m][n][2], acc[m][n][3]);
      u32x2 o = {pack2(bflo(hv.x) + bflo(a0), bfhi(hv.x) + bfhi(a0)), pack2(bflo(hv.y) + bflo(a1), bfhi(hv.y) + bfhi(a1))};
      *(u32x2*)(out + go) = o;
    }
}

__device__ __forceinline__ void task_f1p(rsrc_t wr, const float* CS, const bf16* Wg, const bf16* Wu, int j, char* sm, int tid) {
  const int lane = tid & 63, wid = __builtin_amdgcn_readfirstlane(tid >> 6), kq = wid & 3, ch = wid >> 2, fr = lane & 15, fq = lane >> 4;
  u32x4 w[4][5];
#pragma unroll
  for (int gc = 0; gc < 4; ++gc)
#pragma unroll
    for (int i = 0; i < 5; ++i) {
      const unsigned short* p = (const unsigned short*)((gc >> 1) ? Wu : Wg) + (size_t)((kq * 5 + i) * 32 + fq * 8) * FF + j * 32 + (gc & 1) * 16 + fr;
#pragma unroll
      for (int e = 0; e < 4; ++e) w[gc][i][e] = (unsigned)p[(2 * e) * FF] | ((unsigned)p[(2 * e + 1) * FF] << 16);
    }
  f32x4 cs[2][2];
#pragma unroll
  for (int gu = 0; gu < 2; ++gu)
#pragma unroll
    for (int c = 0; c < 2; ++c) cs[gu][c] = *(const f32x4*)(CS + gu * FF + j * 32 + c * 16 + fq * 4);
  for (int seg = 48; seg < NSEG; seg += 2) {
    dep_wait(wr, Dep{C_T2S + seg * 256, 10, C_T2S + seg * 256 + 256, 10}, wid ^ 7);
    u32x4 a[2][5];
#pragma unroll
    for (int rt = 0; rt < 2; ++rt)
#pragma unroll
      for (int i = 0; i < 5; ++i) a[rt][i] = ldc(wr, WS_H, ((seg * 2 + ch * 2 + rt) * 20 + kq * 5 + i) * 1024 + lane * 16);
    f32x4 p[5];
    if (wid < 4) {
#pragma unroll
      for (int i = 0; i < 5; ++i) p[i] = __builtin_bit_cast(f32x4, ldc(wr, WS_ST, (seg * 32 + wid * 16 + fr) * 80 + i * 16));
    }
#pragma unroll
    for (int rt = 0; rt < 2; ++rt)
#pragma unroll
      for (int gc = 0; gc < 4; ++gc) {
        f32x4 acc = ZERO4;
#pragma unroll
        for (int i = 0; i < 5; ++i) acc = mfma16(w[gc][i], a[rt][i], acc);
        *(f32x4*)(sm + (((ch * 2 + rt) * 4 + gc) * 4 + kq) * 1024 + lane * 16) = acc;
      }
    LBAR();
    if (wid < 4) {
      float s = 0.f;
#pragma unroll
      for (int i = 0; i < 5; ++i) s += p[i][0] + p[i][2];
      const float mean = s * (1.f / 640.f);
      float q = 0.f;
#pragma unroll
      for (int i = 0; i < 5; ++i) {
        const float d0 = p[i][0] * (1.f / 64.f) - mean, d1 = p[i][2] * (1.f / 64.f) - mean;
        q += p[i][1] + p[i][3] + 64.f * (d0 * d0 + d1 * d1);
      }
      const float rstd = 1.f / sqrtf(q * (1.f / 640.f) + 1e-5f);
      f32x4 v[2][2];
#pragma unroll
      for (int gu = 0; gu < 2; ++gu)
#pragma unroll
        for (int c = 0; c < 2; ++c) {
          const char* b = sm + ((wid * 2 + gu) * 2 + c) * 4096 + lane * 16;
          v[gu][c] = (*(const f32x4*)b + *(const f32x4*)(b + 1024)) + (*(const f32x4*)(b + 2048) + *(const f32x4*)(b + 3072));
          v[gu][c] = (v[gu][c] - mean * cs[gu][c]) * rstd;
        }
      u32x4 g = pack8(v[0][0], v[0][1]), u = pack8(v[1][0], v[1][1]), o;
      auto rl = [](float x) { return x > 0.f ? x : 0.f * x; };
#pragma unroll
      for (int e = 0; e < 4; ++e) o[e] = pack2(rl(bflo(g[e])) * bflo(u[e]), rl(bfhi(g[e])) * bfhi(u[e]));
      stc(wr, WS_G, ((seg * 2 + wid) * 54 + j) * 1024 + lane * 16, o);
      WAIT_V(0);
      if (lane == 0) __builtin_amdgcn_raw_ptr_buffer_atomic_add_i32(1, wr, 0, C_F1S + (seg + (wid >> 1)) * 256, 16);
    }
  }
}
__device__ __forceinline__ void task_f2p(rsrc_t wr, const bf16* Wd, bf16* out, int j, int par, char* sm, int tid) {
  const int lane = tid & 63, wid = __builtin_amdgcn_readfirstlane(tid >> 6), fr = lane & 15, fq = lane >> 4;
  u32x4 w[2][7];
#pragma unroll
  for (int i = 0; i < 7; ++i) {
    const int ks = min(wid + 8 * i, 53);
#pragma unroll
    for (int ct = 0; ct < 2; ++ct) {
      const unsigned short* p = (const unsigned short*)Wd + (size_t)(ks * 32 + fq * 4) * D_ + j * 32 + (fr >> 2) * 8 + ct * 4 + (fr & 3);
#pragma unroll
      for (int e = 0; e < 4; ++e) {
        const int k0 = (e >> 1) * 16 + (e & 1) * 2;
        w[ct][i][e] = wid + 8 * i < 54 ? (unsigned)p[k0 * D_] | ((unsigned)p[(k0 + 1) * D_] << 16) : 0u;
      }
    }
  }
  for (int seg = 48 + par; seg < NSEG; seg += 2) {
    dep_wait(wr, Dep{C_F1S + seg * 256, 108, 0, 0}, wid ^ 7);
    u32x4 a[2][7];
#pragma unroll
    for (int rt = 0; rt < 2; ++rt)
#pragma unroll
      for (int i = 0; i < 7; ++i) a[rt][i] = ldc(wr, WS_G, ((seg * 2 + rt) * 54 + min(wid + 8 * i, 53)) * 1024 + lane * 16);
    u32x4 hv;
    if (wid < 2) hv = ldc(wr, WS_H, (((seg * 2 + wid) * 20 + j) * 64 + lane) * 16);
#pragma unroll
    for (int rt = 0; rt < 2; ++rt)
#pragma unroll
      for (int ct = 0; ct < 2; ++ct) {
        f32x4 acc = ZERO4;
#pragma unroll
        for (int i = 0; i < 7; ++i) acc = mfma16(w[ct][i], a[rt][i], acc);
        *(f32x4*)(sm + ((rt * 2 + ct) * 8 + wid) * 1024 + lane * 16) = acc;
      }
    LBAR();
    if (wid < 2) {
      f32x4 v[2];
#pragma unroll
      for (int ct = 0; ct < 2; ++ct) {
        const char* b = sm + (wid * 2 + ct) * 8192 + lane * 16;
        v[ct] = *(const f32x4*)b;
#pragma unroll
        for (int i = 1; i < 8; ++i) v[ct] += *(const f32x4*)(b + i * 1024);
      }
      const u32x4 av = pack8(v[0], v[1]);
      u32x4 o;
#pragma unroll
      for (int e = 0; e < 4; ++e) o[e] = pack2(bflo(hv[e]) + bflo(av[e]), bfhi(hv[e]) + bfhi(av[e]));
      *(u32x4*)(out + (size_t)(seg * 32 + wid * 16 + fr) * D_ + j * 32 + fq * 8) = o;
    }
  }
}

__device__ __forceinline__ unsigned xoff(int step, unsigned o, unsigned par) {
  return (unsigned)step * 65536u + ((o >> 13) << 14) + (o & 8191u) + (((par >> (step >> 5)) & 1u) << 13);
}

__global__ void __launch_bounds__(512, 2) k_prologue(const bf16* x, const bf16* mem0, const bf16* Wv, const bf16* Wo, const bf16* Wg, const bf16* Wu, char* ws) {
  extern __shared__ __attribute__((aligned(16))) char sm[];
  const int tid = threadIdx.x, bid = blockIdx.x;
  if (bid == 0) {
    if (tid < 64) {
      const __amdgpu_buffer_rsrc_t r = __builtin_amdgcn_make_buffer_rsrc(ws + WS_XCH, 0, NC << 22, 0x00020000);
      for (int g = 0; g < 2; ++g) {
        unsigned best[2] = {~0u, ~0u};
        for (int k = 0; k < 8; ++k)
          for (int p = 0; p < 2; ++p) {
            unsigned off = (g << 21) + (k * 4 + 1) * 65536 + 3 * 16384 + p * 8192 + 6144 + 512;
            asm volatile("s_waitcnt vmcnt(0)" ::: "memory");
            unsigned long long t0 = __builtin_amdgcn_s_memtime();
            unsigned v = __builtin_amdgcn_raw_buffer_load_b32(r, off, 0, 16);
            asm volatile("s_waitcnt vmcnt(0)" ::"v"(v) : "memory");
            unsigned long long t1 = __builtin_amdgcn_s_memtime();
            unsigned d = (unsigned)(t1 - t0); if (d < best[p]) best[p] = d;
          }
        ((unsigned*)sm)[2 * g] = best[0]; ((unsigned*)sm)[2 * g + 1] = best[1];
      }
    } else if (tid >= 256) {
      for (unsigned o = C_QH + (tid - 256) * 16; o < C_END; o += 256 * 16) *(u32x4*)(ws + o) = u32x4{o < C_QH + 1024 && !(o & 255) ? 36u : 0u, 0u, 0u, 0u};
    }
    LBAR();
    if (tid < 16) {
      const u32x4 b = *(const u32x4*)sm;
      unsigned tab = (b[1] < b[0] ? 1u : 0u) | (b[3] < b[2] ? 2u : 0u);
      if (xcc_id() >= 4) tab ^= 3u;
      ((unsigned*)(ws + WS_CTL))[tid] = tid == 0 ? tab : tid == 1 ? xcc_id() : 0u;
    }
    return;
  }
  if (bid <= 16) {
    for (int i = tid; i < 4 * NC * 4096; i += 512) {
      const int step = (bid - 1) * 4 * NC + (i >> 12);
      const unsigned po = (i & 4095) * 16;
      const unsigned lo = ((po >> 14) << 13) + (po & 8191u);
      if (lo >= 30720u) continue;
      u32x4 v = {SENT, SENT, SENT, SENT};
      if (step < NC && lo < 10240u) {
        const int b = lo >> 7, row = (lo >> 4) & 7;
        v = *(const u32x4*)(mem0 + row * D_ + b * 8);
        v.x = canon(v.x); v.y = canon(v.y); v.z = canon(v.z); v.w = canon(v.w);
      }
      *(u32x4*)(ws + WS_XCH + (size_t)step * 65536 + po) = v;
    }
    return;
  }
  if (bid <= 48) {
    if (tid < 256) { const int rt = (bid - 17) * 4 + (tid >> 6); ln_tile(x + (size_t)rt * 16 * D_, (bf16*)(ws + WS_XN) + (size_t)rt * 10240, tid & 63); }
    return;
  }
  if (bid <= 75) {
    const int f0 = (bid - 49) * 64, ch = tid & 7, rg = tid >> 3;
    float* red = (float*)sm;
#pragma unroll
    for (int w = 0; w < 2; ++w) {
      const bf16* W = w ? Wu : Wg;
      float a[8] = {};
#pragma unroll
      for (int i = 0; i < 10; ++i) {
        u32x4 v = *(const u32x4*)(W + (size_t)(rg * 10 + i) * FF + f0 + ch * 8);
#pragma unroll
        for (int e = 0; e < 4; ++e) { a[2 * e] += bflo(v[e]); a[2 * e + 1] += bfhi(v[e]); }
      }
      *(f32x4*)(red + (w * 64 + rg) * 64 + ch * 8) = f32x4{a[0], a[1], a[2], a[3]};
      *(f32x4*)(red + (w * 64 + rg) * 64 + ch * 8 + 4) = f32x4{a[4], a[5], a[6], a[7]};
    }
    LBAR();
    if (tid < 128) {
      float t = 0.f;
      for (int i = 0; i < 64; ++i) t += red[((tid >> 6) * 64 + i) * 64 + (tid & 63)];
      ((float*)(ws + WS_CS))[(tid >> 6) * FF + f0 + (tid & 63)] = t;
    }
    return;
  }
  const int t = bid - 76;
  task_u(Wv, Wo, (bf16*)(ws + WS_U), t / 5, t % 5, sm, tid);
}

#define TS(i)
#define TSDECL
#define TSOUT(s)
#define AWIN 8
#define SLT 0
#define STAG 6
#define SLA 30
#define SLM 20
#define SLR 8
#define STL 6
#define DL 1
#define SLV 6
#define VPOS 2
#define SLB 16
__device__ __forceinline__ bool frag_bad(u32x4 v) { return max(max(v.x, v.y), max(v.z, v.w)) == SENT; }
__device__ __forceinline__ void glds16n(const void* base, unsigned voff, unsigned lds) {
  asm volatile("s_mov_b32 m0, %2\n\ts_nop 0\n\tglobal_load_lds_dwordx4 %0, %1 nt" ::"v"(voff), "s"(base), "s"(__builtin_amdgcn_readfirstlane(lds)) : "m0");
}
template <int NL, int DEPTH, int SG, bool LOCAL = false>
__device__ __forceinline__ void poll_lds(const char* xb, const unsigned (&off)[NL], char* const (&dst)[NL], int lane, u32x4 (&v)[NL]) {
  unsigned spins = 0;
#pragma unroll
  for (int d = 0; d < DEPTH - 1; ++d) {
#pragma unroll
    for (int i = 0; i < NL; ++i) { if (LOCAL) glds16n(xb, off[i], lds_addr(dst[i])); else glds16c(xb, off[i], lds_addr(dst[i])); }
    __builtin_amdgcn_s_sleep(SG);
  }
  for (;;) {
    if (LOCAL && (spins & 15) != 15) {
#pragma unroll
      for (int i = 0; i < NL; ++i) glds16n(xb, off[i], lds_addr(dst[i]));
    } else {
#pragma unroll
      for (int i = 0; i < NL; ++i) glds16c(xb, off[i], lds_addr(dst[i]));
    }
    wait_v<NL * (DEPTH - 1)>();
    bool bad = false;
#pragma unroll
    for (int i = 0; i < NL; ++i) v[i] = *(const u32x4*)(dst[i] + lane * 16);
#pragma unroll
    for (int i = 0; i < NL; ++i) bad |= frag_bad(v[i]);
    if (!__any(bad) || ++spins > SPIN_CAP) break;
  }
}
template <int NL>
__device__ __forceinline__ void poll_lds1(const char* xb, const unsigned (&off)[NL], char* const (&dst)[NL], int lane, u32x4 (&v)[NL]) {
  unsigned spins = 0, pend = (1u << NL) - 1u;
  for (;;) {
#pragma unroll
    for (int i = 0; i < NL; ++i) if (pend & (1u << i)) glds16c(xb, off[i], lds_addr(dst[i]));
    WAIT_V(0);
#pragma unroll
    for (int i = 0; i < NL; ++i) v[i] = *(const u32x4*)(dst[i] + lane * 16);
    pend = 0;
#pragma unroll
    for (int i = 0; i < NL; ++i) if (__any(frag_bad(v[i]))) pend |= 1u << i;
    if (!pend || ++spins > SPIN_CAP) break;
  }
}
template <class FB, class FO>
__device__ __forceinline__ void load_wfrags(FB base, FO voff, int fstride, u32x8 (&w)[2][10], char* sm, int tid) {
  const int lane = tid & 63, wid = __builtin_amdgcn_readfirstlane(tid >> 6);
  const int bq = lane >> 4, q = (lane & 15) >> 2, p = lane & 3;
  const unsigned lw = lds_addr(sm) + wid * 1024;
  unsigned vo[4];
#pragma unroll
  for (int i = 0; i < 4; ++i) {
    const int ci = i * 512 + tid, half = i >> 1, k = (ci >> 4) & 63;
    vo[i] = voff(k, half * 16 + ((ci & 15) ^ (((k & 3) << 1) | (((k >> 2) & 1) << 3))));
  }
  auto issue = [&](int f) {
#pragma unroll
    for (int i = 0; i < 4; ++i) glds16((const char*)base(i >> 1) + f * fstride, vo[i], lw + (f & 3) * 32768 + i * 8192);
  };
#pragma unroll
  for (int f = 0; f < 4; ++f) issue(f);
#pragma unroll
  for (int f = 0; f < 10; ++f) {
    if (f < 7) { WAIT_V(12); } else if (f == 7) { WAIT_V(8); } else if (f == 8) { WAIT_V(4); } else { WAIT_V(0); }
    BAR();
#pragma unroll
    for (int t = 0; t < 2; ++t) {
      u32x2 qd[4];
#pragma unroll
      for (int z = 0; z < 4; ++z) {
        const int k = 16 * (bq >> 1) + 4 * (bq & 1) + (z & 1) * 8 + (z >> 1) * 32 + q;
        const int cc = (2 * wid + t) * 2 + (p >> 1);
        qd[z] = tr_read(sm + (f & 3) * 32768 + ((cc >> 4) * 1024 + k * 16 + ((cc & 15) ^ (((k & 3) << 1) | (((k >> 2) & 1) << 3)))) * 16 + 8 * (p & 1));
      }
      w[t][f] = u32x8{qd[0].x, qd[1].x, qd[0].y, qd[1].y, qd[2].x, qd[3].x, qd[2].y, qd[3].y};
    }
    LBAR();
    if (f + 4 < 10) issue(f + 4);
  }
}

template <bool FAST>
__device__ __forceinline__ void poll_mem(const char* xb, int s, unsigned par, char* memA, char* other, int wid, int lane) {
  if (wid < 3) {
    unsigned off[2] = {xoff(s, wid * 1024 + lane * 16, par), xoff(s, (7 + wid) * 1024 + lane * 16, par)};
    char* const dst[2] = {memA + wid * 1024, memA + (7 + wid) * 1024};
    u32x4 v[2];
    if (FAST) {
      poll_lds<2, 2, STAG>(xb, off, dst, lane, v);
      *(u32x4*)(other + wid * 1024 + lane * 16) = u32x4{SENT, SENT, SENT, SENT};
      *(u32x4*)(other + (7 + wid) * 1024 + lane * 16) = u32x4{SENT, SENT, SENT, SENT};
    } else poll_lds1<2>(xb, off, dst, lane, v);
  } else if (wid < 7) {
    unsigned off[1] = {xoff(s, wid * 1024 + lane * 16, par)};
    char* const dst[1] = {memA + wid * 1024};
    u32x4 v[1];
    if (FAST) {
      poll_lds<1, 2, STAG>(xb, off, dst, lane, v);
      *(u32x4*)(other + wid * 1024 + lane * 16) = u32x4{SENT, SENT, SENT, SENT};
    } else poll_lds1<1>(xb, off, dst, lane, v);
  }
}
template <int WIN>
__device__ __forceinline__ f32x4 mem_times_w(const char* memA, const u32x8 (&w)[2][10], int lane) {
  const int sidx = (lane & 15) < 8 ? 0x4444 : 0xEEEE;
  u32x4 a[10];
#pragma unroll
  for (int f = 0; f < WIN; ++f) a[f] = *(const u32x4*)(memA + f * 1024 + lane * 16);
  f32x4 a0 = ZERO4, a1 = ZERO4;
#pragma unroll
  for (int f = 0; f < 10; ++f) {
    a0 = smf(a[f], w[0][f], a0, sidx); a1 = smf(a[f], w[1][f], a1, sidx);
    if (f + WIN < 10) { __builtin_amdgcn_sched_barrier(0); a[f + WIN] = *(const u32x4*)(memA + (f + WIN) * 1024 + lane * 16); }
  }
  f32x4 v;
#pragma unroll
  for (int r = 0; r < 4; ++r) {
    auto sw = __builtin_amdgcn_permlane32_swap(__float_as_uint(a0[r]), __float_as_uint(a1[r]), false, false);
    v[r] = __uint_as_float(sw[0]) + __uint_as_float(sw[1]);
  }
  return v;
}

constexpr int LDS_MAIN = 147456 + 64;
#define TPREF 0
__global__ void __launch_bounds__(512, 2) k_main(const bf16* x, const bf16* Wq, const bf16* Wk, const bf16* Wv, const bf16* Wo, const bf16* Wg, const bf16* Wu,
                                                 const bf16* Wd, char* ws, bf16* out) {
  extern __shared__ __attribute__((aligned(16))) char sm[];
  const int tid = threadIdx.x;
  const int wid = __builtin_amdgcn_readfirstlane(tid >> 6);
  unsigned* ctl = (unsigned*)(ws + WS_CTL);
  const int res = (blockIdx.x + __builtin_amdgcn_readfirstlane(ctl[1])) & 7, bi = blockIdx.x >> 3;
  const int slot = res < 4 && bi < 28 ? res * 28 + bi : 1000;
  const rsrc_t wr = __builtin_amdgcn_make_buffer_rsrc(ws, 0, (unsigned)(WS_END + 8 * MB), 0x00020000);
  auto ltid = [&]() { int l; asm volatile("v_mbcnt_lo_u32_b32 %0, -1, 0\n\tv_mbcnt_hi_u32_b32 %0, -1, %0" : "=v"(l)); return wid * 64 + l; };
  if (slot < 112) {
  const int tid = ltid(), lane = tid & 63;
  const int grp = slot / 28, tsl = slot % 28;
  const int role = tsl < 8 ? grp * 8 + tsl : 32 + grp * 20 + (tsl - 8);
  const unsigned par = ctl[0];
  const int cpy = role % NC;
  const __amdgpu_buffer_rsrc_t xr = __builtin_amdgcn_make_buffer_rsrc(ws + WS_XCH, 0, NC << 22, 0x00020000);
  const int fr = lane & 15, fq = lane >> 4;
  u32x8 w[2][10];
  char* memA = sm;
  if (role < 32) {
    const int h = role;
    load_wfrags([&](int half) { return (half ? Wk : Wq) + h * DH; }, [&](int k, int cc) { return (unsigned)(k * HD + (cc & 15) * 8) * 2u; }, 64 * HD * 2, w, sm, tid);
    char* qk = sm + 10240;
    char* kring = sm + 16384;
    char* memB = sm + 40960;
    char* qring = sm + 51200;
    const bf16* K = (const bf16*)(ws + WS_K) + (size_t)h * 4096;
    const float cs = 0.08838834764831845f * 1.4426950408889634f;
    auto kdma = [&](int s) {
      const bf16* src = K + (size_t)s * NH * 4096;
      const unsigned d = lds_addr(kring) + (s % 3) * 8192;
#pragma unroll
      for (int i = 0; i < 8; ++i) glds16c(src + i * 512, lane * 16, d + i * 1024);
#pragma unroll
      for (int i = 0; i < 8; ++i) glds16c(src - (WS_K - WS_Q) / 2 + i * 512, lane * 16, d + (51200 - 16384) + i * 1024);
    };
    if (wid == 7) { dep_poll(wr, C_K, 32); dep_poll(wr, C_Q, 32); kdma(0); kdma(1); WAIT_V(16); }
    for (int i = tid; i < 640; i += 512) *(u32x4*)(memA + i * 16) = u32x4{SENT, SENT, SENT, SENT};
    LBAR();
    const int Ost = fr < 8 ? 5 * h + 1 + fq : 5 * h;
    const unsigned pst = 10240 + (Ost >> 3) * 1024 + ((Ost & 3) * 16 + ((Ost >> 2) & 1) * 8 + (fr & 7)) * 16;
    for (int s = 0; s < NSEG; ++s) {
      TSDECL; TS(0);
      char* mcur = (s & 1) ? memB : memA;
      u32x4 kf[2][4];
      if (wid == 0 || wid == 7) {
        const char* kt = kring + (s % 3) * 8192 + lane * 16;
#pragma unroll
        for (int t = 0; t < 4; ++t) { kf[0][t] = *(const u32x4*)(kt + t * 1024); kf[1][t] = *(const u32x4*)(kt + 4096 + t * 1024); }
      }
      poll_mem<true>(ws + WS_XCH, s, par, mcur, (s & 1) ? memA : memB, wid, lane);
      if (wid == 7) { WAIT_V(4); }
      TS(1);
      LBAR();
      TS(2);
      if (role == 0 && tid == 384) __builtin_amdgcn_raw_buffer_store_b32(s, wr, C_PROG, 0, 16);
      { f32x4 v = mem_times_w<AWIN>(mcur, w, lane);
        const int col = (2 * wid + (lane >> 5)) * 16 + fr, e = col & 127;
        char* dst = qk + (col >> 7) * 2176 + (fq & 1) * 4 * 272 + (e >> 5) * 64 + ((e >> 2) & 3) * 16 + ((e >> 4) & 1) * 8 + (e & 3) * 2;
#pragma unroll
        for (int r = 0; r < 4; ++r) *(unsigned short*)(dst + r * 272) = (unsigned short)(pack2(v[r], v[r]) & 0xffffu); }
      TS(3);
      LBAR();
      TS(4);
      if (wid == 0) {
        if (s < NSEG - 1) {
          u32x4 qf[4], km[4];
#pragma unroll
          for (int t = 0; t < 4; ++t) {
            qf[t] = *(const u32x4*)(qk + (fr & 7) * 272 + t * 64 + fq * 16);
            km[t] = *(const u32x4*)(qk + 2176 + (fr & 7) * 272 + t * 64 + fq * 16);
          }
          asm volatile("" : "+v"(qf[0]), "+v"(qf[1]), "+v"(qf[2]), "+v"(qf[3]), "+v"(km[0]), "+v"(km[1]), "+v"(km[2]), "+v"(km[3]));
          f32x4 sc[3] = {};
#pragma unroll
          for (int t = 0; t < 4; ++t) { sc[1] = mfma16(kf[0][t], qf[t], sc[1]); sc[2] = mfma16(kf[1][t], qf[t], sc[2]); sc[0] = mfma16(km[t], qf[t], sc[0]); }
          const bool hi = fr >= 8;
          float e[6];
#pragma unroll
          for (int r = 0; r < 4; ++r) e[r] = hi ? sc[2][r] : sc[1][r];
          e[4] = hi ? sc[0][2] : sc[0][0]; e[5] = hi ? sc[0][3] : sc[0][1];
          if (fq >= 2) { e[4] = -INFINITY; e[5] = -INFINITY; }
          float mx = fmaxf(fmaxf(fmaxf(e[0], e[1]), fmaxf(e[2], e[3])), fmaxf(e[4], e[5]));
          mx = fmaxf(mx, __uint_as_float(dpp_ror8(__float_as_uint(mx))));
          mx = red_max_q(mx) * cs;
          float sum = 0.f;
#pragma unroll
          for (int r = 0; r < 6; ++r) { e[r] = __builtin_amdgcn_exp2f(__builtin_fmaf(e[r], cs, -mx)); sum += e[r]; }
          sum += __uint_as_float(dpp_ror8(__float_as_uint(sum)));
          sum = red_sum_q(sum);
          const float inv = __builtin_amdgcn_rcpf(sum);
          const unsigned p0 = canon(pack2(e[0] * inv, e[1] * inv)), p1 = canon(pack2(e[2] * inv, e[3] * inv)), pm = canon(pack2(e[4] * inv, e[5] * inv));
          const unsigned q0 = dpp_ror8(p0), q1 = dpp_ror8(p1), qm = dpp_ror8(pm);
          auto s0 = __builtin_amdgcn_permlane16_swap(qm, qm, false, false);
          auto s1 = __builtin_amdgcn_permlane16_swap(pm, pm, false, false);
          const u32x4 o = hi ? u32x4{qm, pm, s0[1], s1[1]} : u32x4{p0, p1, q0, q1};
          if (fr < 8 || fq == 0) __builtin_amdgcn_raw_buffer_store_b128(o, xr, xoff(s, pst, par), 0, 16);
        }
      } else if (wid == 7) {
        if (s > 48) { WAIT_V(0); if (lane == 0) __builtin_amdgcn_raw_ptr_buffer_atomic_add_i32(1, wr, 0, C_SEG + (s - 1) * 256, 16); }
        if (s + 2 < NSEG) {
          if (!((s + 2) & 3)) { dep_poll(wr, C_K + ((s + 2) >> 2) * 256, 32); dep_poll(wr, C_Q + ((s + 2) >> 2) * 256, 32); }
          kdma(s + 2);
        }
        if (SLT) __builtin_amdgcn_s_sleep(SLT);
        u32x4 km[4];
        const int l7 = ltid() & 63;
#pragma unroll
        for (int t = 0; t < 4; ++t) km[t] = *(const u32x4*)(qk + 2176 + (l7 & 7) * 272 + t * 64 + (l7 >> 4) * 16);
        wave_t1(wr, qring + (s % 3) * 8192, kf, km, s, h, l7);
      }
      TS(5);
      TSOUT(s);
      if (wid < 7) __builtin_amdgcn_s_sleep(SLA);
    }
    WAIT_V(0);
  } else {
    const int b = role - 32;
    const bf16* U = (const bf16*)(ws + WS_U) + (size_t)b * NH * D_ * 8;
    load_wfrags([&](int half) { return U; }, [&](int k, int cc) { return (unsigned)(cc * D_ + k) * 16u; }, 64 * 16, w, sm, tid);
    float* red = (float*)(sm + 10240);
    char* vring = sm + 16384;
    char* pbuf = sm + 16384 + 61440;
    bf16* VO = (bf16*)(ws + WS_VO) + (size_t)b * 20 * 512;
    const int ng = wid < 6 ? 3 : 2;
    auto vdma = [&](int s) {
      const bf16* src = VO + (size_t)s * 80 * 20 * 512;
      const unsigned d = lds_addr(vring) + (s % 3) * 20480;
#pragma unroll
      for (int i = 0; i < 20; ++i) glds16c(src + i * 512, lane * 16, d + i * 1024);
    };
    if (wid == 7) { dep_poll(wr, C_V, 32); vdma(0); vdma(1); }
    if (tid < 128) red[(tid >> 1) * 20 + 14 + (tid & 1)] = 0.f;
    for (int s = 0; s < NSEG; ++s) {
      char* vt = vring + (s % 3) * 20480;
      TSDECL; TS(0);
      if (SLM) __builtin_amdgcn_s_sleep(SLM);
      poll_mem<false>(ws + WS_XCH, s, par, memA, memA, wid, lane);
      if (wid == 7) {
        if (VPOS == 2) { __builtin_amdgcn_s_sleep(SLV); vdma(s + 2 < NSEG ? s + 2 : s + 2 - 3); wait_v<40>(); } else { WAIT_V(20); }
      }
      TS(1);
      LBAR();
      TS(2);
      { f32x4 v = mem_times_w<10>(memA, w, lane);
        const int O = 5 * ((2 * wid + (lane >> 5)) * 2 + (fr >> 3));
        *(u32x2*)(vt + (O >> 3) * 1024 + ((O & 3) * 16 + ((O >> 2) & 1) * 8 + (fr & 7)) * 16 + (fq & 1) * 8) = u32x2{pack2(v[0], v[1]), pack2(v[2], v[3])}; }
      TS(3);
      LBAR();
      TS(4);
      if (wid == 7) {
#pragma unroll
        for (int j = 0; j < 4; ++j) {
          const int i = j * 64 + lane, O = 5 * (i >> 3);
          const int off = (O >> 3) * 1024 + ((O & 3) * 16 + ((O >> 2) & 1) * 8 + (i & 7)) * 16;
          stc(wr, WS_VO, (s * 80 + b) * 20480 + off, *(const u32x4*)(vt + off));
        }
        if (!((s + 3) & 3) && s + 3 < NSEG) dep_poll(wr, C_V + ((s + 3) >> 2) * 256, 32);
        if (VPOS == 0) { WAIT_L0(); vdma(s + 2 < NSEG ? s + 2 : s + 2 - 3); }
      } else if (s < NSEG - 1) {
        unsigned off[3]; u32x4 vf[3];
#pragma unroll
        for (int i = 0; i < 3; ++i) {
          const int g = i < ng ? wid + 7 * i : wid;
          off[i] = xoff(s, 10240 + g * 1024 + lane * 16, par);
          vf[i] = *(const u32x4*)(vt + g * 1024 + lane * 16);
        }
        __builtin_amdgcn_s_sleep(SLB);
        char* const dst[3] = {pbuf + wid * 3072, pbuf + wid * 3072 + 1024, pbuf + wid * 3072 + 2048};
        u32x4 pv[3];
        if (DL == 1) poll_lds1<3>(ws + WS_XCH, off, dst, lane, pv); else poll_lds<3, 2, STL>(ws + WS_XCH, off, dst, lane, pv);
        TS(5);
        f32x4 acc = ZERO4;
#pragma unroll
        for (int i = 0; i < 3; ++i) if (i < ng) acc = mfma16(pv[i], vf[i], acc);
        if ((fr >> 3) == (fq >> 1)) {
#pragma unroll
          for (int r = 0; r < 4; ++r) red[(((fq & 1) * 4 + r) * 8 + (fr & 7)) * 20 + wid * 2 + (fr >> 3)] = acc[r];
        }
      }
      if (s < NSEG - 1) {
        LBAR();
        TS(6);
        if (s >= 48 && wid == 7) { WAIT_V(0); if (lane == 0) __builtin_amdgcn_raw_ptr_buffer_atomic_add_i32(1, wr, 0, C_SEG + s * 256, 16); }
        if (VPOS == 1 && wid == 7) { __builtin_amdgcn_s_sleep(SLV); vdma(s + 2 < NSEG ? s + 2 : s + 2 - 3); }
        if (wid == 0) {
          const f32x4* rp = (const f32x4*)(red + lane * 20);
          f32x4 t0 = rp[0] + rp[1], t1 = rp[2] + rp[3]; t0 += t1;
          float v = (t0[0] + t0[1]) + (t0[2] + t0[3]);
          const float nb = __uint_as_float(__builtin_amdgcn_mov_dpp(__float_as_uint(v), 0xB1, 0xf, 0xf, true));
          u32x4 o; o.x = canon(pack2(v, nb));
          o.y = __builtin_amdgcn_mov_dpp(o.x, 0x4E, 0xf, 0xf, true);
          o.z = __builtin_amdgcn_mov_dpp(o.x, 0x104, 0xf, 0xf, true);
          o.w = __builtin_amdgcn_mov_dpp(o.y, 0x104, 0xf, 0xf, true);
          if (!(lane & 7)) {
#pragma unroll
            for (int c = 0; c < NC; ++c) __builtin_amdgcn_raw_buffer_store_b128(o, xr, xoff((s + 1) * NC + c, b * 128 + lane * 2, par), 0, 16);
          }
        }
      }
      TS(7); TSOUT(s);
    }
    WAIT_V(0);
  }
  LBAR();
  if (tid == 0) __hip_atomic_fetch_add((unsigned*)(ws + C_FIN), 1u, __ATOMIC_RELAXED, __HIP_MEMORY_SCOPE_AGENT);
  }
  const bf16* xn = (const bf16*)(ws + WS_XN);
  unsigned* bc = (unsigned*)(sm + 147456);
  const int g0 = res & 3;
  int qi = slot < 112 ? 4 : 0;
  constexpr unsigned NPQ = 148, NPOST = 640 + 12 * 19 + 94;
  auto deq = [&](int q) { return __hip_atomic_fetch_add((unsigned*)(ws + C_QH + (q < 4 ? ((g0 + q) & 3) : 4) * 256), 1u, __ATOMIC_RELAXED, __HIP_MEMORY_SCOPE_AGENT); };
  unsigned nxt = res < 4 ? bi + 4 : bi;
  if (slot < 112 && ltid() == 0) nxt = deq(qi);
  for (;;) {
    if (ltid() == 0) *bc = nxt;
    LBAR();
    unsigned t = __builtin_amdgcn_readfirstlane(*bc);
    qi = __builtin_amdgcn_readfirstlane(qi);
    if (qi < 4 && t >= NPQ) { ++qi; BAR(); if (ltid() == 0) nxt = deq(qi); continue; }
    if (qi == 4 && t >= NPOST) break;
    const bool pref = qi < 4 && t >= 60 && t + 24 < NPQ;
    if (pref && ltid() == 0) nxt = deq(qi);
    const int tl = ltid();
    unsigned sig, inc = 1;
    if (qi < 4) {
      const int hb = ((g0 + qi) & 3) * 8;
      int rb, j, nv, nh;
      if (t < 32) { rb = t >> 4; j = t & 15; nv = 1; nh = 2; } else if (t < 44) { rb = 2; j = t - 32; nv = 2; nh = 2; }
      else { rb = 3 + ((t - 44) >> 3); j = (t - 44) & 7; nv = 2; nh = 4; }
      rb = __builtin_amdgcn_readfirstlane(rb); j = __builtin_amdgcn_readfirstlane(j); nv = __builtin_amdgcn_readfirstlane(nv); nh = __builtin_amdgcn_readfirstlane(nh);
      if (nv == 1 && j < 8) { task_vvo<1>(xn, Wv, Wo, wr, rb, hb + j, sm, tl); sig = C_V + rb * 256; }
      else if (nv == 2 && j < 4) { task_vvo<2>(xn, Wv, Wo, wr, rb, hb + 2 * j, sm, tl); sig = C_V + rb * 256; inc = 2; }
      else {
        const int hj = (j - (nv == 1 ? 8 : 4)) * nh;
        task_kq(xn, hj < 8 ? Wk : Wq, wr, hj < 8 ? (unsigned)WS_K : (unsigned)WS_Q, rb, hb + (hj & 7), nh, sm, tl);
        sig = (hj < 8 ? C_K : C_Q) + rb * 256; inc = nh;
      }
    } else {
      int stg = 0; unsigned sbase = 0;
      for (;;) {
        const unsigned sz = (stg == 12 ? 94 : 0) + 40 + (stg >= 1 && stg < 13 ? 14 : 0) + (stg >= 2 && stg < 14 ? 5 : 0);
        if (t - sbase < sz) break;
        sbase += sz; ++stg;
      }
      stg = __builtin_amdgcn_readfirstlane(stg); sbase = __builtin_amdgcn_readfirstlane(sbase);
      t -= sbase;
      int ty = -1, ix = 0;
      auto ent = [&](int k, int id, unsigned n) { if (ty < 0) { if (t < n) { ty = k; ix = id; } else t -= n; } };
      ent(4, 0, stg == 12 ? 54 : 0); ent(5, 0, stg == 12 ? 40 : 0);
#pragma unroll
      for (int i = 0; i < 4; ++i) ent(1, stg * 4 + i, 10);
      ent(2, stg - 1, stg >= 1 && stg < 13 ? 14 : 0); ent(3, stg - 2, stg >= 2 && stg < 14 ? 5 : 0);
      ty = __builtin_amdgcn_readfirstlane(ty); ix = __builtin_amdgcn_readfirstlane(ix); t = __builtin_amdgcn_readfirstlane(t);
      sig = C_FIN + 128;
      if (ty == 1) {
        task_t2(x, wr, Dep{C_V + (ix >> 2) * 256, 32, ix < 48 ? C_PROG : ix < 63 ? C_SEG + ix * 256 : C_FIN, ix < 48 ? ix + 3u : 112u}, bc, ix, t, sm, tl);
        sig = ix < 48 ? C_T2 + (ix >> 2) * 256 : C_T2S + ix * 256;
      } else if (ty == 2) {
        task_f1(wr, Dep{C_T2 + ix * 256, 40, 0, 0}, (const float*)(ws + WS_CS), Wg, Wu, ix, t * 2, t < 13 ? 2 : 1, sm, tl); sig = C_F1 + ix * 256;
      } else if (ty == 3) {
        dep_wait(wr, Dep{C_F1 + ix * 256, 14, 0, 0}, wid);
        task_f2((const bf16*)(ws + WS_G), Wd, wr, out, ix, t, sm, tl);
      } else if (ty == 4) {
        task_f1p(wr, (const float*)(ws + WS_CS), Wg, Wu, t, sm, tl);
      } else {
        task_f2p(wr, Wd, out, t % 20, t / 20, sm, tl);
      }
    }
    WAIT_V(0);
    LBAR();
    if (ltid() == 0) {
      __hip_atomic_fetch_add((unsigned*)(ws + sig), inc, __ATOMIC_RELAXED, __HIP_MEMORY_SCOPE_AGENT);
      if (!pref) nxt = deq(qi);
    }
  }
}

extern "C" void kernel_launch(void* const* d_in, const int* in_sizes, int n_in, void* d_out, int out_size, void* d_ws, size_t ws_size, hipStream_t stream) {
  const bf16 *x = (const bf16*)d_in[0], *mem0 = (const bf16*)d_in[1], *Wq = (const bf16*)d_in[2], *Wk = (const bf16*)d_in[3], *Wv = (const bf16*)d_in[4], *Wo = (const bf16*)d_in[5];
  const bf16 *Wg = (const bf16*)d_in[8], *Wu = (const bf16*)d_in[9], *Wd = (const bf16*)d_in[10];
  char* ws = (char*)(((size_t)d_ws + (2 * MB - 1)) & ~(size_t)(2 * MB - 1));
  (void)hipFuncSetAttribute((const void*)k_prologue, hipFuncAttributeMaxDynamicSharedMemorySize, 131072);
  (void)hipFuncSetAttribute((const void*)k_main, hipFuncAttributeMaxDynamicSharedMemorySize, LDS_MAIN);
  k_prologue<<<76 + 160, 512, 131072, stream>>>(x, mem0, Wv, Wo, Wg, Wu, ws);
  k_main<<<256, 512, LDS_MAIN, stream>>>(x, Wq, Wk, Wv, Wo, Wg, Wu, Wd, ws, (bf16*)d_out);
}
```

```cpp
#define NO_KM
#include <hip/hip_runtime.h>
#include <hip/hip_bf16.h>

typedef __hip_bfloat16 bf16;
typedef __bf16 bf16x8 __attribute__((ext_vector_type(8)));
typedef __bf16 bf16x16 __attribute__((ext_vector_type(16)));
typedef float f32x4 __attribute__((ext_vector_type(4)));
typedef unsigned u32x4 __attribute__((ext_vector_type(4)));
typedef unsigned u32x2 __attribute__((ext_vector_type(2)));
typedef unsigned u32x8 __attribute__((ext_vector_type(8)));
typedef short s16x4 __attribute__((ext_vector_type(4)));
typedef __attribute__((address_space(3))) s16x4* lds_s16x4_p;

constexpr int S_ = 2048, D_ = 640, NH = 32, DH = 128, HD = 4096, FF = 1728, NSEG = 64;
constexpr size_t MB = 1u << 20;
#define NC 1
constexpr size_t WS_XCH = 0;
constexpr size_t WS_LP = 4 * MB;
constexpr size_t WS_CTL = 16 * MB;
constexpr size_t WS_Q = 17 * MB;
constexpr size_t WS_K = WS_Q + 16 * MB;
constexpr size_t WS_U = WS_K + 16 * MB;
constexpr size_t WS_VO = WS_U + 25 * MB;
constexpr size_t WS_KM = WS_VO + 100 * MB;
constexpr size_t WS_P = WS_KM + 4 * MB;
constexpr size_t WS_H = WS_P + 5 * MB;
constexpr size_t WS_G = WS_H + 3 * MB;
constexpr size_t WS_XN = WS_G + 7 * MB;
constexpr size_t WS_ST = WS_XN + 3 * MB;
constexpr size_t WS_CS = WS_ST + 1 * MB;
constexpr size_t WS_END = WS_CS + 1 * MB;

#define SLD 8
#define SLP 4
#define T2H 0u
constexpr unsigned C_QH = WS_CTL + 256 * 1, C_PROG = WS_CTL + 256 * 6, C_FIN = WS_CTL + 256 * 7, C_K = WS_CTL + 256 * 8, C_V = WS_CTL + 256 * 24,
                   C_Q = WS_CTL + 256 * 40, C_T2 = WS_CTL + 256 * 56, C_F1 = WS_CTL + 256 * 72, C_T1 = WS_CTL + 256 * 88,
                   C_T2S = WS_CTL + 256 * (152 - 48), C_F1S = WS_CTL + 256 * (168 - 48), C_SEG = WS_CTL + 256 * (184 - 48), C_END = WS_CTL + 256 * 200;
constexpr unsigned C_FILL = C_FIN;
#define PSEG 48
constexpr int PS = PSEG, PSTG = PS / 4, NFILL = NSEG - PS;
constexpr unsigned SENT = 0xFFFFFFFFu;
constexpr unsigned SPIN_CAP = 1u << 18;

__device__ __forceinline__ unsigned pack2(float lo, float hi) {
  typedef __bf16 b2 __attribute__((ext_vector_type(2)));
  typedef float f2 __attribute__((ext_vector_type(2)));
  return __builtin_bit_cast(unsigned, __builtin_convertvector(f2{lo, hi}, b2));
}
__device__ __forceinline__ u32x4 pack8(f32x4 a, f32x4 b) { return u32x4{pack2(a[0], a[1]), pack2(a[2], a[3]), pack2(b[0], b[1]), pack2(b[2], b[3])}; }
__device__ __forceinline__ float bflo(unsigned w) { return __uint_as_float(w << 16); }
__device__ __forceinline__ float bfhi(unsigned w) { return __uint_as_float(w & 0xffff0000u); }
__device__ __forceinline__ unsigned canon(unsigned w) { return min(w, 0xFFFFFFFEu); }
__device__ __forceinline__ u32x4 canon4(u32x4 v) { return u32x4{canon(v.x), canon(v.y), canon(v.z), canon(v.w)}; }
__device__ __forceinline__ unsigned max4(u32x4 v) { return max(max(v.x, v.y), max(v.z, v.w)); }
__device__ __forceinline__ void glds16(const void* base, unsigned voff, unsigned lds) {
  asm volatile("s_mov_b32 m0, %2\n\ts_nop 0\n\tglobal_load_lds_dwordx4 %0, %1" ::"v"(voff), "s"(base), "s"(__builtin_amdgcn_readfirstlane(lds)) : "m0");
}
__device__ __forceinline__ void glds16c(const void* base, unsigned voff, unsigned lds) {
  asm volatile("s_mov_b32 m0, %2\n\ts_nop 0\n\tglobal_load_lds_dwordx4 %0, %1 sc1" ::"v"(voff), "s"(base), "s"(__builtin_amdgcn_readfirstlane(lds)) : "m0");
}
typedef __amdgpu_buffer_rsrc_t rsrc_t;
__device__ __forceinline__ u32x4 ldc(rsrc_t r, unsigned base, unsigned off) { return __builtin_amdgcn_raw_buffer_load_b128(r, off, base, 16); }
__device__ __forceinline__ u32x2 ldc2(rsrc_t r, unsigned base, unsigned off) { return __builtin_amdgcn_raw_buffer_load_b64(r, off, base, 16); }
__device__ __forceinline__ void stc(rsrc_t r, unsigned base, unsigned off, u32x4 v) {
  __builtin_amdgcn_raw_buffer_store_b128(v, r, off, base, 16);
  asm volatile("s_nop 1" ::"v"(v));
}
__device__ __forceinline__ void stc2(rsrc_t r, unsigned base, unsigned off, u32x2 v) { __builtin_amdgcn_raw_buffer_store_b64(v, r, off, base, 16); }
template <int N> __device__ __forceinline__ void wait_v() { asm volatile("s_waitcnt vmcnt(%0)" ::"n"(N) : "memory"); }
__device__ __forceinline__ unsigned lds_addr(const void* p) { return (unsigned)(size_t)(const __attribute__((address_space(3))) char*)p; }
__device__ __forceinline__ u32x2 tr_read(const char* p) {
  s16x4 r = __builtin_amdgcn_ds_read_tr16_b64_v4i16((lds_s16x4_p)(p));
  return __builtin_bit_cast(u32x2, r);
}
#define WAIT_V(n) asm volatile("s_waitcnt vmcnt(" #n ")" ::: "memory")
#define WAIT_L0() asm volatile("s_waitcnt lgkmcnt(0)" ::: "memory")
#define BAR() asm volatile("s_barrier" ::: "memory")
#define LBAR() asm volatile("s_waitcnt lgkmcnt(0)\n\ts_barrier" ::: "memory")
__device__ __forceinline__ f32x4 mfma16(u32x4 a, u32x4 b, f32x4 c) {
  return __builtin_amdgcn_mfma_f32_16x16x32_bf16(__builtin_bit_cast(bf16x8, a), __builtin_bit_cast(bf16x8, b), c, 0, 0, 0);
}
__device__ __forceinline__ f32x4 smf(u32x4 a, u32x8 b, f32x4 c, int idx) {
  return __builtin_amdgcn_smfmac_f32_16x16x64_bf16(__builtin_bit_cast(bf16x8, a), __builtin_bit_cast(bf16x16, b), c, idx, 0, 0);
}
__device__ __forceinline__ unsigned xcc_id() { return (unsigned)__builtin_amdgcn_s_getreg(20 | (3 << 11)) & 0xfu; }

__device__ __forceinline__ unsigned dpp_ror8(unsigned v) { return __builtin_amdgcn_mov_dpp(v, 0x128, 0xf, 0xf, true); }
__device__ __forceinline__ float red_max_q(float v) {
  auto a = __builtin_amdgcn_permlane16_swap(__float_as_uint(v), __float_as_uint(v), false, false);
  v = fmaxf(__uint_as_float(a[0]), __uint_as_float(a[1]));
  auto b = __builtin_amdgcn_permlane32_swap(__float_as_uint(v), __float_as_uint(v), false, false);
  return fmaxf(__uint_as_float(b[0]), __uint_as_float(b[1]));
}
__device__ __forceinline__ float red_sum_q(float v) {
  auto a = __builtin_amdgcn_permlane16_swap(__float_as_uint(v), __float_as_uint(v), false, false);
  v = __uint_as_float(a[0]) + __uint_as_float(a[1]);
  auto b = __builtin_amdgcn_permlane32_swap(__float_as_uint(v), __float_as_uint(v), false, false);
  return __uint_as_float(b[0]) + __uint_as_float(b[1]);
}

__device__ __forceinline__ float red_sum8(float v) {
  v += __uint_as_float(__builtin_amdgcn_mov_dpp(__float_as_uint(v), 0xB1, 0xf, 0xf, true));
  v += __uint_as_float(__builtin_amdgcn_mov_dpp(__float_as_uint(v), 0x4E, 0xf, 0xf, true));
  return v + __uint_as_float(__builtin_amdgcn_mov_dpp(__float_as_uint(v), 0x141, 0xf, 0xf, true));
}
__device__ __forceinline__ int bswz(int row) { return (((row >> 2) ^ (row >> 3)) & 1) << 1; }
__device__ __forceinline__ void stageB_rc(int p, int& row, int& ch) {
  row = ((p >> 7) << 3) | ((p >> 2) & 7);
  ch = (((p >> 5) & 3) << 2) | ((p & 3) ^ bswz(row));
}
template <bool PERM>
__device__ __forceinline__ unsigned trB_base(int lane, int j) {
  const int g = lane >> 4, q = (lane & 15) >> 2, p = lane & 3;
  return PERM ? 2048 * (g >> 1) + 64 * (4 * (g & 1) + q) + 8 * p + 32 * ((j ^ g ^ (g >> 1)) & 1) : 2048 * g + 64 * q + 8 * p + 32 * ((j ^ g) & 1);
}
__device__ __forceinline__ u32x2 tr_read_a(unsigned a) {
  s16x4 r = __builtin_amdgcn_ds_read_tr16_b64_v4i16((lds_s16x4_p)(size_t)a);
  return __builtin_bit_cast(u32x2, r);
}

template <bool SW, bool PERM, int CSTR>
__device__ __forceinline__ void mma_tile(const u32x4& a00, const u32x4& a10, const u32x4& a01, const u32x4& a11, unsigned tb0, unsigned tb1, int so, f32x4 (&acc)[2][4]) {
#pragma unroll
  for (int ks = 0; ks < 2; ++ks) {
    u32x4 b[4];
#pragma unroll
    for (int n = 0; n < 4; ++n) {
      const int o = so + 8192 * ks + CSTR * (n >> 1);
      u32x2 lo = tr_read_a(((n & 1) ? tb1 : tb0) + o), hi = tr_read_a((PERM ? ((n & 1) ? tb1 : tb0) + 4096 : ((n & 1) ? tb0 : tb1) + 256) + o);
      b[n] = u32x4{lo.x, lo.y, hi.x, hi.y};
    }
    const u32x4 a0 = ks ? a01 : a00, a1 = ks ? a11 : a10;
#pragma unroll
    for (int n = 0; n < 4; ++n) {
      acc[0][n] = SW ? mfma16(b[n], a0, acc[0][n]) : mfma16(a0, b[n], acc[0][n]);
      acc[1][n] = SW ? mfma16(b[n], a1, acc[1][n]) : mfma16(a1, b[n], acc[1][n]);
    }
  }
}

__device__ __forceinline__ void ln_tile(const bf16* src  , bf16* dst, int lane) {
  const int fr = lane & 15, fq = lane >> 4;
  u32x4 af[20];
#pragma unroll
  for (int s = 0; s < 20; ++s) af[s] = *(const u32x4*)(src + (size_t)fr * D_ + s * 32 + fq * 8);
  float s = 0.f;
#pragma unroll
  for (int i = 0; i < 20; ++i)
#pragma unroll
    for (int e = 0; e < 4; ++e) s += bflo(af[i][e]) + bfhi(af[i][e]);
  s += __shfl_xor(s, 16); s += __shfl_xor(s, 32);
  const float mean = s * (1.f / 640.f);
  float q = 0.f;
#pragma unroll
  for (int i = 0; i < 20; ++i)
#pragma unroll
    for (int e = 0; e < 4; ++e) { float a = bflo(af[i][e]) - mean, b = bfhi(af[i][e]) - mean; q += a * a + b * b; }
  q += __shfl_xor(q, 16); q += __shfl_xor(q, 32);
  const float rstd = 1.f / sqrtf(q * (1.f / 640.f) + 1e-5f);
#pragma unroll
  for (int i = 0; i < 20; ++i) {
    u32x4 o;
#pragma unroll
    for (int e = 0; e < 4; ++e) o[e] = pack2((bflo(af[i][e]) - mean) * rstd, (bfhi(af[i][e]) - mean) * rstd);
    *(u32x4*)(dst + i * 512 + lane * 8) = o;
  }
}
__device__ __forceinline__ void load_af(const bf16* src, u32x4 (&af)[2][20], int lane) {
#pragma unroll
  for (int s = 0; s < 20; ++s)
#pragma unroll
    for (int m = 0; m < 2; ++m) af[m][s] = *(const u32x4*)(src + (m * 20 + s) * 512 + lane * 8);
}

__device__ __forceinline__ void load_afc(rsrc_t r, unsigned off, u32x4 (&af)[2][20], int lane) {
#pragma unroll
  for (int s = 0; s < 20; ++s)
#pragma unroll
    for (int m = 0; m < 2; ++m) af[m][s] = ldc(r, off, (m * 20 + s) * 1024 + lane * 16);
}
__device__ __forceinline__ void dep_poll(rsrc_t r, unsigned off, unsigned target) {
  const unsigned t0 = (unsigned)__builtin_amdgcn_s_memrealtime();
  while ((unsigned)__builtin_amdgcn_readfirstlane(__builtin_amdgcn_raw_buffer_load_b32(r, 0, off, 16)) < target) {
    if ((unsigned)__builtin_amdgcn_s_memrealtime() - t0 > 5000000u) break;
    __builtin_amdgcn_s_sleep(SLD);
  }
}
struct Dep { unsigned o0, t0, o1, t1; };
__device__ __forceinline__ void dep_wait(rsrc_t r, const Dep& d, int wid) {
  if (wid == 0) { dep_poll(r, d.o0, d.t0); if (d.t1) dep_poll(r, d.o1, d.t1); }
  BAR();
}
__device__ __forceinline__ bool dep_first(rsrc_t r, const Dep& d, int wid, unsigned* bc) {
  if (wid == 0) {
    const unsigned a = __builtin_amdgcn_raw_buffer_load_b32(r, 0, d.o0, 16), b = __builtin_amdgcn_raw_buffer_load_b32(r, 0, d.o1, 16);
    if ((unsigned)__builtin_amdgcn_readfirstlane(a) < d.t0) dep_poll(r, d.o0, d.t0);
    bc[1] = b;
  }
  LBAR();
  return (unsigned)__builtin_amdgcn_readfirstlane(bc[1]) >= d.t1;
}
__device__ __forceinline__ void dep_second(rsrc_t r, const Dep& d, int wid) {
  if (wid == 0) dep_poll(r, d.o1, d.t1);
  BAR();
}
constexpr int RING = 5;
#define ZERO4 f32x4{0.f, 0.f, 0.f, 0.f}
__device__ __forceinline__ void ring_wait(int t, int T) {
  const int rem = T - 1 - t;
  if (rem >= 3) { WAIT_V(6); } else if (rem == 2) { WAIT_V(4); } else if (rem == 1) { WAIT_V(2); } else { WAIT_V(0); }
  LBAR();
}

__device__ __forceinline__ void task_kq(const bf16* x, const bf16* W, rsrc_t wr, unsigned out, int rb, int h0, int nh, char* sm, int tid) {
  const int lane = tid & 63, wid = __builtin_amdgcn_readfirstlane(tid >> 6), wm = wid >> 1, wn = wid & 1;
  const unsigned lw = lds_addr(sm) + wid * 1024;
  const unsigned tb0 = lds_addr(sm) + wn * 1024 + trB_base<false>(lane, 0), tb1 = lds_addr(sm) + wn * 1024 + trB_base<false>(lane, 1);
  int r0, c0, r1, c1; stageB_rc(tid, r0, c0); stageB_rc(tid + 512, r1, c1);
  const unsigned o0 = (r0 * HD + c0 * 8) * 2, o1 = (r1 * HD + c1 * 8) * 2;
  const int T = nh * 10;
  auto issue = [&](int hh, int u) {
    const bf16* b = W + (size_t)(u * 64) * HD + (h0 + hh) * DH;
    const unsigned d = lw + (u % RING) * 16384;
    glds16(b, o0, d); glds16(b, o1, d + 8192);
  };
#pragma unroll
  for (int u = 0; u < RING - 1; ++u) issue(0, u);
  u32x4 af[2][20];
  load_af(x + (size_t)(rb * 8 + wm * 2) * 10240, af, lane);
  for (int hh = 0; hh < nh; ++hh) {
    f32x4 acc[2][4] = {};
#pragma unroll
    for (int u = 0; u < 10; ++u) {
      if (u < 4) { WAIT_V(10); LBAR(); } else ring_wait(hh * 10 + u, T);
      if (u + RING - 1 < 10) issue(hh, u + RING - 1); else if (hh + 1 < nh) issue(hh + 1, u + RING - 1 - 10);
      mma_tile<true, false, 512>(af[0][2 * u], af[1][2 * u], af[0][2 * u + 1], af[1][2 * u + 1], tb0, tb1, (u % RING) * 16384, acc);
    }
    const unsigned o = out + (((rb * 4 + wm) * NH + h0 + hh) * 8 + 2 * wn) * 1024;
#pragma unroll
    for (int m = 0; m < 2; ++m)
#pragma unroll
      for (int t = 0; t < 2; ++t) stc(wr, o, (m * 4 + t) * 1024 + lane * 16, pack8(acc[m][2 * t], acc[m][2 * t + 1]));
  }
}

constexpr int vvo_allow(int u, int T) {
  int n = 0;
  for (int j = u + 1; j <= u + 3 && j < T; ++j) n += 2;
  for (int j = u - 4; j < u; ++j) if (j >= T / 2 && (j & 1)) n += 4;
  return n;
}
template <int NHH>
__device__ __forceinline__ void task_vvo(const bf16* x, const bf16* Wv, const bf16* Wo, rsrc_t wr, int rb, int h0, char* sm, int tid) {
  const int lane = tid & 63, wid = __builtin_amdgcn_readfirstlane(tid >> 6), wm = wid >> 1, wn = wid & 1;
  const unsigned lw = lds_addr(sm) + wid * 1024;
  const unsigned tb0 = lds_addr(sm) + wn * 1024 + trB_base<false>(lane, 0), tb1 = lds_addr(sm) + wn * 1024 + trB_base<false>(lane, 1);
  const unsigned tp0 = lds_addr(sm) + wn * 1024 + trB_base<true>(lane, 0), tp1 = lds_addr(sm) + wn * 1024 + trB_base<true>(lane, 1);
  char* vbuf = sm + RING * 16384;
  int r0, c0, r1, c1; stageB_rc(tid, r0, c0); stageB_rc(tid + 512, r1, c1);
  const unsigned o0 = (r0 * HD + c0 * 8) * 2, o1 = (r1 * HD + c1 * 8) * 2, p0 = (r0 * D_ + c0 * 8) * 2, p1 = (r1 * D_ + c1 * 8) * 2;
  constexpr int T = NHH * 20;
  auto issue = [&](int t) {
    const unsigned d = lw + (t % RING) * 16384;
    if (t < NHH * 10) {
      const bf16* b = Wv + (size_t)((t % 10) * 64) * HD + (h0 + t / 10) * DH;
      glds16(b, o0, d); glds16(b, o1, d + 8192);
    } else {
      const int v = (t - NHH * 10) % 10, h = h0 + (t - NHH * 10) / 10;
      const bf16* b = Wo + (size_t)(h * DH + (v & 1) * 64) * D_ + (v >> 1) * 128;
      glds16(b, p0, d); glds16(b, p1, d + 8192);
    }
  };
#pragma unroll
  for (int u = 0; u < RING - 1; ++u) issue(u);
  {
    u32x4 af[2][20];
    load_af(x + (size_t)(rb * 8 + wm * 2) * 10240, af, lane);
#pragma unroll
    for (int hh = 0; hh < NHH; ++hh) {
      f32x4 acc[2][4] = {};
#pragma unroll
      for (int u = 0; u < 10; ++u) {
        ring_wait(hh * 10 + u, T);
        issue(hh * 10 + u + RING - 1);
        mma_tile<true, false, 512>(af[0][2 * u], af[1][2 * u], af[0][2 * u + 1], af[1][2 * u + 1], tb0, tb1, (u % RING) * 16384, acc);
      }
#pragma unroll
      for (int m = 0; m < 2; ++m)
#pragma unroll
        for (int t = 0; t < 2; ++t) *(u32x4*)(vbuf + (((hh * 4 + wm) * 2 + m) * 4 + 2 * wn + t) * 1024 + lane * 16) = pack8(acc[m][2 * t], acc[m][2 * t + 1]);
    }
  }
#pragma unroll
  for (int hh = 0; hh < NHH; ++hh) {
    const int h = h0 + hh;
    const int seg = rb * 4 + wm, O = 5 * h + 1 + (lane >> 4);
    const unsigned vo = ((seg * 80 * 20 + (O >> 3)) * 64 + (O & 3) * 16 + ((O >> 2) & 1) * 8 + (lane & 7)) * 16 + ((lane >> 3) & 1) * 20480;
    u32x4 vf[2][4];
#pragma unroll
    for (int nt = 0; nt < 5; ++nt) {
      f32x4 acc[2][4] = {};
#pragma unroll
      for (int kh = 0; kh < 2; ++kh) {
        const int u = NHH * 10 + hh * 10 + nt * 2 + kh;
        switch (vvo_allow(u, T)) {
          case 14: WAIT_V(14); break; case 12: WAIT_V(12); break; case 10: WAIT_V(10); break; case 8: WAIT_V(8); break;
          default: WAIT_V(6); break;
        }
        LBAR();
        if (u + RING - 1 < T) issue(u + RING - 1);
        if (nt == 0 && kh == 0) {
#pragma unroll
          for (int m = 0; m < 2; ++m)
#pragma unroll
            for (int t = 0; t < 4; ++t) vf[m][t] = *(const u32x4*)(vbuf + (((hh * 4 + wm) * 2 + m) * 4 + t) * 1024 + lane * 16);
        }
        mma_tile<false, true, 512>(vf[0][2 * kh], vf[1][2 * kh], vf[0][2 * kh + 1], vf[1][2 * kh + 1], tp0, tp1, (u % RING) * 16384, acc);
      }
#pragma unroll
      for (int n = 0; n < 4; ++n)
        stc(wr, WS_VO + (nt * 16 + wn * 8 + n * 2) * 20480, vo, pack8(acc[0][n], acc[1][n]));
    }
  }
}

__device__ __forceinline__ void task_u(const bf16* Wv, const bf16* Wo, bf16* U, int h, int db, char* sm, int tid) {
  const int lane = tid & 63, wid = __builtin_amdgcn_readfirstlane(tid >> 6), wm = wid >> 1, wn = wid & 1;
  const unsigned lw = lds_addr(sm) + wid * 1024;
  const unsigned tb0 = lds_addr(sm) + wn * 1024 + trB_base<false>(lane, 0), tb1 = lds_addr(sm) + wn * 1024 + trB_base<false>(lane, 1);
  int r0, c0, r1, c1; stageB_rc(tid, r0, c0); stageB_rc(tid + 512, r1, c1);
  const unsigned p0 = (r0 * D_ + c0 * 8) * 2, p1 = (r1 * D_ + c1 * 8) * 2;
  auto issue = [&](int v) {
    const unsigned d = lw + (v % RING) * 16384;
    const bf16* b = Wo + (size_t)(h * DH + (v & 1) * 64) * D_ + (v >> 1) * 128;
    glds16(b, p0, d); glds16(b, p1, d + 8192);
  };
#pragma unroll
  for (int u = 0; u < RING - 1; ++u) issue(u);
  u32x4 vf[2][4];
#pragma unroll
  for (int m = 0; m < 2; ++m)
#pragma unroll
    for (int s = 0; s < 4; ++s) vf[m][s] = *(const u32x4*)(Wv + (size_t)(db * 128 + wm * 32 + m * 16 + (lane & 15)) * HD + h * DH + s * 32 + (lane >> 4) * 8);
#pragma unroll
  for (int nt = 0; nt < 5; ++nt) {
    f32x4 acc[2][4] = {};
#pragma unroll
    for (int kh = 0; kh < 2; ++kh) {
      const int v = nt * 2 + kh;
      ring_wait(v, 10);
      if (v + RING - 1 < 10) issue(v + RING - 1);
      mma_tile<true, false, 512>(vf[0][2 * kh], vf[1][2 * kh], vf[0][2 * kh + 1], vf[1][2 * kh + 1], tb0, tb1, (v % RING) * 16384, acc);
    }
#pragma unroll
    for (int m = 0; m < 2; ++m)
#pragma unroll
      for (int t = 0; t < 2; ++t) {
        const int d1 = db * 128 + wm * 32 + m * 16 + (lane & 15);
        const int c8 = nt * 16 + wn * 8 + t * 4 + ((lane >> 4) & 1) * 2 + (lane >> 5);
        auto s0 = __builtin_amdgcn_permlane16_swap(pack2(acc[m][2 * t][0], acc[m][2 * t][1]), pack2(acc[m][2 * t + 1][0], acc[m][2 * t + 1][1]), false, false);
        auto s1 = __builtin_amdgcn_permlane16_swap(pack2(acc[m][2 * t][2], acc[m][2 * t][3]), pack2(acc[m][2 * t + 1][2], acc[m][2 * t + 1][3]), false, false);
        *(u32x4*)(U + (((size_t)c8 * NH + h) * D_ + d1) * 8) = u32x4{s0[0], s1[0], s0[1], s1[1]};
      }
  }
  LBAR();
}

__device__ __forceinline__ void wave_t1(rsrc_t wr, const char* ql, const u32x4 (&kf)[2][4], const u32x4 (&km)[4], int seg, int h, int lane) {
  const int fr = lane & 15, fq = lane >> 4;
  const float cs = 0.08838834764831845f * 1.4426950408889634f;
#pragma unroll
  for (int i = 0; i < 2; ++i) {
    f32x4 sc[3] = {};
#pragma unroll
    for (int t = 0; t < 4; ++t) {
      const u32x4 qf = *(const u32x4*)(ql + (i * 4 + t) * 1024 + lane * 16);
      sc[0] = mfma16(km[t], qf, sc[0]); sc[1] = mfma16(kf[0][t], qf, sc[1]); sc[2] = mfma16(kf[1][t], qf, sc[2]);
    }
    const int qi = i * 16 + fr;
    float mx = -INFINITY;
#pragma unroll
    for (int r = 0; r < 4; ++r) {
      if (fq >= 2) sc[0][r] = -INFINITY;
      if (fq * 4 + r > qi) sc[1][r] = -INFINITY;
      if (16 + fq * 4 + r > qi) sc[2][r] = -INFINITY;
      mx = fmaxf(mx, fmaxf(sc[0][r], fmaxf(sc[1][r], sc[2][r])));
    }
    mx = red_max_q(mx);
    float sum = 0.f;
#pragma unroll
    for (int k = 0; k < 3; ++k)
#pragma unroll
      for (int r = 0; r < 4; ++r) { sc[k][r] = __builtin_amdgcn_exp2f((sc[k][r] - mx) * cs); sum += sc[k][r]; }
    sum = red_sum_q(sum);
    const float inv = 1.f / sum;
    const unsigned prow = ((seg * 2 + i) * 160 + 5 * h) * 256 + fr * 16;
    stc(wr, WS_P, prow + (1 + fq) * 256, canon4(pack8(sc[1] * inv, sc[2] * inv)));
    if (fq < 2) { u32x2 o2 = {canon(pack2(sc[0][0] * inv, sc[0][1] * inv)), canon(pack2(sc[0][2] * inv, sc[0][3] * inv))}; stc2(wr, WS_P, prow + fq * 8, o2); }
    __builtin_amdgcn_sched_barrier(0);
  }
}

__device__ __forceinline__ void task_t2(const bf16* x, rsrc_t wr, const Dep& dep, unsigned* bc, int seg, int cs, char* sm, int tid) {
  const int lane = tid & 63, wid = __builtin_amdgcn_readfirstlane(tid >> 6), fr = lane & 15, fq = lane >> 4;
  const bool rdy = dep_first(wr, dep, wid, bc);
  f32x4 acc[2][4] = {};
  u32x4 a[5][2], b[5][4];
#pragma unroll
  for (int k = 0; k < 5; ++k) {
    const int O = (wid * 5 + k) * 4 + fq;
#pragma unroll
    for (int n = 0; n < 4; ++n) {
      const int col = cs * 64 + n * 16 + fr;
      b[k][n] = ldc(wr, WS_VO, (((seg * 80 + (col >> 3)) * 20 + (O >> 3)) * 64 + (O & 3) * 16 + ((O >> 2) & 1) * 8 + (col & 7)) * 16);
    }
  }
  const u32x4 xv = *(const u32x4*)(x + (size_t)(seg * 32 + ((tid >> 3) & 31)) * D_ + cs * 64 + (tid & 7) * 8);
  if (!rdy) dep_second(wr, dep, wid);
  const unsigned t0 = (unsigned)__builtin_amdgcn_s_memrealtime();
  unsigned pend = rdy ? 0x3ffu : 0x7ffu;
  for (;;) {
    if (pend >> 10) {
#pragma unroll
      for (int k = 0; k < 5; ++k) {
        const int O = (wid * 5 + k) * 4 + fq;
        if (O % 5 == 0) {
#pragma unroll
          for (int n = 0; n < 4; ++n) {
            const int col = cs * 64 + n * 16 + fr;
            b[k][n] = ldc(wr, WS_VO, (((seg * 80 + (col >> 3)) * 20 + (O >> 3)) * 64 + (O & 3) * 16 + ((O >> 2) & 1) * 8 + (col & 7)) * 16);
          }
        }
      }
    }
#pragma unroll
    for (int k = 0; k < 5; ++k)
#pragma unroll
      for (int m = 0; m < 2; ++m) if (pend >> (k * 2 + m) & 1) a[k][m] = ldc(wr, WS_P, ((seg * 2 + m) * 40 + wid * 5 + k) * 1024 + lane * 16);
    if (seg < PS) break;
    unsigned mx = 0;
#pragma unroll
    for (int k = 0; k < 5; ++k)
      if (((wid * 5 + k) * 4 + fq) % 5 == 0) mx = max(max(mx, max(max4(b[k][0]), max4(b[k][1]))), max(max4(b[k][2]), max4(b[k][3])));
    pend = __any(mx == SENT) ? 0x400u : 0u;
#pragma unroll
    for (int k = 0; k < 5; ++k)
#pragma unroll
      for (int m = 0; m < 2; ++m) if (__any(max4(a[k][m]) == SENT)) pend |= 1u << (k * 2 + m);
    if (!pend || (unsigned)__builtin_amdgcn_s_memrealtime() - t0 > 5000000u) break;
    if (SLP) __builtin_amdgcn_s_sleep(SLP);
  }
#pragma unroll
  for (int k = 0; k < 5; ++k)
#pragma unroll
    for (int m = 0; m < 2; ++m)
#pragma unroll
      for (int n = 0; n < 4; ++n) acc[m][n] = mfma16(b[k][n], a[k][m], acc[m][n]);
  float* red = (float*)sm;
#pragma unroll
  for (int m = 0; m < 2; ++m)
#pragma unroll
    for (int n = 0; n < 4; ++n) *(f32x4*)(red + (wid * 32 + m * 16 + fr) * 68 + n * 16 + fq * 4) = acc[m][n];
  LBAR();
  if (tid < 256) {
    const int row = tid >> 3, c8 = (tid & 7) * 8;
    const int col = cs * 64 + c8, grow = seg * 32 + row;
    f32x4 s0 = *(const f32x4*)(red + row * 68 + c8), s1 = *(const f32x4*)(red + row * 68 + c8 + 4);
#pragma unroll
    for (int w = 1; w < 8; ++w) { s0 += *(const f32x4*)(red + (w * 32 + row) * 68 + c8); s1 += *(const f32x4*)(red + (w * 32 + row) * 68 + c8 + 4); }
    const u32x4 av = pack8(s0, s1);
    u32x4 o;
#pragma unroll
    for (int e = 0; e < 4; ++e) o[e] = canon(pack2(bflo(xv[e]) + bflo(av[e]), bfhi(xv[e]) + bfhi(av[e])));
    stc(wr, WS_H, ((((grow >> 4) * 20 + (col >> 5)) * 4 + ((col >> 3) & 3)) * 16 + (grow & 15)) * 16, o);
    float sm8 = 0.f;
#pragma unroll
    for (int e = 0; e < 4; ++e) sm8 += bflo(o[e]) + bfhi(o[e]);
    sm8 = red_sum8(sm8);
    const float mu = sm8 * (1.f / 64.f);
    float m2 = 0.f;
#pragma unroll
    for (int e = 0; e < 4; ++e) { const float a = bflo(o[e]) - mu, b = bfhi(o[e]) - mu; m2 += a * a + b * b; }
    m2 = red_sum8(m2);
    if ((tid & 7) == 0) stc2(wr, WS_ST, (grow * 10 + cs) * 8, u32x2{canon(__float_as_uint(sm8)), canon(__float_as_uint(m2))});
  }
}

__device__ __forceinline__ void task_f1(rsrc_t wr, const Dep& dep, const float* CS, const bf16* Wg, const bf16* Wu, int rb, int f0, int nt, char* sm, int tid) {
  const int lane = tid & 63, wid = __builtin_amdgcn_readfirstlane(tid >> 6), wm = wid >> 1, wn = wid & 1;
  const unsigned lw = lds_addr(sm) + wid * 1024;
  const unsigned tb0 = lds_addr(sm) + wn * 512 + trB_base<false>(lane, 0), tb1 = lds_addr(sm) + wn * 512 + trB_base<false>(lane, 1);
  int r0, c0, r1, c1; stageB_rc(tid, r0, c0); stageB_rc(tid + 512, r1, c1);
  const bf16* pw = (wid & 1) ? Wu : Wg;
  const unsigned o0 = (r0 * FF + (c0 & 7) * 8) * 2, o1 = (r1 * FF + (c1 & 7) * 8) * 2;
  const int T = nt * 10;
  auto issue = [&](int ff, int u) {
    const size_t o = (size_t)(u * 64) * FF + (f0 + ff) * 64;
    const unsigned d = lw + (u % RING) * 16384;
    glds16(pw + o, o0, d); glds16(pw + o, o1, d + 8192);
  };
#pragma unroll
  for (int u = 0; u < RING - 1; ++u) issue(0, u);
  u32x4 af[2][20];
  float* lcs = (float*)(sm + RING * 16384);
  if (tid < nt * 128) lcs[tid] = CS[((tid >> 6) & 1) * FF + (f0 + (tid >> 7)) * 64 + (tid & 63)];
  dep_wait(wr, dep, wid);
  load_afc(wr, (unsigned)WS_H + (rb * 8 + wm * 2) * 20480, af, lane);
  float mean[2], rstd[2];
#pragma unroll
  for (int m = 0; m < 2; ++m) {
    const unsigned sp = (rb * 128 + wm * 32 + m * 16 + (lane & 15)) * 80;
    f32x4 p[5];
#pragma unroll
    for (int i = 0; i < 5; ++i) p[i] = __builtin_bit_cast(f32x4, ldc(wr, WS_ST, sp + i * 16));
    float s = 0.f;
#pragma unroll
    for (int i = 0; i < 5; ++i) s += p[i][0] + p[i][2];
    mean[m] = s * (1.f / 640.f);
    float q = 0.f;
#pragma unroll
    for (int i = 0; i < 5; ++i) {
      const float d0 = p[i][0] * (1.f / 64.f) - mean[m], d1 = p[i][2] * (1.f / 64.f) - mean[m];
      q += p[i][1] + p[i][3] + 64.f * (d0 * d0 + d1 * d1);
    }
    rstd[m] = 1.f / sqrtf(q * (1.f / 640.f) + 1e-5f);
  }
  for (int ff = 0; ff < nt; ++ff) {
    f32x4 acc[2][4] = {};
#pragma unroll
    for (int u = 0; u < 10; ++u) {
      if (u < 4) { WAIT_V(8); LBAR(); } else ring_wait(ff * 10 + u, T);
      if (u + RING - 1 < 10) issue(ff, u + RING - 1); else if (ff + 1 < nt) issue(ff + 1, u + RING - 1 - 10);
      mma_tile<true, false, 1024>(af[0][2 * u], af[1][2 * u], af[0][2 * u + 1], af[1][2 * u + 1], tb0, tb1, (u % RING) * 16384, acc);
    }
    f32x4 cs[4];
#pragma unroll
    for (int n = 0; n < 4; ++n) cs[n] = *(const f32x4*)(lcs + ff * 128 + (n >> 1) * 64 + (2 * wn + (n & 1)) * 16 + (lane >> 4) * 4);
#pragma unroll
    for (int m = 0; m < 2; ++m) {
#pragma unroll
      for (int n = 0; n < 4; ++n) acc[m][n] = (acc[m][n] - mean[m] * cs[n]) * rstd[m];
      u32x4 g = pack8(acc[m][0], acc[m][1]), u = pack8(acc[m][2], acc[m][3]), o;
      auto rl = [](float v) { return v > 0.f ? v : 0.f * v; };
#pragma unroll
      for (int e = 0; e < 4; ++e) o[e] = pack2(rl(bflo(g[e])) * bflo(u[e]), rl(bfhi(g[e])) * bfhi(u[e]));
      stc(wr, WS_G, ((rb * 8 + wm * 2 + m) * 54 + (f0 + ff) * 2 + wn) * 1024 + lane * 16, o);
    }
  }
}

__device__ __forceinline__ void task_f2(const bf16* G, const bf16* Wd, rsrc_t wr, bf16* out, int rb, int cs, char* sm, int tid) {
  const int lane = tid & 63, wid = __builtin_amdgcn_readfirstlane(tid >> 6), wm = wid >> 1, wn = wid & 1;
  const unsigned lw = lds_addr(sm) + wid * 1024;
  const unsigned tp0 = lds_addr(sm) + wn * 1024 + trB_base<true>(lane, 0), tp1 = lds_addr(sm) + wn * 1024 + trB_base<true>(lane, 1);
  int r0, c0, r1, c1; stageB_rc(tid, r0, c0); stageB_rc(tid + 512, r1, c1);
  const bf16* pa = G + (size_t)(rb * 8 + wid) * 54 * 512;
  const bf16* pb = Wd + cs * 128;
  const unsigned o0 = (r0 * D_ + c0 * 8) * 2, o1 = (r1 * D_ + c1 * 8) * 2;
  constexpr int R4 = 4;
  auto issue = [&](int t) {
    const unsigned d = lds_addr(sm) + (t % R4) * 32768;
    glds16c(pa + t * 1024, lane * 16, d + wid * 2048); glds16c(pa + t * 1024 + 512, lane * 16, d + wid * 2048 + 1024);
    glds16(pb + (size_t)t * 64 * D_, o0, d + 16384 + wid * 1024); glds16(pb + (size_t)t * 64 * D_, o1, d + 24576 + wid * 1024);
  };
  (void)lw;
  issue(0); issue(1); issue(2);
  f32x4 acc[2][4] = {};
  for (int t = 0; t < 27; ++t) {
    if (t + 2 < 27) { WAIT_V(8); } else if (t + 1 < 27) { WAIT_V(4); } else { WAIT_V(0); }
    LBAR();
    if (t + 3 < 27) issue(t + 3);
    const char* d = sm + (t % R4) * 32768;
    const u32x4 a00 = *(const u32x4*)(d + (wm * 2) * 2048 + lane * 16), a01 = *(const u32x4*)(d + (wm * 2) * 2048 + 1024 + lane * 16);
    const u32x4 a10 = *(const u32x4*)(d + (wm * 2 + 1) * 2048 + lane * 16), a11 = *(const u32x4*)(d + (wm * 2 + 1) * 2048 + 1024 + lane * 16);
    mma_tile<true, true, 512>(a00, a10, a01, a11, tp0 + (t % R4) * 32768, tp1 + (t % R4) * 32768, 16384, acc);
  }
#pragma unroll
  for (int m = 0; m < 2; ++m)
#pragma unroll
    for (int n = 0; n < 4; ++n) {
      const size_t go = (size_t)(rb * 128 + wm * 32 + m * 16 + (lane & 15)) * D_ + cs * 128 + wn * 64 + n * 16 + (lane >> 4) * 4;
      const int col = cs * 128 + wn * 64 + n * 16 + (lane >> 4) * 4;
      u32x2 hv = ldc2(wr, WS_H, ((((rb * 8 + wm * 2 + m) * 20 + (col >> 5)) * 64 + ((col >> 3) & 3) * 16 + (lane & 15)) * 8 + (col & 7)) * 2);
      unsigned a0 = pack2(acc[m][n][0], acc[m][n][1]), a1 = pack2(acc[m][n][2], acc[m][n][3]);
      u32x2 o = {pack2(bflo(hv.x) + bflo(a0), bfhi(hv.x) + bfhi(a0)), pack2(bflo(hv.y) + bflo(a1), bfhi(hv.y) + bfhi(a1))};
      *(u32x2*)(out + go) = o;
    }
}

__device__ __forceinline__ void task_f1p(rsrc_t wr, const float* CS, const bf16* Wg, const bf16* Wu, int j, char* sm, int tid) {
  const int lane = tid & 63, wid = __builtin_amdgcn_readfirstlane(tid >> 6), kq = wid & 3, ch = wid >> 2, fr = lane & 15, fq = lane >> 4;
  u32x4 w[4][5];
#pragma unroll
  for (int gc = 0; gc < 4; ++gc)
#pragma unroll
    for (int i = 0; i < 5; ++i) {
      const unsigned short* p = (const unsigned short*)((gc >> 1) ? Wu : Wg) + (size_t)((kq * 5 + i) * 32 + fq * 8) * FF + j * 32 + (gc & 1) * 16 + fr;
#pragma unroll
      for (int e = 0; e < 4; ++e) w[gc][i][e] = (unsigned)p[(2 * e) * FF] | ((unsigned)p[(2 * e + 1) * FF] << 16);
    }
  f32x4 cs[2][2];
  dep_wait(wr, Dep{C_FILL, NFILL, 0, 0}, wid);
#pragma unroll
  for (int gu = 0; gu < 2; ++gu)
#pragma unroll
    for (int c = 0; c < 2; ++c) cs[gu][c] = *(const f32x4*)(CS + gu * FF + j * 32 + c * 16 + fq * 4);
  for (int seg = PS; seg < NSEG; seg += 2) {
    const unsigned t0 = (unsigned)__builtin_amdgcn_s_memrealtime();
    u32x4 p_[5] = {};
    const bool last = seg == NSEG - 2;
    if (last ? wid == 7 : wid < 4) {
      for (;;) {
        unsigned mx = 0;
        if (last) mx = max4(ldc(wr, WS_P, (NSEG - 1) * 81920 + lane * 16));
        else {
#pragma unroll
          for (int i = 0; i < 5; ++i) p_[i] = ldc(wr, WS_ST, (seg * 32 + wid * 16 + fr) * 80 + i * 16);
#pragma unroll
          for (int i = 0; i < 5; ++i) mx = max(mx, max4(p_[i]));
        }
        if (!__any(mx == SENT) || (unsigned)__builtin_amdgcn_s_memrealtime() - t0 > 5000000u) break;
        __builtin_amdgcn_s_sleep(SLP);
      }
    }
    BAR();
    u32x4 a[2][5];
    unsigned pend = last && wid < 4 ? 0x7ffu : 0x3ffu;
    for (;;) {
#pragma unroll
      for (int rt = 0; rt < 2; ++rt)
#pragma unroll
        for (int i = 0; i < 5; ++i) if (pend >> (rt * 5 + i) & 1) a[rt][i] = ldc(wr, WS_H, ((seg * 2 + ch * 2 + rt) * 20 + kq * 5 + i) * 1024 + lane * 16);
      if (pend >> 10) {
#pragma unroll
        for (int i = 0; i < 5; ++i) p_[i] = ldc(wr, WS_ST, (seg * 32 + wid * 16 + fr) * 80 + i * 16);
      }
      unsigned mx = 0;
#pragma unroll
      for (int i = 0; i < 5; ++i) mx = max(mx, max4(p_[i]));
      pend = __any(mx == SENT) ? 0x400u : 0u;
#pragma unroll
      for (int rt = 0; rt < 2; ++rt)
#pragma unroll
        for (int i = 0; i < 5; ++i) if (__any(max4(a[rt][i]) == SENT)) pend |= 1u << (rt * 5 + i);
      if (!pend || (unsigned)__builtin_amdgcn_s_memrealtime() - t0 > 5000000u) break;
    }
    f32x4 p[5];
#pragma unroll
    for (int i = 0; i < 5; ++i) p[i] = __builtin_bit_cast(f32x4, p_[i]);
#pragma unroll
    for (int rt = 0; rt < 2; ++rt)
#pragma unroll
      for (int gc = 0; gc < 4; ++gc) {
        f32x4 acc = ZERO4;
#pragma unroll
        for (int i = 0; i < 5; ++i) acc = mfma16(w[gc][i], a[rt][i], acc);
        *(f32x4*)(sm + (((ch * 2 + rt) * 4 + gc) * 4 + kq) * 1024 + lane * 16) = acc;
      }
    LBAR();
    if (wid < 4) {
      float s = 0.f;
#pragma unroll
      for (int i = 0; i < 5; ++i) s += p[i][0] + p[i][2];
      const float mean = s * (1.f / 640.f);
      float q = 0.f;
#pragma unroll
      for (int i = 0; i < 5; ++i) {
        const float d0 = p[i][0] * (1.f / 64.f) - mean, d1 = p[i][2] * (1.f / 64.f) - mean;
        q += p[i][1] + p[i][3] + 64.f * (d0 * d0 + d1 * d1);
      }
      const float rstd = 1.f / sqrtf(q * (1.f / 640.f) + 1e-5f);
      f32x4 v[2][2];
#pragma unroll
      for (int gu = 0; gu < 2; ++gu)
#pragma unroll
        for (int c = 0; c < 2; ++c) {
          const char* b = sm + ((wid * 2 + gu) * 2 + c) * 4096 + lane * 16;
          v[gu][c] = (*(const f32x4*)b + *(const f32x4*)(b + 1024)) + (*(const f32x4*)(b + 2048) + *(const f32x4*)(b + 3072));
          v[gu][c] = (v[gu][c] - mean * cs[gu][c]) * rstd;
        }
      u32x4 g = pack8(v[0][0], v[0][1]), u = pack8(v[1][0], v[1][1]), o;
      auto rl = [](float x) { return x > 0.f ? x : 0.f * x; };
#pragma unroll
      for (int e = 0; e < 4; ++e) o[e] = canon(pack2(rl(bflo(g[e])) * bflo(u[e]), rl(bfhi(g[e])) * bfhi(u[e])));
      stc(wr, WS_G, ((seg * 2 + wid) * 54 + j) * 1024 + lane * 16, o);
    }
  }
}
__device__ __forceinline__ void task_f2p(rsrc_t wr, const bf16* Wd, bf16* out, int j, int par, char* sm, int tid) {
  const int lane = tid & 63, wid = __builtin_amdgcn_readfirstlane(tid >> 6), fr = lane & 15, fq = lane >> 4;
  u32x4 w[2][7];
#pragma unroll
  for (int i = 0; i < 7; ++i) {
    const int ks = min(wid + 8 * i, 53);
#pragma unroll
    for (int ct = 0; ct < 2; ++ct) {
      const unsigned short* p = (const unsigned short*)Wd + (size_t)(ks * 32 + fq * 4) * D_ + j * 32 + (fr >> 2) * 8 + ct * 4 + (fr & 3);
#pragma unroll
      for (int e = 0; e < 4; ++e) {
        const int k0 = (e >> 1) * 16 + (e & 1) * 2;
        w[ct][i][e] = wid + 8 * i < 54 ? (unsigned)p[k0 * D_] | ((unsigned)p[(k0 + 1) * D_] << 16) : 0u;
      }
    }
  }
  dep_wait(wr, Dep{C_FILL, NFILL, 0, 0}, wid);
  for (int seg = PS + par; seg < NSEG; seg += 2) {
    BAR();
    const unsigned t0 = (unsigned)__builtin_amdgcn_s_memrealtime();
    u32x4 a[2][7];
    u32x4 hv;
    for (;;) {
      hv = ldc(wr, WS_H, (((seg * 2 + (wid & 1)) * 20 + j) * 64 + lane) * 16);
      if (!__any(max4(hv) == SENT) || (unsigned)__builtin_amdgcn_s_memrealtime() - t0 > 5000000u) break;
      __builtin_amdgcn_s_sleep(SLP);
    }
    unsigned pend = 0x3fffu;
    for (;;) {
#pragma unroll
      for (int rt = 0; rt < 2; ++rt)
#pragma unroll
        for (int i = 0; i < 7; ++i) if (pend >> (rt * 7 + i) & 1) a[rt][i] = ldc(wr, WS_G, ((seg * 2 + rt) * 54 + min(wid + 8 * i, 53)) * 1024 + lane * 16);
      pend = 0;
#pragma unroll
      for (int rt = 0; rt < 2; ++rt)
#pragma unroll
        for (int i = 0; i < 7; ++i) if (__any(max4(a[rt][i]) == SENT)) pend |= 1u << (rt * 7 + i);
      if (!pend || (unsigned)__builtin_amdgcn_s_memrealtime() - t0 > 5000000u) break;
    }
#pragma unroll
    for (int rt = 0; rt < 2; ++rt)
#pragma unroll
      for (int ct = 0; ct < 2; ++ct) {
        f32x4 acc = ZERO4;
#pragma unroll
        for (int i = 0; i < 7; ++i) acc = mfma16(w[ct][i], a[rt][i], acc);
        *(f32x4*)(sm + ((rt * 2 + ct) * 8 + wid) * 1024 + lane * 16) = acc;
      }
    LBAR();
    if (wid < 2) {
      f32x4 v[2];
#pragma unroll
      for (int ct = 0; ct < 2; ++ct) {
        const char* b = sm + (wid * 2 + ct) * 8192 + lane * 16;
        v[ct] = *(const f32x4*)b;
#pragma unroll
        for (int i = 1; i < 8; ++i) v[ct] += *(const f32x4*)(b + i * 1024);
      }
      const u32x4 av = pack8(v[0], v[1]);
      u32x4 o;
#pragma unroll
      for (int e = 0; e < 4; ++e) o[e] = pack2(bflo(hv[e]) + bflo(av[e]), bfhi(hv[e]) + bfhi(av[e]));
      *(u32x4*)(out + (size_t)(seg * 32 + wid * 16 + fr) * D_ + j * 32 + fq * 8) = o;
    }
  }
}

__device__ __forceinline__ void task_fill(rsrc_t wr, int seg, int tid) {
  unsigned sv; asm volatile("v_mov_b32 %0, -1" : "=v"(sv));
  const u32x4 S = {sv, sv, sv, sv};
#pragma unroll
  for (int i = 0; i < 10; ++i) stc(wr, WS_P + seg * 81920, (i * 512 + tid) * 16, S);
#pragma unroll
  for (int i = 0; i < 5; ++i) stc(wr, WS_H + seg * 40960, (i * 512 + tid) * 16, S);
  if (tid < 160) stc(wr, WS_ST + seg * 2560, tid * 16, S);
#pragma unroll
  for (int i = 0; i < 14; ++i) if (i * 512 + tid < 6912) stc(wr, WS_G + seg * 110592, (i * 512 + tid) * 16, S);
  const int O = 5 * ((tid >> 3) & 31);
  const unsigned off = (tid >> 8) * 20480 + (O >> 3) * 1024 + ((O & 3) * 16 + ((O >> 2) & 1) * 8 + (tid & 7)) * 16;
#pragma unroll 8
  for (int i = 0; i < 40; ++i) stc(wr, WS_VO + (seg * 80 + i * 2) * 20480, off, S);
}

__device__ __forceinline__ unsigned xoff(int step, unsigned o, unsigned par) {
  return (unsigned)step * 65536u + ((o >> 13) << 14) + (o & 8191u) + (((par >> (step >> 5)) & 1u) << 13);
}

__global__ void __launch_bounds__(512, 2) k_prologue(const bf16* x, const bf16* mem0, const bf16* Wv, const bf16* Wo, const bf16* Wg, const bf16* Wu, char* ws) {
  extern __shared__ __attribute__((aligned(16))) char sm[];
  const int tid = threadIdx.x, bid = blockIdx.x;
  if (bid == 0) {
    if (tid < 64) {
      const __amdgpu_buffer_rsrc_t r = __builtin_amdgcn_make_buffer_rsrc(ws + WS_XCH, 0, NC << 22, 0x00020000);
      for (int g = 0; g < 2; ++g) {
        unsigned best[2] = {~0u, ~0u};
        for (int k = 0; k < 8; ++k)
          for (int p = 0; p < 2; ++p) {
            unsigned off = (g << 21) + (k * 4 + 1) * 65536 + 3 * 16384 + p * 8192 + 6144 + 512;
            asm volatile("s_waitcnt vmcnt(0)" ::: "memory");
            unsigned long long t0 = __builtin_amdgcn_s_memtime();
            unsigned v = __builtin_amdgcn_raw_buffer_load_b32(r, off, 0, 16);
            asm volatile("s_waitcnt vmcnt(0)" ::"v"(v) : "memory");
            unsigned long long t1 = __builtin_amdgcn_s_memtime();
            unsigned d = (unsigned)(t1 - t0); if (d < best[p]) best[p] = d;
          }
        ((unsigned*)sm)[2 * g] = best[0]; ((unsigned*)sm)[2 * g + 1] = best[1];
      }
    } else if (tid >= 256) {
      for (unsigned o = C_QH + (tid - 256) * 16; o < C_END; o += 256 * 16) *(u32x4*)(ws + o) = u32x4{o < C_QH + 1024 && !(o & 255) ? 36u : 0u, 0u, 0u, 0u};
    }
    LBAR();
    if (tid < 16) {
      const u32x4 b = *(const u32x4*)sm;
      unsigned tab = (b[1] < b[0] ? 1u : 0u) | (b[3] < b[2] ? 2u : 0u);
      if (xcc_id() >= 4) tab ^= 3u;
      ((unsigned*)(ws + WS_CTL))[tid] = tid == 0 ? tab : tid == 1 ? xcc_id() : 0u;
    }
    return;
  }
  if (bid <= 16) {
    for (int i = tid; i < 4 * NC * 4096; i += 512) {
      const int step = (bid - 1) * 4 * NC + (i >> 12);
      const unsigned po = (i & 4095) * 16;
      const unsigned lo = ((po >> 14) << 13) + (po & 8191u);
      if (lo >= 30720u) continue;
      u32x4 v = {SENT, SENT, SENT, SENT};
      if (step < NC && lo < 10240u) {
        const int b = lo >> 7, row = (lo >> 4) & 7;
        v = *(const u32x4*)(mem0 + row * D_ + b * 8);
        v.x = canon(v.x); v.y = canon(v.y); v.z = canon(v.z); v.w = canon(v.w);
      }
      *(u32x4*)(ws + WS_XCH + (size_t)step * 65536 + po) = v;
    }
    return;
  }
  if (bid <= 48) {
    if (tid < 256) { const int rt = (bid - 17) * 4 + (tid >> 6); ln_tile(x + (size_t)rt * 16 * D_, (bf16*)(ws + WS_XN) + (size_t)rt * 10240, tid & 63); }
    return;
  }
  if (bid <= 75) {
    const int f0 = (bid - 49) * 64, ch = tid & 7, rg = tid >> 3;
    float* red = (float*)sm;
#pragma unroll
    for (int w = 0; w < 2; ++w) {
      const bf16* W = w ? Wu : Wg;
      float a[8] = {};
#pragma unroll
      for (int i = 0; i < 10; ++i) {
        u32x4 v = *(const u32x4*)(W + (size_t)(rg * 10 + i) * FF + f0 + ch * 8);
#pragma unroll
        for (int e = 0; e < 4; ++e) { a[2 * e] += bflo(v[e]); a[2 * e + 1] += bfhi(v[e]); }
      }
      *(f32x4*)(red + (w * 64 + rg) * 64 + ch * 8) = f32x4{a[0], a[1], a[2], a[3]};
      *(f32x4*)(red + (w * 64 + rg) * 64 + ch * 8 + 4) = f32x4{a[4], a[5], a[6], a[7]};
    }
    LBAR();
    if (tid < 128) {
      float t = 0.f;
      for (int i = 0; i < 64; ++i) t += red[((tid >> 6) * 64 + i) * 64 + (tid & 63)];
      ((float*)(ws + WS_CS))[(tid >> 6) * FF + f0 + (tid & 63)] = t;
    }
    return;
  }
  const int t = bid - 76;
  task_u(Wv, Wo, (bf16*)(ws + WS_U), t / 5, t % 5, sm, tid);
}

#define TS(i)
#define TSDECL
#define TSOUT(s)
#define AWIN 8
#define SLT 0
#define STAG 6
#define SLA 30
#define SLM 20
#define SLR 8
#define STL 6
#define DL 1
#define SLV 6
#define VPOS 2
#define SLB 16
__device__ __forceinline__ bool frag_bad(u32x4 v) { return max(max(v.x, v.y), max(v.z, v.w)) == SENT; }
__device__ __forceinline__ void glds16n(const void* base, unsigned voff, unsigned lds) {
  asm volatile("s_mov_b32 m0, %2\n\ts_nop 0\n\tglobal_load_lds_dwordx4 %0, %1 nt" ::"v"(voff), "s"(base), "s"(__builtin_amdgcn_readfirstlane(lds)) : "m0");
}
template <int NL, int DEPTH, int SG, bool LOCAL = false>
__device__ __forceinline__ void poll_lds(const char* xb, const unsigned (&off)[NL], char* const (&dst)[NL], int lane, u32x4 (&v)[NL]) {
  unsigned spins = 0;
#pragma unroll
  for (int d = 0; d < DEPTH - 1; ++d) {
#pragma unroll
    for (int i = 0; i < NL; ++i) { if (LOCAL) glds16n(xb, off[i], lds_addr(dst[i])); else glds16c(xb, off[i], lds_addr(dst[i])); }
    __builtin_amdgcn_s_sleep(SG);
  }
  for (;;) {
    if (LOCAL && (spins & 15) != 15) {
#pragma unroll
      for (int i = 0; i < NL; ++i) glds16n(xb, off[i], lds_addr(dst[i]));
    } else {
#pragma unroll
      for (int i = 0; i < NL; ++i) glds16c(xb, off[i], lds_addr(dst[i]));
    }
    wait_v<NL * (DEPTH - 1)>();
    bool bad = false;
#pragma unroll
    for (int i = 0; i < NL; ++i) v[i] = *(const u32x4*)(dst[i] + lane * 16);
#pragma unroll
    for (int i = 0; i < NL; ++i) bad |= frag_bad(v[i]);
    if (!__any(bad) || ++spins > SPIN_CAP) break;
  }
}
template <int NL>
__device__ __forceinline__ void poll_lds1(const char* xb, const unsigned (&off)[NL], char* const (&dst)[NL], int lane, u32x4 (&v)[NL]) {
  unsigned spins = 0, pend = (1u << NL) - 1u;
  for (;;) {
#pragma unroll
    for (int i = 0; i < NL; ++i) if (pend & (1u << i)) glds16c(xb, off[i], lds_addr(dst[i]));
    WAIT_V(0);
#pragma unroll
    for (int i = 0; i < NL; ++i) v[i] = *(const u32x4*)(dst[i] + lane * 16);
    pend = 0;
#pragma unroll
    for (int i = 0; i < NL; ++i) if (__any(frag_bad(v[i]))) pend |= 1u << i;
    if (!pend || ++spins > SPIN_CAP) break;
  }
}
template <class FB, class FO>
__device__ __forceinline__ void load_wfrags(FB base, FO voff, int fstride, u32x8 (&w)[2][10], char* sm, int tid) {
  const int lane = tid & 63, wid = __builtin_amdgcn_readfirstlane(tid >> 6);
  const int bq = lane >> 4, q = (lane & 15) >> 2, p = lane & 3;
  const unsigned lw = lds_addr(sm) + wid * 1024;
  unsigned vo[4];
#pragma unroll
  for (int i = 0; i < 4; ++i) {
    const int ci = i * 512 + tid, half = i >> 1, k = (ci >> 4) & 63;
    vo[i] = voff(k, half * 16 + ((ci & 15) ^ (((k & 3) << 1) | (((k >> 2) & 1) << 3))));
  }
  auto issue = [&](int f) {
#pragma unroll
    for (int i = 0; i < 4; ++i) glds16((const char*)base(i >> 1) + f * fstride, vo[i], lw + (f & 3) * 32768 + i * 8192);
  };
#pragma unroll
  for (int f = 0; f < 4; ++f) issue(f);
#pragma unroll
  for (int f = 0; f < 10; ++f) {
    if (f < 7) { WAIT_V(12); } else if (f == 7) { WAIT_V(8); } else if (f == 8) { WAIT_V(4); } else { WAIT_V(0); }
    BAR();
#pragma unroll
    for (int t = 0; t < 2; ++t) {
      u32x2 qd[4];
#pragma unroll
      for (int z = 0; z < 4; ++z) {
        const int k = 16 * (bq >> 1) + 4 * (bq & 1) + (z & 1) * 8 + (z >> 1) * 32 + q;
        const int cc = (2 * wid + t) * 2 + (p >> 1);
        qd[z] = tr_read(sm + (f & 3) * 32768 + ((cc >> 4) * 1024 + k * 16 + ((cc & 15) ^ (((k & 3) << 1) | (((k >> 2) & 1) << 3)))) * 16 + 8 * (p & 1));
      }
      w[t][f] = u32x8{qd[0].x, qd[1].x, qd[0].y, qd[1].y, qd[2].x, qd[3].x, qd[2].y, qd[3].y};
    }
    LBAR();
    if (f + 4 < 10) issue(f + 4);
  }
}

template <bool FAST>
__device__ __forceinline__ void poll_mem(const char* xb, int s, unsigned par, char* memA, char* other, int wid, int lane) {
  if (wid < 3) {
    unsigned off[2] = {xoff(s, wid * 1024 + lane * 16, par), xoff(s, (7 + wid) * 1024 + lane * 16, par)};
    char* const dst[2] = {memA + wid * 1024, memA + (7 + wid) * 1024};
    u32x4 v[2];
    if (FAST) {
      poll_lds<2, 2, STAG>(xb, off, dst, lane, v);
      *(u32x4*)(other + wid * 1024 + lane * 16) = u32x4{SENT, SENT, SENT, SENT};
      *(u32x4*)(other + (7 + wid) * 1024 + lane * 16) = u32x4{SENT, SENT, SENT, SENT};
    } else poll_lds1<2>(xb, off, dst, lane, v);
  } else if (wid < 7) {
    unsigned off[1] = {xoff(s, wid * 1024 + lane * 16, par)};
    char* const dst[1] = {memA + wid * 1024};
    u32x4 v[1];
    if (FAST) {
      poll_lds<1, 2, STAG>(xb, off, dst, lane, v);
      *(u32x4*)(other + wid * 1024 + lane * 16) = u32x4{SENT, SENT, SENT, SENT};
    } else poll_lds1<1>(xb, off, dst, lane, v);
  }
}
template <int WIN>
__device__ __forceinline__ f32x4 mem_times_w(const char* memA, const u32x8 (&w)[2][10], int lane) {
  const int sidx = (lane & 15) < 8 ? 0x4444 : 0xEEEE;
  u32x4 a[10];
#pragma unroll
  for (int f = 0; f < WIN; ++f) a[f] = *(const u32x4*)(memA + f * 1024 + lane * 16);
  f32x4 a0 = ZERO4, a1 = ZERO4;
#pragma unroll
  for (int f = 0; f < 10; ++f) {
    a0 = smf(a[f], w[0][f], a0, sidx); a1 = smf(a[f], w[1][f], a1, sidx);
    if (f + WIN < 10) { __builtin_amdgcn_sched_barrier(0); a[f + WIN] = *(const u32x4*)(memA + (f + WIN) * 1024 + lane * 16); }
  }
  f32x4 v;
#pragma unroll
  for (int r = 0; r < 4; ++r) {
    auto sw = __builtin_amdgcn_permlane32_swap(__float_as_uint(a0[r]), __float_as_uint(a1[r]), false, false);
    v[r] = __uint_as_float(sw[0]) + __uint_as_float(sw[1]);
  }
  return v;
}

constexpr int LDS_MAIN = 147456 + 64;
#define TPREF 0
__global__ void __launch_bounds__(512, 2) k_main(const bf16* x, const bf16* Wq, const bf16* Wk, const bf16* Wv, const bf16* Wo, const bf16* Wg, const bf16* Wu,
                                                 const bf16* Wd, char* ws, bf16* out) {
  extern __shared__ __attribute__((aligned(16))) char sm[];
  const int tid = threadIdx.x;
  const int wid = __builtin_amdgcn_readfirstlane(tid >> 6);
  unsigned* ctl = (unsigned*)(ws + WS_CTL);
  const int res = (blockIdx.x + __builtin_amdgcn_readfirstlane(ctl[1])) & 7, bi = blockIdx.x >> 3;
  const int slot = res < 4 && bi < 28 ? res * 28 + bi : 1000;
  const rsrc_t wr = __builtin_amdgcn_make_buffer_rsrc(ws, 0, (unsigned)(WS_END + 8 * MB), 0x00020000);
  auto ltid = [&]() { int l; asm volatile("v_mbcnt_lo_u32_b32 %0, -1, 0\n\tv_mbcnt_hi_u32_b32 %0, -1, %0" : "=v"(l)); return wid * 64 + l; };
  if (slot < 112) {
  const int tid = ltid(), lane = tid & 63;
  const int grp = slot / 28, tsl = slot % 28;
  const int role = tsl < 8 ? grp * 8 + tsl : 32 + grp * 20 + (tsl - 8);
  const unsigned par = ctl[0];
  const int cpy = role % NC;
  const __amdgpu_buffer_rsrc_t xr = __builtin_amdgcn_make_buffer_rsrc(ws + WS_XCH, 0, NC << 22, 0x00020000);
  const int fr = lane & 15, fq = lane >> 4;
  u32x8 w[2][10];
  char* memA = sm;
  if (role < 32) {
    const int h = role;
    load_wfrags([&](int half) { return (half ? Wk : Wq) + h * DH; }, [&](int k, int cc) { return (unsigned)(k * HD + (cc & 15) * 8) * 2u; }, 64 * HD * 2, w, sm, tid);
    char* qk = sm + 10240;
    char* kring = sm + 16384;
    char* memB = sm + 40960;
    char* qring = sm + 51200;
    const bf16* K = (const bf16*)(ws + WS_K) + (size_t)h * 4096;
    const float cs = 0.08838834764831845f * 1.4426950408889634f;
    auto kdma = [&](int s) {
      const bf16* src = K + (size_t)s * NH * 4096;
      const unsigned d = lds_addr(kring) + (s % 3) * 8192;
#pragma unroll
      for (int i = 0; i < 8; ++i) glds16c(src + i * 512, lane * 16, d + i * 1024);
#pragma unroll
      for (int i = 0; i < 8; ++i) glds16c(src - (WS_K - WS_Q) / 2 + i * 512, lane * 16, d + (51200 - 16384) + i * 1024);
    };
    if (wid == 7) { dep_poll(wr, C_K, 32); dep_poll(wr, C_Q, 32); kdma(0); kdma(1); WAIT_V(16); }
    for (int i = tid; i < 640; i += 512) *(u32x4*)(memA + i * 16) = u32x4{SENT, SENT, SENT, SENT};
    LBAR();
    const int Ost = fr < 8 ? 5 * h + 1 + fq : 5 * h;
    const unsigned pst = 10240 + (Ost >> 3) * 1024 + ((Ost & 3) * 16 + ((Ost >> 2) & 1) * 8 + (fr & 7)) * 16;
    for (int s = 0; s < NSEG; ++s) {
      TSDECL; TS(0);
      char* mcur = (s & 1) ? memB : memA;
      u32x4 kf[2][4];
      if (wid == 0 || wid == 7) {
        const char* kt = kring + (s % 3) * 8192 + lane * 16;
#pragma unroll
        for (int t = 0; t < 4; ++t) { kf[0][t] = *(const u32x4*)(kt + t * 1024); kf[1][t] = *(const u32x4*)(kt + 4096 + t * 1024); }
      }
      poll_mem<true>(ws + WS_XCH, s, par, mcur, (s & 1) ? memA : memB, wid, lane);
      if (wid == 7) { WAIT_V(4); }
      TS(1);
      LBAR();
      TS(2);
      if (role == 0 && tid == 384) __builtin_amdgcn_raw_buffer_store_b32(s, wr, C_PROG, 0, 16);
      { f32x4 v = mem_times_w<AWIN>(mcur, w, lane);
        const int col = (2 * wid + (lane >> 5)) * 16 + fr, e = col & 127;
        char* dst = qk + (col >> 7) * 2176 + (fq & 1) * 4 * 272 + (e >> 5) * 64 + ((e >> 2) & 3) * 16 + ((e >> 4) & 1) * 8 + (e & 3) * 2;
#pragma unroll
        for (int r = 0; r < 4; ++r) *(unsigned short*)(dst + r * 272) = (unsigned short)(pack2(v[r], v[r]) & 0xffffu); }
      TS(3);
      LBAR();
      TS(4);
      if (wid == 0) {
        if (s < NSEG - 1) {
          u32x4 qf[4], km[4];
#pragma unroll
          for (int t = 0; t < 4; ++t) {
            qf[t] = *(const u32x4*)(qk + (fr & 7) * 272 + t * 64 + fq * 16);
            km[t] = *(const u32x4*)(qk + 2176 + (fr & 7) * 272 + t * 64 + fq * 16);
          }
          asm volatile("" : "+v"(qf[0]), "+v"(qf[1]), "+v"(qf[2]), "+v"(qf[3]), "+v"(km[0]), "+v"(km[1]), "+v"(km[2]), "+v"(km[3]));
          f32x4 sc[3] = {};
#pragma unroll
          for (int t = 0; t < 4; ++t) { sc[1] = mfma16(kf[0][t], qf[t], sc[1]); sc[2] = mfma16(kf[1][t], qf[t], sc[2]); sc[0] = mfma16(km[t], qf[t], sc[0]); }
          const bool hi = fr >= 8;
          float e[6];
#pragma unroll
          for (int r = 0; r < 4; ++r) e[r] = hi ? sc[2][r] : sc[1][r];
          e[4] = hi ? sc[0][2] : sc[0][0]; e[5] = hi ? sc[0][3] : sc[0][1];
          if (fq >= 2) { e[4] = -INFINITY; e[5] = -INFINITY; }
          float mx = fmaxf(fmaxf(fmaxf(e[0], e[1]), fmaxf(e[2], e[3])), fmaxf(e[4], e[5]));
          mx = fmaxf(mx, __uint_as_float(dpp_ror8(__float_as_uint(mx))));
          mx = red_max_q(mx) * cs;
          float sum = 0.f;
#pragma unroll
          for (int r = 0; r < 6; ++r) { e[r] = __builtin_amdgcn_exp2f(__builtin_fmaf(e[r], cs, -mx)); sum += e[r]; }
          sum += __uint_as_float(dpp_ror8(__float_as_uint(sum)));
          sum = red_sum_q(sum);
          const float inv = __builtin_amdgcn_rcpf(sum);
          const unsigned p0 = canon(pack2(e[0] * inv, e[1] * inv)), p1 = canon(pack2(e[2] * inv, e[3] * inv)), pm = canon(pack2(e[4] * inv, e[5] * inv));
          const unsigned q0 = dpp_ror8(p0), q1 = dpp_ror8(p1), qm = dpp_ror8(pm);
          auto s0 = __builtin_amdgcn_permlane16_swap(qm, qm, false, false);
          auto s1 = __builtin_amdgcn_permlane16_swap(pm, pm, false, false);
          const u32x4 o = hi ? u32x4{qm, pm, s0[1], s1[1]} : u32x4{p0, p1, q0, q1};
          if (fr < 8 || fq == 0) __builtin_amdgcn_raw_buffer_store_b128(o, xr, xoff(s, pst, par), 0, 16);
        }
      } else if (wid == 7) {
        if (s == PS - 1) dep_poll(wr, C_FILL, NFILL);
        if (s + 2 < NSEG) {
          if (!((s + 2) & 3)) { dep_poll(wr, C_K + ((s + 2) >> 2) * 256, 32); dep_poll(wr, C_Q + ((s + 2) >> 2) * 256, 32); }
          kdma(s + 2);
        }
        if (SLT) __builtin_amdgcn_s_sleep(SLT);
        u32x4 km[4];
        const int l7 = ltid() & 63;
#pragma unroll
        for (int t = 0; t < 4; ++t) km[t] = *(const u32x4*)(qk + 2176 + (l7 & 7) * 272 + t * 64 + (l7 >> 4) * 16);
        wave_t1(wr, qring + (s % 3) * 8192, kf, km, s, h, l7);
      }
      TS(5);
      TSOUT(s);
      if (wid < 7) __builtin_amdgcn_s_sleep(SLA);
    }
    WAIT_V(0);
  } else {
    const int b = role - 32;
    const bf16* U = (const bf16*)(ws + WS_U) + (size_t)b * NH * D_ * 8;
    load_wfrags([&](int half) { return U; }, [&](int k, int cc) { return (unsigned)(cc * D_ + k) * 16u; }, 64 * 16, w, sm, tid);
    float* red = (float*)(sm + 10240);
    char* vring = sm + 16384;
    char* pbuf = sm + 16384 + 61440;
    bf16* VO = (bf16*)(ws + WS_VO) + (size_t)b * 20 * 512;
    const int ng = wid < 6 ? 3 : 2;
    auto vdma = [&](int s) {
      const bf16* src = VO + (size_t)s * 80 * 20 * 512;
      const unsigned d = lds_addr(vring) + (s % 3) * 20480;
#pragma unroll
      for (int i = 0; i < 20; ++i) glds16c(src + i * 512, lane * 16, d + i * 1024);
    };
    if (wid == 7) { dep_poll(wr, C_V, 32); vdma(0); vdma(1); }
    if (tid < 128) red[(tid >> 1) * 20 + 14 + (tid & 1)] = 0.f;
    for (int s = 0; s < NSEG; ++s) {
      char* vt = vring + (s % 3) * 20480;
      TSDECL; TS(0);
      if (SLM) __builtin_amdgcn_s_sleep(SLM);
      poll_mem<false>(ws + WS_XCH, s, par, memA, memA, wid, lane);
      if (wid == 7) {
        if (VPOS == 2) { __builtin_amdgcn_s_sleep(SLV); vdma(s + 2 < NSEG ? s + 2 : s + 2 - 3); wait_v<40>(); } else { WAIT_V(20); }
      }
      TS(1);
      LBAR();
      TS(2);
      { f32x4 v = mem_times_w<10>(memA, w, lane);
        const int O = 5 * ((2 * wid + (lane >> 5)) * 2 + (fr >> 3));
        *(u32x2*)(vt + (O >> 3) * 1024 + ((O & 3) * 16 + ((O >> 2) & 1) * 8 + (fr & 7)) * 16 + (fq & 1) * 8) = u32x2{pack2(v[0], v[1]), pack2(v[2], v[3])}; }
      TS(3);
      LBAR();
      TS(4);
      if (wid == 7) {
#pragma unroll
        for (int j = 0; j < 4; ++j) {
          const int i = j * 64 + lane, O = 5 * (i >> 3);
          const int off = (O >> 3) * 1024 + ((O & 3) * 16 + ((O >> 2) & 1) * 8 + (i & 7)) * 16;
          stc(wr, WS_VO, (s * 80 + b) * 20480 + off, canon4(*(const u32x4*)(vt + off)));
        }
        if (s == PS - 1) dep_poll(wr, C_FILL, NFILL);
        if (!((s + 3) & 3) && s + 3 < NSEG) dep_poll(wr, C_V + ((s + 3) >> 2) * 256, 32);
        if (VPOS == 0) { WAIT_L0(); vdma(s + 2 < NSEG ? s + 2 : s + 2 - 3); }
      } else if (s < NSEG - 1) {
        unsigned off[3]; u32x4 vf[3];
#pragma unroll
        for (int i = 0; i < 3; ++i) {
          const int g = i < ng ? wid + 7 * i : wid;
          off[i] = xoff(s, 10240 + g * 1024 + lane * 16, par);
          vf[i] = *(const u32x4*)(vt + g * 1024 + lane * 16);
        }
        __builtin_amdgcn_s_sleep(SLB);
        char* const dst[3] = {pbuf + wid * 3072, pbuf + wid * 3072 + 1024, pbuf + wid * 3072 + 2048};
        u32x4 pv[3];
        if (DL == 1) poll_lds1<3>(ws + WS_XCH, off, dst, lane, pv); else poll_lds<3, 2, STL>(ws + WS_XCH, off, dst, lane, pv);
        TS(5);
        f32x4 acc = ZERO4;
#pragma unroll
        for (int i = 0; i < 3; ++i) if (i < ng) acc = mfma16(pv[i], vf[i], acc);
        if ((fr >> 3) == (fq >> 1)) {
#pragma unroll
          for (int r = 0; r < 4; ++r) red[(((fq & 1) * 4 + r) * 8 + (fr & 7)) * 20 + wid * 2 + (fr >> 3)] = acc[r];
        }
      }
      if (s < NSEG - 1) {
        LBAR();
        TS(6);
        if (VPOS == 1 && wid == 7) { __builtin_amdgcn_s_sleep(SLV); vdma(s + 2 < NSEG ? s + 2 : s + 2 - 3); }
        if (wid == 0) {
          const f32x4* rp = (const f32x4*)(red + lane * 20);
          f32x4 t0 = rp[0] + rp[1], t1 = rp[2] + rp[3]; t0 += t1;
          float v = (t0[0] + t0[1]) + (t0[2] + t0[3]);
          const float nb = __uint_as_float(__builtin_amdgcn_mov_dpp(__float_as_uint(v), 0xB1, 0xf, 0xf, true));
          u32x4 o; o.x = canon(pack2(v, nb));
          o.y = __builtin_amdgcn_mov_dpp(o.x, 0x4E, 0xf, 0xf, true);
          o.z = __builtin_amdgcn_mov_dpp(o.x, 0x104, 0xf, 0xf, true);
          o.w = __builtin_amdgcn_mov_dpp(o.y, 0x104, 0xf, 0xf, true);
          if (!(lane & 7)) {
#pragma unroll
            for (int c = 0; c < NC; ++c) __builtin_amdgcn_raw_buffer_store_b128(o, xr, xoff((s + 1) * NC + c, b * 128 + lane * 2, par), 0, 16);
          }
        }
      }
      TS(7); TSOUT(s);
    }
    WAIT_V(0);
  }
  LBAR();
  }
  const bf16* xn = (const bf16*)(ws + WS_XN);
  unsigned* bc = (unsigned*)(sm + 147456);
  const int g0 = res & 3;
  int qi = slot < 112 ? 4 : 0;
  constexpr unsigned NPQ = 148, NPOST = NFILL + 640 + PSTG * 19 + 94;
  auto deq = [&](int q) { return __hip_atomic_fetch_add((unsigned*)(ws + C_QH + (q < 4 ? ((g0 + q) & 3) : 4) * 256), 1u, __ATOMIC_RELAXED, __HIP_MEMORY_SCOPE_AGENT); };
  unsigned nxt = res < 4 ? bi + 4 : bi;
  if (slot < 112 && ltid() == 0) nxt = deq(qi);
  for (;;) {
    if (ltid() == 0) *bc = nxt;
    LBAR();
    unsigned t = __builtin_amdgcn_readfirstlane(*bc);
    qi = __builtin_amdgcn_readfirstlane(qi);
    if (qi < 4 && t >= NPQ) { ++qi; BAR(); if (ltid() == 0) nxt = deq(qi); continue; }
    if (qi == 4 && t >= NPOST) break;
    const bool pref = qi < 4 && t >= 60 && t + 24 < NPQ;
    if (pref && ltid() == 0) nxt = deq(qi);
    const int tl = ltid();
    unsigned sig, inc = 1;
    if (qi < 4) {
      const int hb = ((g0 + qi) & 3) * 8;
      int rb, j, nv, nh;
      if (t < 32) { rb = t >> 4; j = t & 15; nv = 1; nh = 2; } else if (t < 44) { rb = 2; j = t - 32; nv = 2; nh = 2; }
      else { rb = 3 + ((t - 44) >> 3); j = (t - 44) & 7; nv = 2; nh = 4; }
      rb = __builtin_amdgcn_readfirstlane(rb); j = __builtin_amdgcn_readfirstlane(j); nv = __builtin_amdgcn_readfirstlane(nv); nh = __builtin_amdgcn_readfirstlane(nh);
      if (nv == 1 && j < 8) { task_vvo<1>(xn, Wv, Wo, wr, rb, hb + j, sm, tl); sig = C_V + rb * 256; }
      else if (nv == 2 && j < 4) { task_vvo<2>(xn, Wv, Wo, wr, rb, hb + 2 * j, sm, tl); sig = C_V + rb * 256; inc = 2; }
      else {
        const int hj = (j - (nv == 1 ? 8 : 4)) * nh;
        task_kq(xn, hj < 8 ? Wk : Wq, wr, hj < 8 ? (unsigned)WS_K : (unsigned)WS_Q, rb, hb + (hj & 7), nh, sm, tl);
        sig = (hj < 8 ? C_K : C_Q) + rb * 256; inc = nh;
      }
    } else {
      int stg = 0; unsigned sbase = 0;
      for (;;) {
        const unsigned sz = (stg == 0 ? NFILL : 0) + (stg == PSTG ? 94 : 0) + 40 + (stg >= 1 && stg <= PSTG ? 14 : 0) + (stg >= 2 && stg <= PSTG + 1 ? 5 : 0);
        if (t - sbase < sz) break;
        sbase += sz; ++stg;
      }
      stg = __builtin_amdgcn_readfirstlane(stg); sbase = __builtin_amdgcn_readfirstlane(sbase);
      t -= sbase;
      int ty = -1, ix = 0;
      auto ent = [&](int k, int id, unsigned n) { if (ty < 0) { if (t < n) { ty = k; ix = id; } else t -= n; } };
      ent(6, 0, stg == 0 ? NFILL : 0); ent(4, 0, stg == PSTG ? 54 : 0); ent(5, 0, stg == PSTG ? 40 : 0);
#pragma unroll
      for (int i = 0; i < 4; ++i) ent(1, stg * 4 + i, 10);
      ent(2, stg - 1, stg >= 1 && stg <= PSTG ? 14 : 0); ent(3, stg - 2, stg >= 2 && stg <= PSTG + 1 ? 5 : 0);
      ty = __builtin_amdgcn_readfirstlane(ty); ix = __builtin_amdgcn_readfirstlane(ix); t = __builtin_amdgcn_readfirstlane(t);
      sig = C_FIN + 128;
      if (ty == 1) {
        task_t2(x, wr, Dep{C_V + (ix >> 2) * 256, 32, C_PROG, ix < PS ? ix + 3u : min(ix + T2H, 63u)}, bc, ix, t, sm, tl);
        sig = C_T2 + (ix >> 2) * 256;
      } else if (ty == 2) {
        task_f1(wr, Dep{C_T2 + ix * 256, 40, 0, 0}, (const float*)(ws + WS_CS), Wg, Wu, ix, t * 2, t < 13 ? 2 : 1, sm, tl); sig = C_F1 + ix * 256;
      } else if (ty == 3) {
        dep_wait(wr, Dep{C_F1 + ix * 256, 14, 0, 0}, wid);
        task_f2((const bf16*)(ws + WS_G), Wd, wr, out, ix, t, sm, tl);
      } else if (ty == 4) {
        task_f1p(wr, (const float*)(ws + WS_CS), Wg, Wu, t, sm, tl);
      } else if (ty == 5) {
        task_f2p(wr, Wd, out, t % 20, t / 20, sm, tl);
      } else {
        task_fill(wr, PS + t, tl); sig = C_FILL;
      }
    }
    WAIT_V(0);
    LBAR();
    if (ltid() == 0) {
      __hip_atomic_fetch_add((unsigned*)(ws + sig), inc, __ATOMIC_RELAXED, __HIP_MEMORY_SCOPE_AGENT);
      if (!pref) nxt = deq(qi);
    }
  }
}

extern "C" void kernel_launch(void* const* d_in, const int* in_sizes, int n_in, void* d_out, int out_size, void* d_ws, size_t ws_size, hipStream_t stream) {
  const bf16 *x = (const bf16*)d_in[0], *mem0 = (const bf16*)d_in[1], *Wq = (const bf16*)d_in[2], *Wk = (const bf16*)d_in[3], *Wv = (const bf16*)d_in[4], *Wo = (const bf16*)d_in[5];
  const bf16 *Wg = (const bf16*)d_in[8], *Wu = (const bf16*)d_in[9], *Wd = (const bf16*)d_in[10];
  char* ws = (char*)(((size_t)d_ws + (2 * MB - 1)) & ~(size_t)(2 * MB - 1));
  (void)hipFuncSetAttribute((const void*)k_prologue, hipFuncAttributeMaxDynamicSharedMemorySize, 131072);
  (void)hipFuncSetAttribute((const void*)k_main, hipFuncAttributeMaxDynamicSharedMemorySize, LDS_MAIN);
  k_prologue<<<76 + 160, 512, 131072, stream>>>(x, mem0, Wv, Wo, Wg, Wu, ws);
  k_main<<<256, 512, LDS_MAIN, stream>>>(x, Wq, Wk, Wv, Wo, Wg, Wu, Wd, ws, (bf16*)d_out);
}
```

```cpp
#define NO_KM
#include <hip/hip_runtime.h>
#include <hip/hip_bf16.h>

typedef __hip_bfloat16 bf16;
typedef __bf16 bf16x8 __attribute__((ext_vector_type(8)));
typedef __bf16 bf16x16 __attribute__((ext_vector_type(16)));
typedef float f32x4 __attribute__((ext_vector_type(4)));
typedef unsigned u32x4 __attribute__((ext_vector_type(4)));
typedef unsigned u32x2 __attribute__((ext_vector_type(2)));
typedef unsigned u32x8 __attribute__((ext_vector_type(8)));
typedef short s16x4 __attribute__((ext_vector_type(4)));
typedef __attribute__((address_space(3))) s16x4* lds_s16x4_p;

constexpr int S_ = 2048, D_ = 640, NH = 32, DH = 128, HD = 4096, FF = 1728, NSEG = 64;
constexpr size_t MB = 1u << 20;
#define NC 1
constexpr size_t WS_XCH = 0;
constexpr size_t WS_LP = 4 * MB;
constexpr size_t WS_CTL = 16 * MB;
constexpr size_t WS_Q = 17 * MB;
constexpr size_t WS_K = WS_Q + 16 * MB;
constexpr size_t WS_U = WS_K + 16 * MB;
constexpr size_t WS_VO = WS_U + 25 * MB;
constexpr size_t WS_KM = WS_VO + 100 * MB;
constexpr size_t WS_P = WS_KM + 4 * MB;
constexpr size_t WS_H = WS_P + 5 * MB;
constexpr size_t WS_G = WS_H + 3 * MB;
constexpr size_t WS_XN = WS_G + 7 * MB;
constexpr size_t WS_ST = WS_XN + 3 * MB;
constexpr size_t WS_CS = WS_ST + 1 * MB;
constexpr size_t WS_END = WS_CS + 1 * MB;

#define SLD 8
#define NCAL 8
#define UAUX 0
#define OAUX 0
#define SAUX 16
#define SLP 4
#define T2H 0u
constexpr unsigned C_QH = WS_CTL + 256 * 1, C_PROG = WS_CTL + 256 * 6, C_FIN = WS_CTL + 256 * 7, C_K = WS_CTL + 256 * 8, C_V = WS_CTL + 256 * 24,
                   C_Q = WS_CTL + 256 * 40, C_T2 = WS_CTL + 256 * 56, C_F1 = WS_CTL + 256 * 72, C_T1 = WS_CTL + 256 * 88,
                   C_T2S = WS_CTL + 256 * (152 - 48), C_F1S = WS_CTL + 256 * (168 - 48), C_SEG = WS_CTL + 256 * (184 - 48), C_END = WS_CTL + 256 * 200;
constexpr unsigned C_FILL = C_FIN;
#define PSEG 48
constexpr int PS = PSEG, PSTG = PS / 4, NFILL = NSEG - PS;
constexpr unsigned SENT = 0xFFFFFFFFu;
constexpr unsigned SPIN_CAP = 1u << 18;

__device__ __forceinline__ unsigned pack2(float lo, float hi) {
  typedef __bf16 b2 __attribute__((ext_vector_type(2)));
  typedef float f2 __attribute__((ext_vector_type(2)));
  return __builtin_bit_cast(unsigned, __builtin_convertvector(f2{lo, hi}, b2));
}
__device__ __forceinline__ u32x4 pack8(f32x4 a, f32x4 b) { return u32x4{pack2(a[0], a[1]), pack2(a[2], a[3]), pack2(b[0], b[1]), pack2(b[2], b[3])}; }
__device__ __forceinline__ float bflo(unsigned w) { return __uint_as_float(w << 16); }
__device__ __forceinline__ float bfhi(unsigned w) { return __uint_as_float(w & 0xffff0000u); }
__device__ __forceinline__ unsigned canon(unsigned w) { return min(w, 0xFFFFFFFEu); }
__device__ __forceinline__ u32x4 canon4(u32x4 v) { return u32x4{canon(v.x), canon(v.y), canon(v.z), canon(v.w)}; }
__device__ __forceinline__ unsigned max4(u32x4 v) { return max(max(v.x, v.y), max(v.z, v.w)); }
__device__ __forceinline__ void glds16(const void* base, unsigned voff, unsigned lds) {
  asm volatile("s_mov_b32 m0, %2\n\ts_nop 0\n\tglobal_load_lds_dwordx4 %0, %1" ::"v"(voff), "s"(base), "s"(__builtin_amdgcn_readfirstlane(lds)) : "m0");
}
__device__ __forceinline__ void glds16c(const void* base, unsigned voff, unsigned lds) {
  asm volatile("s_mov_b32 m0, %2\n\ts_nop 0\n\tglobal_load_lds_dwordx4 %0, %1 sc1" ::"v"(voff), "s"(base), "s"(__builtin_amdgcn_readfirstlane(lds)) : "m0");
}
typedef __amdgpu_buffer_rsrc_t rsrc_t;
__device__ __forceinline__ u32x4 ldc(rsrc_t r, unsigned base, unsigned off) { return __builtin_amdgcn_raw_buffer_load_b128(r, off, base, 16); }
__device__ __forceinline__ u32x2 ldc2(rsrc_t r, unsigned base, unsigned off) { return __builtin_amdgcn_raw_buffer_load_b64(r, off, base, 16); }
__device__ __forceinline__ void stc(rsrc_t r, unsigned base, unsigned off, u32x4 v) {
  __builtin_amdgcn_raw_buffer_store_b128(v, r, off, base, SAUX);
  asm volatile("s_nop 1" ::"v"(v));
}
__device__ __forceinline__ void stc2(rsrc_t r, unsigned base, unsigned off, u32x2 v) { __builtin_amdgcn_raw_buffer_store_b64(v, r, off, base, 16); }
template <int N> __device__ __forceinline__ void wait_v() { asm volatile("s_waitcnt vmcnt(%0)" ::"n"(N) : "memory"); }
__device__ __forceinline__ unsigned lds_addr(const void* p) { return (unsigned)(size_t)(const __attribute__((address_space(3))) char*)p; }
__device__ __forceinline__ u32x2 tr_read(const char* p) {
  s16x4 r = __builtin_amdgcn_ds_read_tr16_b64_v4i16((lds_s16x4_p)(p));
  return __builtin_bit_cast(u32x2, r);
}
#define WAIT_V(n) asm volatile("s_waitcnt vmcnt(" #n ")" ::: "memory")
#define WAIT_L0() asm volatile("s_waitcnt lgkmcnt(0)" ::: "memory")
#define BAR() asm volatile("s_barrier" ::: "memory")
#define LBAR() asm volatile("s_waitcnt lgkmcnt(0)\n\ts_barrier" ::: "memory")
__device__ __forceinline__ f32x4 mfma16(u32x4 a, u32x4 b, f32x4 c) {
  return __builtin_amdgcn_mfma_f32_16x16x32_bf16(__builtin_bit_cast(bf16x8, a), __builtin_bit_cast(bf16x8, b), c, 0, 0, 0);
}
__device__ __forceinline__ f32x4 smf(u32x4 a, u32x8 b, f32x4 c, int idx) {
  return __builtin_amdgcn_smfmac_f32_16x16x64_bf16(__builtin_bit_cast(bf16x8, a), __builtin_bit_cast(bf16x16, b), c, idx, 0, 0);
}
__device__ __forceinline__ unsigned xcc_id() { return (unsigned)__builtin_amdgcn_s_getreg(20 | (3 << 11)) & 0xfu; }

__device__ __forceinline__ unsigned dpp_ror8(unsigned v) { return __builtin_amdgcn_mov_dpp(v, 0x128, 0xf, 0xf, true); }
__device__ __forceinline__ float red_max_q(float v) {
  auto a = __builtin_amdgcn_permlane16_swap(__float_as_uint(v), __float_as_uint(v), false, false);
  v = fmaxf(__uint_as_float(a[0]), __uint_as_float(a[1]));
  auto b = __builtin_amdgcn_permlane32_swap(__float_as_uint(v), __float_as_uint(v), false, false);
  return fmaxf(__uint_as_float(b[0]), __uint_as_float(b[1]));
}
__device__ __forceinline__ float red_sum_q(float v) {
  auto a = __builtin_amdgcn_permlane16_swap(__float_as_uint(v), __float_as_uint(v), false, false);
  v = __uint_as_float(a[0]) + __uint_as_float(a[1]);
  auto b = __builtin_amdgcn_permlane32_swap(__float_as_uint(v), __float_as_uint(v), false, false);
  return __uint_as_float(b[0]) + __uint_as_float(b[1]);
}

__device__ __forceinline__ float red_sum8(float v) {
  v += __uint_as_float(__builtin_amdgcn_mov_dpp(__float_as_uint(v), 0xB1, 0xf, 0xf, true));
  v += __uint_as_float(__builtin_amdgcn_mov_dpp(__float_as_uint(v), 0x4E, 0xf, 0xf, true));
  return v + __uint_as_float(__builtin_amdgcn_mov_dpp(__float_as_uint(v), 0x141, 0xf, 0xf, true));
}
__device__ __forceinline__ int bswz(int row) { return (((row >> 2) ^ (row >> 3)) & 1) << 1; }
__device__ __forceinline__ void stageB_rc(int p, int& row, int& ch) {
  row = ((p >> 7) << 3) | ((p >> 2) & 7);
  ch = (((p >> 5) & 3) << 2) | ((p & 3) ^ bswz(row));
}
template <bool PERM>
__device__ __forceinline__ unsigned trB_base(int lane, int j) {
  const int g = lane >> 4, q = (lane & 15) >> 2, p = lane & 3;
  return PERM ? 2048 * (g >> 1) + 64 * (4 * (g & 1) + q) + 8 * p + 32 * ((j ^ g ^ (g >> 1)) & 1) : 2048 * g + 64 * q + 8 * p + 32 * ((j ^ g) & 1);
}
__device__ __forceinline__ u32x2 tr_read_a(unsigned a) {
  s16x4 r = __builtin_amdgcn_ds_read_tr16_b64_v4i16((lds_s16x4_p)(size_t)a);
  return __builtin_bit_cast(u32x2, r);
}

template <bool SW, bool PERM, int CSTR>
__device__ __forceinline__ void mma_tile(const u32x4& a00, const u32x4& a10, const u32x4& a01, const u32x4& a11, unsigned tb0, unsigned tb1, int so, f32x4 (&acc)[2][4]) {
#pragma unroll
  for (int ks = 0; ks < 2; ++ks) {
    u32x4 b[4];
#pragma unroll
    for (int n = 0; n < 4; ++n) {
      const int o = so + 8192 * ks + CSTR * (n >> 1);
      u32x2 lo = tr_read_a(((n & 1) ? tb1 : tb0) + o), hi = tr_read_a((PERM ? ((n & 1) ? tb1 : tb0) + 4096 : ((n & 1) ? tb0 : tb1) + 256) + o);
      b[n] = u32x4{lo.x, lo.y, hi.x, hi.y};
    }
    const u32x4 a0 = ks ? a01 : a00, a1 = ks ? a11 : a10;
#pragma unroll
    for (int n = 0; n < 4; ++n) {
      acc[0][n] = SW ? mfma16(b[n], a0, acc[0][n]) : mfma16(a0, b[n], acc[0][n]);
      acc[1][n] = SW ? mfma16(b[n], a1, acc[1][n]) : mfma16(a1, b[n], acc[1][n]);
    }
  }
}

__device__ __forceinline__ void ln_tile(const bf16* src  , bf16* dst, int lane) {
  const int fr = lane & 15, fq = lane >> 4;
  u32x4 af[20];
#pragma unroll
  for (int s = 0; s < 20; ++s) af[s] = *(const u32x4*)(src + (size_t)fr * D_ + s * 32 + fq * 8);
  float s = 0.f;
#pragma unroll
  for (int i = 0; i < 20; ++i)
#pragma unroll
    for (int e = 0; e < 4; ++e) s += bflo(af[i][e]) + bfhi(af[i][e]);
  s += __shfl_xor(s, 16); s += __shfl_xor(s, 32);
  const float mean = s * (1.f / 640.f);
  float q = 0.f;
#pragma unroll
  for (int i = 0; i < 20; ++i)
#pragma unroll
    for (int e = 0; e < 4; ++e) { float a = bflo(af[i][e]) - mean, b = bfhi(af[i][e]) - mean; q += a * a + b * b; }
  q += __shfl_xor(q, 16); q += __shfl_xor(q, 32);
  const float rstd = 1.f / sqrtf(q * (1.f / 640.f) + 1e-5f);
#pragma unroll
  for (int i = 0; i < 20; ++i) {
    u32x4 o;
#pragma unroll
    for (int e = 0; e < 4; ++e) o[e] = pack2((bflo(af[i][e]) - mean) * rstd, (bfhi(af[i][e]) - mean) * rstd);
    *(u32x4*)(dst + i * 512 + lane * 8) = o;
  }
}
__device__ __forceinline__ void load_af(const bf16* src, u32x4 (&af)[2][20], int lane) {
#pragma unroll
  for (int s = 0; s < 20; ++s)
#pragma unroll
    for (int m = 0; m < 2; ++m) af[m][s] = *(const u32x4*)(src + (m * 20 + s) * 512 + lane * 8);
}

__device__ __forceinline__ void load_afc(rsrc_t r, unsigned off, u32x4 (&af)[2][20], int lane) {
#pragma unroll
  for (int s = 0; s < 20; ++s)
#pragma unroll
    for (int m = 0; m < 2; ++m) af[m][s] = ldc(r, off, (m * 20 + s) * 1024 + lane * 16);
}
__device__ __forceinline__ void dep_poll(rsrc_t r, unsigned off, unsigned target) {
  const unsigned t0 = (unsigned)__builtin_amdgcn_s_memrealtime();
  while ((unsigned)__builtin_amdgcn_readfirstlane(__builtin_amdgcn_raw_buffer_load_b32(r, 0, off, 16)) < target) {
    if ((unsigned)__builtin_amdgcn_s_memrealtime() - t0 > 5000000u) break;
    __builtin_amdgcn_s_sleep(SLD);
  }
}
struct Dep { unsigned o0, t0, o1, t1; };
__device__ __forceinline__ void dep_wait(rsrc_t r, const Dep& d, int wid) {
  if (wid == 0) { dep_poll(r, d.o0, d.t0); if (d.t1) dep_poll(r, d.o1, d.t1); }
  BAR();
}
__device__ __forceinline__ bool dep_first(rsrc_t r, const Dep& d, int wid, unsigned* bc) {
  if (wid == 0) {
    const unsigned a = __builtin_amdgcn_raw_buffer_load_b32(r, 0, d.o0, 16), b = __builtin_amdgcn_raw_buffer_load_b32(r, 0, d.o1, 16);
    if ((unsigned)__builtin_amdgcn_readfirstlane(a) < d.t0) dep_poll(r, d.o0, d.t0);
    bc[1] = b;
  }
  LBAR();
  return (unsigned)__builtin_amdgcn_readfirstlane(bc[1]) >= d.t1;
}
__device__ __forceinline__ void dep_second(rsrc_t r, const Dep& d, int wid) {
  if (wid == 0) dep_poll(r, d.o1, d.t1);
  BAR();
}
constexpr int RING = 5;
#define ZERO4 f32x4{0.f, 0.f, 0.f, 0.f}
__device__ __forceinline__ void ring_wait(int t, int T) {
  const int rem = T - 1 - t;
  if (rem >= 3) { WAIT_V(6); } else if (rem == 2) { WAIT_V(4); } else if (rem == 1) { WAIT_V(2); } else { WAIT_V(0); }
  LBAR();
}

__device__ __forceinline__ void task_kq(const bf16* x, const bf16* W, rsrc_t wr, unsigned out, int rb, int h0, int nh, char* sm, int tid) {
  const int lane = tid & 63, wid = __builtin_amdgcn_readfirstlane(tid >> 6), wm = wid >> 1, wn = wid & 1;
  const unsigned lw = lds_addr(sm) + wid * 1024;
  const unsigned tb0 = lds_addr(sm) + wn * 1024 + trB_base<false>(lane, 0), tb1 = lds_addr(sm) + wn * 1024 + trB_base<false>(lane, 1);
  int r0, c0, r1, c1; stageB_rc(tid, r0, c0); stageB_rc(tid + 512, r1, c1);
  const unsigned o0 = (r0 * HD + c0 * 8) * 2, o1 = (r1 * HD + c1 * 8) * 2;
  const int T = nh * 10;
  auto issue = [&](int hh, int u) {
    const bf16* b = W + (size_t)(u * 64) * HD + (h0 + hh) * DH;
    const unsigned d = lw + (u % RING) * 16384;
    glds16(b, o0, d); glds16(b, o1, d + 8192);
  };
#pragma unroll
  for (int u = 0; u < RING - 1; ++u) issue(0, u);
  u32x4 af[2][20];
  load_af(x + (size_t)(rb * 8 + wm * 2) * 10240, af, lane);
  for (int hh = 0; hh < nh; ++hh) {
    f32x4 acc[2][4] = {};
#pragma unroll
    for (int u = 0; u < 10; ++u) {
      if (u < 4) { WAIT_V(10); LBAR(); } else ring_wait(hh * 10 + u, T);
      if (u + RING - 1 < 10) issue(hh, u + RING - 1); else if (hh + 1 < nh) issue(hh + 1, u + RING - 1 - 10);
      mma_tile<true, false, 512>(af[0][2 * u], af[1][2 * u], af[0][2 * u + 1], af[1][2 * u + 1], tb0, tb1, (u % RING) * 16384, acc);
    }
    const unsigned o = out + (((rb * 4 + wm) * NH + h0 + hh) * 8 + 2 * wn) * 1024;
#pragma unroll
    for (int m = 0; m < 2; ++m)
#pragma unroll
      for (int t = 0; t < 2; ++t) stc(wr, o, (m * 4 + t) * 1024 + lane * 16, pack8(acc[m][2 * t], acc[m][2 * t + 1]));
  }
}

constexpr int vvo_allow(int u, int T) {
  int n = 0;
  for (int j = u + 1; j <= u + 3 && j < T; ++j) n += 2;
  for (int j = u - 4; j < u; ++j) if (j >= T / 2 && (j & 1)) n += 4;
  return n;
}
template <int NHH>
__device__ __forceinline__ void task_vvo(const bf16* x, const bf16* Wv, const bf16* Wo, rsrc_t wr, int rb, int h0, char* sm, int tid) {
  const int lane = tid & 63, wid = __builtin_amdgcn_readfirstlane(tid >> 6), wm = wid >> 1, wn = wid & 1;
  const unsigned lw = lds_addr(sm) + wid * 1024;
  const unsigned tb0 = lds_addr(sm) + wn * 1024 + trB_base<false>(lane, 0), tb1 = lds_addr(sm) + wn * 1024 + trB_base<false>(lane, 1);
  const unsigned tp0 = lds_addr(sm) + wn * 1024 + trB_base<true>(lane, 0), tp1 = lds_addr(sm) + wn * 1024 + trB_base<true>(lane, 1);
  char* vbuf = sm + RING * 16384;
  int r0, c0, r1, c1; stageB_rc(tid, r0, c0); stageB_rc(tid + 512, r1, c1);
  const unsigned o0 = (r0 * HD + c0 * 8) * 2, o1 = (r1 * HD + c1 * 8) * 2, p0 = (r0 * D_ + c0 * 8) * 2, p1 = (r1 * D_ + c1 * 8) * 2;
  constexpr int T = NHH * 20;
  auto issue = [&](int t) {
    const unsigned d = lw + (t % RING) * 16384;
    if (t < NHH * 10) {
      const bf16* b = Wv + (size_t)((t % 10) * 64) * HD + (h0 + t / 10) * DH;
      glds16(b, o0, d); glds16(b, o1, d + 8192);
    } else {
      const int v = (t - NHH * 10) % 10, h = h0 + (t - NHH * 10) / 10;
      const bf16* b = Wo + (size_t)(h * DH + (v & 1) * 64) * D_ + (v >> 1) * 128;
      glds16(b, p0, d); glds16(b, p1, d + 8192);
    }
  };
#pragma unroll
  for (int u = 0; u < RING - 1; ++u) issue(u);
  {
    u32x4 af[2][20];
    load_af(x + (size_t)(rb * 8 + wm * 2) * 10240, af, lane);
#pragma unroll
    for (int hh = 0; hh < NHH; ++hh) {
      f32x4 acc[2][4] = {};
#pragma unroll
      for (int u = 0; u < 10; ++u) {
        ring_wait(hh * 10 + u, T);
        issue(hh * 10 + u + RING - 1);
        mma_tile<true, false, 512>(af[0][2 * u], af[1][2 * u], af[0][2 * u + 1], af[1][2 * u + 1], tb0, tb1, (u % RING) * 16384, acc);
      }
#pragma unroll
      for (int m = 0; m < 2; ++m)
#pragma unroll
        for (int t = 0; t < 2; ++t) *(u32x4*)(vbuf + (((hh * 4 + wm) * 2 + m) * 4 + 2 * wn + t) * 1024 + lane * 16) = pack8(acc[m][2 * t], acc[m][2 * t + 1]);
    }
  }
#pragma unroll
  for (int hh = 0; hh < NHH; ++hh) {
    const int h = h0 + hh;
    const int seg = rb * 4 + wm, O = 5 * h + 1 + (lane >> 4);
    const unsigned vo = ((seg * 80 * 20 + (O >> 3)) * 64 + (O & 3) * 16 + ((O >> 2) & 1) * 8 + (lane & 7)) * 16 + ((lane >> 3) & 1) * 20480;
    u32x4 vf[2][4];
#pragma unroll
    for (int nt = 0; nt < 5; ++nt) {
      f32x4 acc[2][4] = {};
#pragma unroll
      for (int kh = 0; kh < 2; ++kh) {
        const int u = NHH * 10 + hh * 10 + nt * 2 + kh;
        switch (vvo_allow(u, T)) {
          case 14: WAIT_V(14); break; case 12: WAIT_V(12); break; case 10: WAIT_V(10); break; case 8: WAIT_V(8); break;
          default: WAIT_V(6); break;
        }
        LBAR();
        if (u + RING - 1 < T) issue(u + RING - 1);
        if (nt == 0 && kh == 0) {
#pragma unroll
          for (int m = 0; m < 2; ++m)
#pragma unroll
            for (int t = 0; t < 4; ++t) vf[m][t] = *(const u32x4*)(vbuf + (((hh * 4 + wm) * 2 + m) * 4 + t) * 1024 + lane * 16);
        }
        mma_tile<false, true, 512>(vf[0][2 * kh], vf[1][2 * kh], vf[0][2 * kh + 1], vf[1][2 * kh + 1], tp0, tp1, (u % RING) * 16384, acc);
      }
#pragma unroll
      for (int n = 0; n < 4; ++n)
        stc(wr, WS_VO + (nt * 16 + wn * 8 + n * 2) * 20480, vo, pack8(acc[0][n], acc[1][n]));
    }
  }
}

__device__ __forceinline__ void task_u(const bf16* Wv, const bf16* Wo, bf16* U, int h, int db, char* sm, int tid) {
  const int lane = tid & 63, wid = __builtin_amdgcn_readfirstlane(tid >> 6), wm = wid >> 1, wn = wid & 1;
  const unsigned lw = lds_addr(sm) + wid * 1024;
  const unsigned tb0 = lds_addr(sm) + wn * 1024 + trB_base<false>(lane, 0), tb1 = lds_addr(sm) + wn * 1024 + trB_base<false>(lane, 1);
  int r0, c0, r1, c1; stageB_rc(tid, r0, c0); stageB_rc(tid + 512, r1, c1);
  const unsigned p0 = (r0 * D_ + c0 * 8) * 2, p1 = (r1 * D_ + c1 * 8) * 2;
  auto issue = [&](int v) {
    const unsigned d = lw + (v % RING) * 16384;
    const bf16* b = Wo + (size_t)(h * DH + (v & 1) * 64) * D_ + (v >> 1) * 128;
    glds16(b, p0, d); glds16(b, p1, d + 8192);
  };
#pragma unroll
  for (int u = 0; u < RING - 1; ++u) issue(u);
  u32x4 vf[2][4];
#pragma unroll
  for (int m = 0; m < 2; ++m)
#pragma unroll
    for (int s = 0; s < 4; ++s) vf[m][s] = *(const u32x4*)(Wv + (size_t)(db * 128 + wm * 32 + m * 16 + (lane & 15)) * HD + h * DH + s * 32 + (lane >> 4) * 8);
#pragma unroll
  for (int nt = 0; nt < 5; ++nt) {
    f32x4 acc[2][4] = {};
#pragma unroll
    for (int kh = 0; kh < 2; ++kh) {
      const int v = nt * 2 + kh;
      ring_wait(v, 10);
      if (v + RING - 1 < 10) issue(v + RING - 1);
      mma_tile<true, false, 512>(vf[0][2 * kh], vf[1][2 * kh], vf[0][2 * kh + 1], vf[1][2 * kh + 1], tb0, tb1, (v % RING) * 16384, acc);
    }
#pragma unroll
    for (int m = 0; m < 2; ++m)
#pragma unroll
      for (int t = 0; t < 2; ++t) {
        const int d1 = db * 128 + wm * 32 + m * 16 + (lane & 15);
        const int c8 = nt * 16 + wn * 8 + t * 4 + ((lane >> 4) & 1) * 2 + (lane >> 5);
        auto s0 = __builtin_amdgcn_permlane16_swap(pack2(acc[m][2 * t][0], acc[m][2 * t][1]), pack2(acc[m][2 * t + 1][0], acc[m][2 * t + 1][1]), false, false);
        auto s1 = __builtin_amdgcn_permlane16_swap(pack2(acc[m][2 * t][2], acc[m][2 * t][3]), pack2(acc[m][2 * t + 1][2], acc[m][2 * t + 1][3]), false, false);
        __builtin_amdgcn_raw_buffer_store_b128(u32x4{s0[0], s1[0], s0[1], s1[1]}, __builtin_amdgcn_make_buffer_rsrc(U, 0, 25 * MB, 0x00020000), ((c8 * NH + h) * D_ + d1) * 16, 0, UAUX);
      }
  }
  LBAR();
}

__device__ __forceinline__ void wave_t1(rsrc_t wr, const char* ql, const u32x4 (&kf)[2][4], const u32x4 (&km)[4], int seg, int h, int lane) {
  const int fr = lane & 15, fq = lane >> 4;
  const float cs = 0.08838834764831845f * 1.4426950408889634f;
#pragma unroll
  for (int i = 0; i < 2; ++i) {
    f32x4 sc[3] = {};
#pragma unroll
    for (int t = 0; t < 4; ++t) {
      const u32x4 qf = *(const u32x4*)(ql + (i * 4 + t) * 1024 + lane * 16);
      sc[0] = mfma16(km[t], qf, sc[0]); sc[1] = mfma16(kf[0][t], qf, sc[1]); sc[2] = mfma16(kf[1][t], qf, sc[2]);
    }
    const int qi = i * 16 + fr;
    float mx = -INFINITY;
#pragma unroll
    for (int r = 0; r < 4; ++r) {
      if (fq >= 2) sc[0][r] = -INFINITY;
      if (fq * 4 + r > qi) sc[1][r] = -INFINITY;
      if (16 + fq * 4 + r > qi) sc[2][r] = -INFINITY;
      mx = fmaxf(mx, fmaxf(sc[0][r], fmaxf(sc[1][r], sc[2][r])));
    }
    mx = red_max_q(mx);
    float sum = 0.f;
#pragma unroll
    for (int k = 0; k < 3; ++k)
#pragma unroll
      for (int r = 0; r < 4; ++r) { sc[k][r] = __builtin_amdgcn_exp2f((sc[k][r] - mx) * cs); sum += sc[k][r]; }
    sum = red_sum_q(sum);
    const float inv = 1.f / sum;
    const unsigned prow = ((seg * 2 + i) * 160 + 5 * h) * 256 + fr * 16;
    stc(wr, WS_P, prow + (1 + fq) * 256, canon4(pack8(sc[1] * inv, sc[2] * inv)));
    if (fq < 2) { u32x2 o2 = {canon(pack2(sc[0][0] * inv, sc[0][1] * inv)), canon(pack2(sc[0][2] * inv, sc[0][3] * inv))}; stc2(wr, WS_P, prow + fq * 8, o2); }
    __builtin_amdgcn_sched_barrier(0);
  }
}

__device__ __forceinline__ void task_t2(const bf16* x, rsrc_t wr, const Dep& dep, unsigned* bc, int seg, int cs, char* sm, int tid) {
  const int lane = tid & 63, wid = __builtin_amdgcn_readfirstlane(tid >> 6), fr = lane & 15, fq = lane >> 4;
  const bool rdy = dep_first(wr, dep, wid, bc);
  f32x4 acc[2][4] = {};
  u32x4 a[5][2], b[5][4];
#pragma unroll
  for (int k = 0; k < 5; ++k) {
    const int O = (wid * 5 + k) * 4 + fq;
#pragma unroll
    for (int n = 0; n < 4; ++n) {
      const int col = cs * 64 + n * 16 + fr;
      b[k][n] = ldc(wr, WS_VO, (((seg * 80 + (col >> 3)) * 20 + (O >> 3)) * 64 + (O & 3) * 16 + ((O >> 2) & 1) * 8 + (col & 7)) * 16);
    }
  }
  const u32x4 xv = *(const u32x4*)(x + (size_t)(seg * 32 + ((tid >> 3) & 31)) * D_ + cs * 64 + (tid & 7) * 8);
  if (!rdy) dep_second(wr, dep, wid);
  const unsigned t0 = (unsigned)__builtin_amdgcn_s_memrealtime();
  unsigned pend = rdy ? 0x3ffu : 0x7ffu;
  for (;;) {
    if (pend >> 10) {
#pragma unroll
      for (int k = 0; k < 5; ++k) {
        const int O = (wid * 5 + k) * 4 + fq;
        if (O % 5 == 0) {
#pragma unroll
          for (int n = 0; n < 4; ++n) {
            const int col = cs * 64 + n * 16 + fr;
            b[k][n] = ldc(wr, WS_VO, (((seg * 80 + (col >> 3)) * 20 + (O >> 3)) * 64 + (O & 3) * 16 + ((O >> 2) & 1) * 8 + (col & 7)) * 16);
          }
        }
      }
    }
#pragma unroll
    for (int k = 0; k < 5; ++k)
#pragma unroll
      for (int m = 0; m < 2; ++m) if (pend >> (k * 2 + m) & 1) a[k][m] = ldc(wr, WS_P, ((seg * 2 + m) * 40 + wid * 5 + k) * 1024 + lane * 16);
    if (seg < PS) break;
    unsigned mx = 0;
#pragma unroll
    for (int k = 0; k < 5; ++k)
      if (((wid * 5 + k) * 4 + fq) % 5 == 0) mx = max(max(mx, max(max4(b[k][0]), max4(b[k][1]))), max(max4(b[k][2]), max4(b[k][3])));
    pend = __any(mx == SENT) ? 0x400u : 0u;
#pragma unroll
    for (int k = 0; k < 5; ++k)
#pragma unroll
      for (int m = 0; m < 2; ++m) if (__any(max4(a[k][m]) == SENT)) pend |= 1u << (k * 2 + m);
    if (!pend || (unsigned)__builtin_amdgcn_s_memrealtime() - t0 > 5000000u) break;
    if (SLP) __builtin_amdgcn_s_sleep(SLP);
  }
#pragma unroll
  for (int k = 0; k < 5; ++k)
#pragma unroll
    for (int m = 0; m < 2; ++m)
#pragma unroll
      for (int n = 0; n < 4; ++n) acc[m][n] = mfma16(b[k][n], a[k][m], acc[m][n]);
  float* red = (float*)sm;
#pragma unroll
  for (int m = 0; m < 2; ++m)
#pragma unroll
    for (int n = 0; n < 4; ++n) *(f32x4*)(red + (wid * 32 + m * 16 + fr) * 68 + n * 16 + fq * 4) = acc[m][n];
  LBAR();
  if (tid < 256) {
    const int row = tid >> 3, c8 = (tid & 7) * 8;
    const int col = cs * 64 + c8, grow = seg * 32 + row;
    f32x4 s0 = *(const f32x4*)(red + row * 68 + c8), s1 = *(const f32x4*)(red + row * 68 + c8 + 4);
#pragma unroll
    for (int w = 1; w < 8; ++w) { s0 += *(const f32x4*)(red + (w * 32 + row) * 68 + c8); s1 += *(const f32x4*)(red + (w * 32 + row) * 68 + c8 + 4); }
    const u32x4 av = pack8(s0, s1);
    u32x4 o;
#pragma unroll
    for (int e = 0; e < 4; ++e) o[e] = canon(pack2(bflo(xv[e]) + bflo(av[e]), bfhi(xv[e]) + bfhi(av[e])));
    stc(wr, WS_H, ((((grow >> 4) * 20 + (col >> 5)) * 4 + ((col >> 3) & 3)) * 16 + (grow & 15)) * 16, o);
    float sm8 = 0.f;
#pragma unroll
    for (int e = 0; e < 4; ++e) sm8 += bflo(o[e]) + bfhi(o[e]);
    sm8 = red_sum8(sm8);
    const float mu = sm8 * (1.f / 64.f);
    float m2 = 0.f;
#pragma unroll
    for (int e = 0; e < 4; ++e) { const float a = bflo(o[e]) - mu, b = bfhi(o[e]) - mu; m2 += a * a + b * b; }
    m2 = red_sum8(m2);
    if ((tid & 7) == 0) stc2(wr, WS_ST, (grow * 10 + cs) * 8, u32x2{canon(__float_as_uint(sm8)), canon(__float_as_uint(m2))});
  }
}

__device__ __forceinline__ void task_f1(rsrc_t wr, const Dep& dep, const float* CS, const bf16* Wg, const bf16* Wu, int rb, int f0, int nt, char* sm, int tid) {
  const int lane = tid & 63, wid = __builtin_amdgcn_readfirstlane(tid >> 6), wm = wid >> 1, wn = wid & 1;
  const unsigned lw = lds_addr(sm) + wid * 1024;
  const unsigned tb0 = lds_addr(sm) + wn * 512 + trB_base<false>(lane, 0), tb1 = lds_addr(sm) + wn * 512 + trB_base<false>(lane, 1);
  int r0, c0, r1, c1; stageB_rc(tid, r0, c0); stageB_rc(tid + 512, r1, c1);
  const bf16* pw = (wid & 1) ? Wu : Wg;
  const unsigned o0 = (r0 * FF + (c0 & 7) * 8) * 2, o1 = (r1 * FF + (c1 & 7) * 8) * 2;
  const int T = nt * 10;
  auto issue = [&](int ff, int u) {
    const size_t o = (size_t)(u * 64) * FF + (f0 + ff) * 64;
    const unsigned d = lw + (u % RING) * 16384;
    glds16(pw + o, o0, d); glds16(pw + o, o1, d + 8192);
  };
#pragma unroll
  for (int u = 0; u < RING - 1; ++u) issue(0, u);
  u32x4 af[2][20];
  float* lcs = (float*)(sm + RING * 16384);
  if (tid < nt * 128) lcs[tid] = CS[((tid >> 6) & 1) * FF + (f0 + (tid >> 7)) * 64 + (tid & 63)];
  dep_wait(wr, dep, wid);
  load_afc(wr, (unsigned)WS_H + (rb * 8 + wm * 2) * 20480, af, lane);
  float mean[2], rstd[2];
#pragma unroll
  for (int m = 0; m < 2; ++m) {
    const unsigned sp = (rb * 128 + wm * 32 + m * 16 + (lane & 15)) * 80;
    f32x4 p[5];
#pragma unroll
    for (int i = 0; i < 5; ++i) p[i] = __builtin_bit_cast(f32x4, ldc(wr, WS_ST, sp + i * 16));
    float s = 0.f;
#pragma unroll
    for (int i = 0; i < 5; ++i) s += p[i][0] + p[i][2];
    mean[m] = s * (1.f / 640.f);
    float q = 0.f;
#pragma unroll
    for (int i = 0; i < 5; ++i) {
      const float d0 = p[i][0] * (1.f / 64.f) - mean[m], d1 = p[i][2] * (1.f / 64.f) - mean[m];
      q += p[i][1] + p[i][3] + 64.f * (d0 * d0 + d1 * d1);
    }
    rstd[m] = 1.f / sqrtf(q * (1.f / 640.f) + 1e-5f);
  }
  for (int ff = 0; ff < nt; ++ff) {
    f32x4 acc[2][4] = {};
#pragma unroll
    for (int u = 0; u < 10; ++u) {
      if (u < 4) { WAIT_V(8); LBAR(); } else ring_wait(ff * 10 + u, T);
      if (u + RING - 1 < 10) issue(ff, u + RING - 1); else if (ff + 1 < nt) issue(ff + 1, u + RING - 1 - 10);
      mma_tile<true, false, 1024>(af[0][2 * u], af[1][2 * u], af[0][2 * u + 1], af[1][2 * u + 1], tb0, tb1, (u % RING) * 16384, acc);
    }
    f32x4 cs[4];
#pragma unroll
    for (int n = 0; n < 4; ++n) cs[n] = *(const f32x4*)(lcs + ff * 128 + (n >> 1) * 64 + (2 * wn + (n & 1)) * 16 + (lane >> 4) * 4);
#pragma unroll
    for (int m = 0; m < 2; ++m) {
#pragma unroll
      for (int n = 0; n < 4; ++n) acc[m][n] = (acc[m][n] - mean[m] * cs[n]) * rstd[m];
      u32x4 g = pack8(acc[m][0], acc[m][1]), u = pack8(acc[m][2], acc[m][3]), o;
      auto rl = [](float v) { return v > 0.f ? v : 0.f * v; };
#pragma unroll
      for (int e = 0; e < 4; ++e) o[e] = pack2(rl(bflo(g[e])) * bflo(u[e]), rl(bfhi(g[e])) * bfhi(u[e]));
      stc(wr, WS_G, ((rb * 8 + wm * 2 + m) * 54 + (f0 + ff) * 2 + wn) * 1024 + lane * 16, o);
    }
  }
}

__device__ __forceinline__ void task_f2(const bf16* G, const bf16* Wd, rsrc_t wr, bf16* out, int rb, int cs, char* sm, int tid) {
  const int lane = tid & 63, wid = __builtin_amdgcn_readfirstlane(tid >> 6), wm = wid >> 1, wn = wid & 1;
  const unsigned lw = lds_addr(sm) + wid * 1024;
  const unsigned tp0 = lds_addr(sm) + wn * 1024 + trB_base<true>(lane, 0), tp1 = lds_addr(sm) + wn * 1024 + trB_base<true>(lane, 1);
  int r0, c0, r1, c1; stageB_rc(tid, r0, c0); stageB_rc(tid + 512, r1, c1);
  const bf16* pa = G + (size_t)(rb * 8 + wid) * 54 * 512;
  const bf16* pb = Wd + cs * 128;
  const unsigned o0 = (r0 * D_ + c0 * 8) * 2, o1 = (r1 * D_ + c1 * 8) * 2;
  constexpr int R4 = 4;
  auto issue = [&](int t) {
    const unsigned d = lds_addr(sm) + (t % R4) * 32768;
    glds16c(pa + t * 1024, lane * 16, d + wid * 2048); glds16c(pa + t * 1024 + 512, lane * 16, d + wid * 2048 + 1024);
    glds16(pb + (size_t)t * 64 * D_, o0, d + 16384 + wid * 1024); glds16(pb + (size_t)t * 64 * D_, o1, d + 24576 + wid * 1024);
  };
  (void)lw;
  issue(0); issue(1); issue(2);
  f32x4 acc[2][4] = {};
  for (int t = 0; t < 27; ++t) {
    if (t + 2 < 27) { WAIT_V(8); } else if (t + 1 < 27) { WAIT_V(4); } else { WAIT_V(0); }
    LBAR();
    if (t + 3 < 27) issue(t + 3);
    const char* d = sm + (t % R4) * 32768;
    const u32x4 a00 = *(const u32x4*)(d + (wm * 2) * 2048 + lane * 16), a01 = *(const u32x4*)(d + (wm * 2) * 2048 + 1024 + lane * 16);
    const u32x4 a10 = *(const u32x4*)(d + (wm * 2 + 1) * 2048 + lane * 16), a11 = *(const u32x4*)(d + (wm * 2 + 1) * 2048 + 1024 + lane * 16);
    mma_tile<true, true, 512>(a00, a10, a01, a11, tp0 + (t % R4) * 32768, tp1 + (t % R4) * 32768, 16384, acc);
  }
#pragma unroll
  for (int m = 0; m < 2; ++m)
#pragma unroll
    for (int n = 0; n < 4; ++n) {
      const size_t go = (size_t)(rb * 128 + wm * 32 + m * 16 + (lane & 15)) * D_ + cs * 128 + wn * 64 + n * 16 + (lane >> 4) * 4;
      const int col = cs * 128 + wn * 64 + n * 16 + (lane >> 4) * 4;
      u32x2 hv = ldc2(wr, WS_H, ((((rb * 8 + wm * 2 + m) * 20 + (col >> 5)) * 64 + ((col >> 3) & 3) * 16 + (lane & 15)) * 8 + (col & 7)) * 2);
      unsigned a0 = pack2(acc[m][n][0], acc[m][n][1]), a1 = pack2(acc[m][n][2], acc[m][n][3]);
      u32x2 o = {pack2(bflo(hv.x) + bflo(a0), bfhi(hv.x) + bfhi(a0)), pack2(bflo(hv.y) + bflo(a1), bfhi(hv.y) + bfhi(a1))};
      __builtin_amdgcn_raw_buffer_store_b64(o, __builtin_amdgcn_make_buffer_rsrc(out, 0, S_ * D_ * 2, 0x00020000), (unsigned)go * 2, 0, OAUX);
    }
}

__device__ __forceinline__ void task_f1p(rsrc_t wr, const float* CS, const bf16* Wg, const bf16* Wu, int j, char* sm, int tid) {
  const int lane = tid & 63, wid = __builtin_amdgcn_readfirstlane(tid >> 6), kq = wid & 3, ch = wid >> 2, fr = lane & 15, fq = lane >> 4;
  u32x4 w[4][5];
#pragma unroll
  for (int gc = 0; gc < 4; ++gc)
#pragma unroll
    for (int i = 0; i < 5; ++i) {
      const unsigned short* p = (const unsigned short*)((gc >> 1) ? Wu : Wg) + (size_t)((kq * 5 + i) * 32 + fq * 8) * FF + j * 32 + (gc & 1) * 16 + fr;
#pragma unroll
      for (int e = 0; e < 4; ++e) w[gc][i][e] = (unsigned)p[(2 * e) * FF] | ((unsigned)p[(2 * e + 1) * FF] << 16);
    }
  f32x4 cs[2][2];
  dep_wait(wr, Dep{C_FILL, NFILL, 0, 0}, wid);
#pragma unroll
  for (int gu = 0; gu < 2; ++gu)
#pragma unroll
    for (int c = 0; c < 2; ++c) cs[gu][c] = *(const f32x4*)(CS + gu * FF + j * 32 + c * 16 + fq * 4);
  for (int seg = PS; seg < NSEG; seg += 2) {
    const unsigned t0 = (unsigned)__builtin_amdgcn_s_memrealtime();
    u32x4 p_[5] = {};
    const bool last = seg == NSEG - 2;
    if (last ? wid == 7 : wid < 4) {
      for (;;) {
        unsigned mx = 0;
        if (last) mx = max4(ldc(wr, WS_P, (NSEG - 1) * 81920 + lane * 16));
        else {
#pragma unroll
          for (int i = 0; i < 5; ++i) p_[i] = ldc(wr, WS_ST, (seg * 32 + wid * 16 + fr) * 80 + i * 16);
#pragma unroll
          for (int i = 0; i < 5; ++i) mx = max(mx, max4(p_[i]));
        }
        if (!__any(mx == SENT) || (unsigned)__builtin_amdgcn_s_memrealtime() - t0 > 5000000u) break;
        __builtin_amdgcn_s_sleep(SLP);
      }
    }
    BAR();
    u32x4 a[2][5];
    unsigned pend = last && wid < 4 ? 0x7ffu : 0x3ffu;
    for (;;) {
#pragma unroll
      for (int rt = 0; rt < 2; ++rt)
#pragma unroll
        for (int i = 0; i < 5; ++i) if (pend >> (rt * 5 + i) & 1) a[rt][i] = ldc(wr, WS_H, ((seg * 2 + ch * 2 + rt) * 20 + kq * 5 + i) * 1024 + lane * 16);
      if (pend >> 10) {
#pragma unroll
        for (int i = 0; i < 5; ++i) p_[i] = ldc(wr, WS_ST, (seg * 32 + wid * 16 + fr) * 80 + i * 16);
      }
      unsigned mx = 0;
#pragma unroll
      for (int i = 0; i < 5; ++i) mx = max(mx, max4(p_[i]));
      pend = __any(mx == SENT) ? 0x400u : 0u;
#pragma unroll
      for (int rt = 0; rt < 2; ++rt)
#pragma unroll
        for (int i = 0; i < 5; ++i) if (__any(max4(a[rt][i]) == SENT)) pend |= 1u << (rt * 5 + i);
      if (!pend || (unsigned)__builtin_amdgcn_s_memrealtime() - t0 > 5000000u) break;
    }
    f32x4 p[5];
#pragma unroll
    for (int i = 0; i < 5; ++i) p[i] = __builtin_bit_cast(f32x4, p_[i]);
#pragma unroll
    for (int rt = 0; rt < 2; ++rt)
#pragma unroll
      for (int gc = 0; gc < 4; ++gc) {
        f32x4 acc = ZERO4;
#pragma unroll
        for (int i = 0; i < 5; ++i) acc = mfma16(w[gc][i], a[rt][i], acc);
        *(f32x4*)(sm + (((ch * 2 + rt) * 4 + gc) * 4 + kq) * 1024 + lane * 16) = acc;
      }
    LBAR();
    if (wid < 4) {
      float s = 0.f;
#pragma unroll
      for (int i = 0; i < 5; ++i) s += p[i][0] + p[i][2];
      const float mean = s * (1.f / 640.f);
      float q = 0.f;
#pragma unroll
      for (int i = 0; i < 5; ++i) {
        const float d0 = p[i][0] * (1.f / 64.f) - mean, d1 = p[i][2] * (1.f / 64.f) - mean;
        q += p[i][1] + p[i][3] + 64.f * (d0 * d0 + d1 * d1);
      }
      const float rstd = 1.f / sqrtf(q * (1.f / 640.f) + 1e-5f);
      f32x4 v[2][2];
#pragma unroll
      for (int gu = 0; gu < 2; ++gu)
#pragma unroll
        for (int c = 0; c < 2; ++c) {
          const char* b = sm + ((wid * 2 + gu) * 2 + c) * 4096 + lane * 16;
          v[gu][c] = (*(const f32x4*)b + *(const f32x4*)(b + 1024)) + (*(const f32x4*)(b + 2048) + *(const f32x4*)(b + 3072));
          v[gu][c] = (v[gu][c] - mean * cs[gu][c]) * rstd;
        }
      u32x4 g = pack8(v[0][0], v[0][1]), u = pack8(v[1][0], v[1][1]), o;
      auto rl = [](float x) { return x > 0.f ? x : 0.f * x; };
#pragma unroll
      for (int e = 0; e < 4; ++e) o[e] = canon(pack2(rl(bflo(g[e])) * bflo(u[e]), rl(bfhi(g[e])) * bfhi(u[e])));
      stc(wr, WS_G, ((seg * 2 + wid) * 54 + j) * 1024 + lane * 16, o);
    }
  }
}
__device__ __forceinline__ void task_f2p(rsrc_t wr, const bf16* Wd, bf16* out, int j, int par, char* sm, int tid) {
  const int lane = tid & 63, wid = __builtin_amdgcn_readfirstlane(tid >> 6), fr = lane & 15, fq = lane >> 4;
  u32x4 w[2][7];
#pragma unroll
  for (int i = 0; i < 7; ++i) {
    const int ks = min(wid + 8 * i, 53);
#pragma unroll
    for (int ct = 0; ct < 2; ++ct) {
      const unsigned short* p = (const unsigned short*)Wd + (size_t)(ks * 32 + fq * 4) * D_ + j * 32 + (fr >> 2) * 8 + ct * 4 + (fr & 3);
#pragma unroll
      for (int e = 0; e < 4; ++e) {
        const int k0 = (e >> 1) * 16 + (e & 1) * 2;
        w[ct][i][e] = wid + 8 * i < 54 ? (unsigned)p[k0 * D_] | ((unsigned)p[(k0 + 1) * D_] << 16) : 0u;
      }
    }
  }
  dep_wait(wr, Dep{C_FILL, NFILL, 0, 0}, wid);
  for (int seg = PS + par; seg < NSEG; seg += 2) {
    BAR();
    const unsigned t0 = (unsigned)__builtin_amdgcn_s_memrealtime();
    u32x4 a[2][7];
    u32x4 hv;
    for (;;) {
      hv = ldc(wr, WS_H, (((seg * 2 + (wid & 1)) * 20 + j) * 64 + lane) * 16);
      if (!__any(max4(hv) == SENT) || (unsigned)__builtin_amdgcn_s_memrealtime() - t0 > 5000000u) break;
      __builtin_amdgcn_s_sleep(SLP);
    }
    unsigned pend = 0x3fffu;
    for (;;) {
#pragma unroll
      for (int rt = 0; rt < 2; ++rt)
#pragma unroll
        for (int i = 0; i < 7; ++i) if (pend >> (rt * 7 + i) & 1) a[rt][i] = ldc(wr, WS_G, ((seg * 2 + rt) * 54 + min(wid + 8 * i, 53)) * 1024 + lane * 16);
      pend = 0;
#pragma unroll
      for (int rt = 0; rt < 2; ++rt)
#pragma unroll
        for (int i = 0; i < 7; ++i) if (__any(max4(a[rt][i]) == SENT)) pend |= 1u << (rt * 7 + i);
      if (!pend || (unsigned)__builtin_amdgcn_s_memrealtime() - t0 > 5000000u) break;
    }
#pragma unroll
    for (int rt = 0; rt < 2; ++rt)
#pragma unroll
      for (int ct = 0; ct < 2; ++ct) {
        f32x4 acc = ZERO4;
#pragma unroll
        for (int i = 0; i < 7; ++i) acc = mfma16(w[ct][i], a[rt][i], acc);
        *(f32x4*)(sm + ((rt * 2 + ct) * 8 + wid) * 1024 + lane * 16) = acc;
      }
    LBAR();
    if (wid < 2) {
      f32x4 v[2];
#pragma unroll
      for (int ct = 0; ct < 2; ++ct) {
        const char* b = sm + (wid * 2 + ct) * 8192 + lane * 16;
        v[ct] = *(const f32x4*)b;
#pragma unroll
        for (int i = 1; i < 8; ++i) v[ct] += *(const f32x4*)(b + i * 1024);
      }
      const u32x4 av = pack8(v[0], v[1]);
      u32x4 o;
#pragma unroll
      for (int e = 0; e < 4; ++e) o[e] = pack2(bflo(hv[e]) + bflo(av[e]), bfhi(hv[e]) + bfhi(av[e]));
      __builtin_amdgcn_raw_buffer_store_b128(o, __builtin_amdgcn_make_buffer_rsrc(out, 0, S_ * D_ * 2, 0x00020000), ((seg * 32 + wid * 16 + fr) * D_ + j * 32 + fq * 8) * 2, 0, OAUX);
    }
  }
}

__device__ __forceinline__ void task_fill(rsrc_t wr, int seg, int tid) {
  unsigned sv; asm volatile("v_mov_b32 %0, -1" : "=v"(sv));
  const u32x4 S = {sv, sv, sv, sv};
#pragma unroll
  for (int i = 0; i < 10; ++i) stc(wr, WS_P + seg * 81920, (i * 512 + tid) * 16, S);
#pragma unroll
  for (int i = 0; i < 5; ++i) stc(wr, WS_H + seg * 40960, (i * 512 + tid) * 16, S);
  if (tid < 160) stc(wr, WS_ST + seg * 2560, tid * 16, S);
#pragma unroll
  for (int i = 0; i < 14; ++i) if (i * 512 + tid < 6912) stc(wr, WS_G + seg * 110592, (i * 512 + tid) * 16, S);
  const int O = 5 * ((tid >> 3) & 31);
  const unsigned off = (tid >> 8) * 20480 + (O >> 3) * 1024 + ((O & 3) * 16 + ((O >> 2) & 1) * 8 + (tid & 7)) * 16;
#pragma unroll 8
  for (int i = 0; i < 40; ++i) stc(wr, WS_VO + (seg * 80 + i * 2) * 20480, off, S);
}

__device__ __forceinline__ unsigned xoff(int step, unsigned o, unsigned par) {
  return (unsigned)step * 65536u + ((o >> 13) << 14) + (o & 8191u) + (((par >> (step >> 5)) & 1u) << 13);
}

__global__ void __launch_bounds__(512, 2) k_prologue(const bf16* x, const bf16* mem0, const bf16* Wv, const bf16* Wo, const bf16* Wg, const bf16* Wu, char* ws) {
  extern __shared__ __attribute__((aligned(16))) char sm[];
  const int tid = threadIdx.x, bid = blockIdx.x;
  if (bid == 0 || bid == 236) {
    const int g = bid ? 1 : 0;
    if (tid < 64) {
      const __amdgpu_buffer_rsrc_t r = __builtin_amdgcn_make_buffer_rsrc(ws + WS_XCH, 0, NC << 22, 0x00020000);
      unsigned best[2] = {~0u, ~0u};
      for (int k = 0; k < NCAL; ++k)
        for (int p = 0; p < 2; ++p) {
          unsigned off = (g << 21) + (k * 4 + 1) * 65536 + 3 * 16384 + p * 8192 + 6144 + 512;
          asm volatile("s_waitcnt vmcnt(0)" ::: "memory");
          unsigned long long t0 = __builtin_amdgcn_s_memtime();
          unsigned v = __builtin_amdgcn_raw_buffer_load_b32(r, off, 0, 16);
          asm volatile("s_waitcnt vmcnt(0)" ::"v"(v) : "memory");
          unsigned long long t1 = __builtin_amdgcn_s_memtime();
          unsigned d = (unsigned)(t1 - t0); if (d < best[p]) best[p] = d;
        }
      const unsigned bit = ((best[1] < best[0]) != (xcc_id() >= 4)) ? 1u << g : 0u;
      if (tid == 0) { ((unsigned*)(ws + WS_CTL))[g * 16] = bit; if (!g) ((unsigned*)(ws + WS_CTL))[1] = xcc_id(); }
    } else if (tid >= 256 && !g) {
      for (unsigned o = C_QH + (tid - 256) * 16; o < C_END; o += 256 * 16) *(u32x4*)(ws + o) = u32x4{o < C_QH + 1024 && !(o & 255) ? 36u : 0u, 0u, 0u, 0u};
    }
    return;
  }
  if (bid <= 16) {
    for (int i = tid; i < 4 * NC * 4096; i += 512) {
      const int step = (bid - 1) * 4 * NC + (i >> 12);
      const unsigned po = (i & 4095) * 16;
      const unsigned lo = ((po >> 14) << 13) + (po & 8191u);
      if (lo >= 30720u) continue;
      u32x4 v = {SENT, SENT, SENT, SENT};
      if (step < NC && lo < 10240u) {
        const int b = lo >> 7, row = (lo >> 4) & 7;
        v = *(const u32x4*)(mem0 + row * D_ + b * 8);
        v.x = canon(v.x); v.y = canon(v.y); v.z = canon(v.z); v.w = canon(v.w);
      }
      *(u32x4*)(ws + WS_XCH + (size_t)step * 65536 + po) = v;
    }
    return;
  }
  if (bid <= 48) {
    if (tid < 256) { const int rt = (bid - 17) * 4 + (tid >> 6); ln_tile(x + (size_t)rt * 16 * D_, (bf16*)(ws + WS_XN) + (size_t)rt * 10240, tid & 63); }
    return;
  }
  if (bid <= 75) {
    const int f0 = (bid - 49) * 64, ch = tid & 7, rg = tid >> 3;
    float* red = (float*)sm;
#pragma unroll
    for (int w = 0; w < 2; ++w) {
      const bf16* W = w ? Wu : Wg;
      float a[8] = {};
#pragma unroll
      for (int i = 0; i < 10; ++i) {
        u32x4 v = *(const u32x4*)(W + (size_t)(rg * 10 + i) * FF + f0 + ch * 8);
#pragma unroll
        for (int e = 0; e < 4; ++e) { a[2 * e] += bflo(v[e]); a[2 * e + 1] += bfhi(v[e]); }
      }
      *(f32x4*)(red + (w * 64 + rg) * 64 + ch * 8) = f32x4{a[0], a[1], a[2], a[3]};
      *(f32x4*)(red + (w * 64 + rg) * 64 + ch * 8 + 4) = f32x4{a[4], a[5], a[6], a[7]};
    }
    LBAR();
    if (tid < 128) {
      float t = 0.f;
      for (int i = 0; i < 64; ++i) t += red[((tid >> 6) * 64 + i) * 64 + (tid & 63)];
      ((float*)(ws + WS_CS))[(tid >> 6) * FF + f0 + (tid & 63)] = t;
    }
    return;
  }
  const int t = bid - 76;
  task_u(Wv, Wo, (bf16*)(ws + WS_U), t / 5, t % 5, sm, tid);
}

#define TS(i)
#define TSDECL
#define TSOUT(s)
#define AWIN 8
#define SLT 0
#define STAG 6
#define SLA 30
#define SLM 20
#define SLR 8
#define STL 6
#define DL 1
#define SLV 6
#define VPOS 2
#define SLB 16
__device__ __forceinline__ bool frag_bad(u32x4 v) { return max(max(v.x, v.y), max(v.z, v.w)) == SENT; }
__device__ __forceinline__ void glds16n(const void* base, unsigned voff, unsigned lds) {
  asm volatile("s_mov_b32 m0, %2\n\ts_nop 0\n\tglobal_load_lds_dwordx4 %0, %1 nt" ::"v"(voff), "s"(base), "s"(__builtin_amdgcn_readfirstlane(lds)) : "m0");
}
template <int NL, int DEPTH, int SG, bool LOCAL = false>
__device__ __forceinline__ void poll_lds(const char* xb, const unsigned (&off)[NL], char* const (&dst)[NL], int lane, u32x4 (&v)[NL]) {
  unsigned spins = 0;
#pragma unroll
  for (int d = 0; d < DEPTH - 1; ++d) {
#pragma unroll
    for (int i = 0; i < NL; ++i) { if (LOCAL) glds16n(xb, off[i], lds_addr(dst[i])); else glds16c(xb, off[i], lds_addr(dst[i])); }
    __builtin_amdgcn_s_sleep(SG);
  }
  for (;;) {
    if (LOCAL && (spins & 15) != 15) {
#pragma unroll
      for (int i = 0; i < NL; ++i) glds16n(xb, off[i], lds_addr(dst[i]));
    } else {
#pragma unroll
      for (int i = 0; i < NL; ++i) glds16c(xb, off[i], lds_addr(dst[i]));
    }
    wait_v<NL * (DEPTH - 1)>();
    bool bad = false;
#pragma unroll
    for (int i = 0; i < NL; ++i) v[i] = *(const u32x4*)(dst[i] + lane * 16);
#pragma unroll
    for (int i = 0; i < NL; ++i) bad |= frag_bad(v[i]);
    if (!__any(bad) || ++spins > SPIN_CAP) break;
  }
}
template <int NL>
__device__ __forceinline__ void poll_lds1(const char* xb, const unsigned (&off)[NL], char* const (&dst)[NL], int lane, u32x4 (&v)[NL]) {
  unsigned spins = 0, pend = (1u << NL) - 1u;
  for (;;) {
#pragma unroll
    for (int i = 0; i < NL; ++i) if (pend & (1u << i)) glds16c(xb, off[i], lds_addr(dst[i]));
    WAIT_V(0);
#pragma unroll
    for (int i = 0; i < NL; ++i) v[i] = *(const u32x4*)(dst[i] + lane * 16);
    pend = 0;
#pragma unroll
    for (int i = 0; i < NL; ++i) if (__any(frag_bad(v[i]))) pend |= 1u << i;
    if (!pend || ++spins > SPIN_CAP) break;
  }
}
template <class FB, class FO>
__device__ __forceinline__ void load_wfrags(FB base, FO voff, int fstride, u32x8 (&w)[2][10], char* sm, int tid) {
  const int lane = tid & 63, wid = __builtin_amdgcn_readfirstlane(tid >> 6);
  const int bq = lane >> 4, q = (lane & 15) >> 2, p = lane & 3;
  const unsigned lw = lds_addr(sm) + wid * 1024;
  unsigned vo[4];
#pragma unroll
  for (int i = 0; i < 4; ++i) {
    const int ci = i * 512 + tid, half = i >> 1, k = (ci >> 4) & 63;
    vo[i] = voff(k, half * 16 + ((ci & 15) ^ (((k & 3) << 1) | (((k >> 2) & 1) << 3))));
  }
  auto issue = [&](int f) {
#pragma unroll
    for (int i = 0; i < 4; ++i) glds16((const char*)base(i >> 1) + f * fstride, vo[i], lw + (f & 3) * 32768 + i * 8192);
  };
#pragma unroll
  for (int f = 0; f < 4; ++f) issue(f);
#pragma unroll
  for (int f = 0; f < 10; ++f) {
    if (f < 7) { WAIT_V(12); } else if (f == 7) { WAIT_V(8); } else if (f == 8) { WAIT_V(4); } else { WAIT_V(0); }
    BAR();
#pragma unroll
    for (int t = 0; t < 2; ++t) {
      u32x2 qd[4];
#pragma unroll
      for (int z = 0; z < 4; ++z) {
        const int k = 16 * (bq >> 1) + 4 * (bq & 1) + (z & 1) * 8 + (z >> 1) * 32 + q;
        const int cc = (2 * wid + t) * 2 + (p >> 1);
        qd[z] = tr_read(sm + (f & 3) * 32768 + ((cc >> 4) * 1024 + k * 16 + ((cc & 15) ^ (((k & 3) << 1) | (((k >> 2) & 1) << 3)))) * 16 + 8 * (p & 1));
      }
      w[t][f] = u32x8{qd[0].x, qd[1].x, qd[0].y, qd[1].y, qd[2].x, qd[3].x, qd[2].y, qd[3].y};
    }
    LBAR();
    if (f + 4 < 10) issue(f + 4);
  }
}

template <bool FAST>
__device__ __forceinline__ void poll_mem(const char* xb, int s, unsigned par, char* memA, char* other, int wid, int lane) {
  if (wid < 3) {
    unsigned off[2] = {xoff(s, wid * 1024 + lane * 16, par), xoff(s, (7 + wid) * 1024 + lane * 16, par)};
    char* const dst[2] = {memA + wid * 1024, memA + (7 + wid) * 1024};
    u32x4 v[2];
    if (FAST) {
      poll_lds<2, 2, STAG>(xb, off, dst, lane, v);
      *(u32x4*)(other + wid * 1024 + lane * 16) = u32x4{SENT, SENT, SENT, SENT};
      *(u32x4*)(other + (7 + wid) * 1024 + lane * 16) = u32x4{SENT, SENT, SENT, SENT};
    } else poll_lds1<2>(xb, off, dst, lane, v);
  } else if (wid < 7) {
    unsigned off[1] = {xoff(s, wid * 1024 + lane * 16, par)};
    char* const dst[1] = {memA + wid * 1024};
    u32x4 v[1];
    if (FAST) {
      poll_lds<1, 2, STAG>(xb, off, dst, lane, v);
      *(u32x4*)(other + wid * 1024 + lane * 16) = u32x4{SENT, SENT, SENT, SENT};
    } else poll_lds1<1>(xb, off, dst, lane, v);
  }
}
template <int WIN>
__device__ __forceinline__ f32x4 mem_times_w(const char* memA, const u32x8 (&w)[2][10], int lane) {
  const int sidx = (lane & 15) < 8 ? 0x4444 : 0xEEEE;
  u32x4 a[10];
#pragma unroll
  for (int f = 0; f < WIN; ++f) a[f] = *(const u32x4*)(memA + f * 1024 + lane * 16);
  f32x4 a0 = ZERO4, a1 = ZERO4;
#pragma unroll
  for (int f = 0; f < 10; ++f) {
    a0 = smf(a[f], w[0][f], a0, sidx); a1 = smf(a[f], w[1][f], a1, sidx);
    if (f + WIN < 10) { __builtin_amdgcn_sched_barrier(0); a[f + WIN] = *(const u32x4*)(memA + (f + WIN) * 1024 + lane * 16); }
  }
  f32x4 v;
#pragma unroll
  for (int r = 0; r < 4; ++r) {
    auto sw = __builtin_amdgcn_permlane32_swap(__float_as_uint(a0[r]), __float_as_uint(a1[r]), false, false);
    v[r] = __uint_as_float(sw[0]) + __uint_as_float(sw[1]);
  }
  return v;
}

constexpr int LDS_MAIN = 147456 + 64;
#define TPREF 0
__global__ void __launch_bounds__(512, 2) k_main(const bf16* x, const bf16* Wq, const bf16* Wk, const bf16* Wv, const bf16* Wo, const bf16* Wg, const bf16* Wu,
                                                 const bf16* Wd, char* ws, bf16* out) {
  extern __shared__ __attribute__((aligned(16))) char sm[];
  const int tid = threadIdx.x;
  const int wid = __builtin_amdgcn_readfirstlane(tid >> 6);
  unsigned* ctl = (unsigned*)(ws + WS_CTL);
  const int res = (blockIdx.x + __builtin_amdgcn_readfirstlane(ctl[1])) & 7, bi = blockIdx.x >> 3;
  const int slot = res < 4 && bi < 28 ? res * 28 + bi : 1000;
  const rsrc_t wr = __builtin_amdgcn_make_buffer_rsrc(ws, 0, (unsigned)(WS_END + 8 * MB), 0x00020000);
  auto ltid = [&]() { int l; asm volatile("v_mbcnt_lo_u32_b32 %0, -1, 0\n\tv_mbcnt_hi_u32_b32 %0, -1, %0" : "=v"(l)); return wid * 64 + l; };
  if (slot < 112) {
  const int tid = ltid(), lane = tid & 63;
  const int grp = slot / 28, tsl = slot % 28;
  const int role = tsl < 8 ? grp * 8 + tsl : 32 + grp * 20 + (tsl - 8);
  const unsigned par = ctl[0] | ctl[16];
  const int cpy = role % NC;
  const __amdgpu_buffer_rsrc_t xr = __builtin_amdgcn_make_buffer_rsrc(ws + WS_XCH, 0, NC << 22, 0x00020000);
  const int fr = lane & 15, fq = lane >> 4;
  u32x8 w[2][10];
  char* memA = sm;
  if (role < 32) {
    const int h = role;
    load_wfrags([&](int half) { return (half ? Wk : Wq) + h * DH; }, [&](int k, int cc) { return (unsigned)(k * HD + (cc & 15) * 8) * 2u; }, 64 * HD * 2, w, sm, tid);
    char* qk = sm + 10240;
    char* kring = sm + 16384;
    char* memB = sm + 40960;
    char* qring = sm + 51200;
    const bf16* K = (const bf16*)(ws + WS_K) + (size_t)h * 4096;
    const float cs = 0.08838834764831845f * 1.4426950408889634f;
    auto kdma = [&](int s) {
      const bf16* src = K + (size_t)s * NH * 4096;
      const unsigned d = lds_addr(kring) + (s % 3) * 8192;
#pragma unroll
      for (int i = 0; i < 8; ++i) glds16c(src + i * 512, lane * 16, d + i * 1024);
#pragma unroll
      for (int i = 0; i < 8; ++i) glds16c(src - (WS_K - WS_Q) / 2 + i * 512, lane * 16, d + (51200 - 16384) + i * 1024);
    };
    if (wid == 7) { dep_poll(wr, C_K, 32); dep_poll(wr, C_Q, 32); kdma(0); kdma(1); WAIT_V(16); }
    for (int i = tid; i < 640; i += 512) *(u32x4*)(memA + i * 16) = u32x4{SENT, SENT, SENT, SENT};
    LBAR();
    const int Ost = fr < 8 ? 5 * h + 1 + fq : 5 * h;
    const unsigned pst = 10240 + (Ost >> 3) * 1024 + ((Ost & 3) * 16 + ((Ost >> 2) & 1) * 8 + (fr & 7)) * 16;
    for (int s = 0; s < NSEG; ++s) {
      TSDECL; TS(0);
      char* mcur = (s & 1) ? memB : memA;
      u32x4 kf[2][4];
      if (wid == 0 || wid == 7) {
        const char* kt = kring + (s % 3) * 8192 + lane * 16;
#pragma unroll
        for (int t = 0; t < 4; ++t) { kf[0][t] = *(const u32x4*)(kt + t * 1024); kf[1][t] = *(const u32x4*)(kt + 4096 + t * 1024); }
      }
      poll_mem<true>(ws + WS_XCH, s, par, mcur, (s & 1) ? memA : memB, wid, lane);
      if (wid == 7) { WAIT_V(4); }
      TS(1);
      LBAR();
      TS(2);
      if (role == 0 && tid == 384) __builtin_amdgcn_raw_buffer_store_b32(s, wr, C_PROG, 0, 16);
      { f32x4 v = mem_times_w<AWIN>(mcur, w, lane);
        const int col = (2 * wid + (lane >> 5)) * 16 + fr, e = col & 127;
        char* dst = qk + (col >> 7) * 2176 + (fq & 1) * 4 * 272 + (e >> 5) * 64 + ((e >> 2) & 3) * 16 + ((e >> 4) & 1) * 8 + (e & 3) * 2;
#pragma unroll
        for (int r = 0; r < 4; ++r) *(unsigned short*)(dst + r * 272) = (unsigned short)(pack2(v[r], v[r]) & 0xffffu); }
      TS(3);
      LBAR();
      TS(4);
      if (wid == 0) {
        if (s < NSEG - 1) {
          u32x4 qf[4], km[4];
#pragma unroll
          for (int t = 0; t < 4; ++t) {
            qf[t] = *(const u32x4*)(qk + (fr & 7) * 272 + t * 64 + fq * 16);
            km[t] = *(const u32x4*)(qk + 2176 + (fr & 7) * 272 + t * 64 + fq * 16);
          }
          asm volatile("" : "+v"(qf[0]), "+v"(qf[1]), "+v"(qf[2]), "+v"(qf[3]), "+v"(km[0]), "+v"(km[1]), "+v"(km[2]), "+v"(km[3]));
          f32x4 sc[3] = {};
#pragma unroll
          for (int t = 0; t < 4; ++t) { sc[1] = mfma16(kf[0][t], qf[t], sc[1]); sc[2] = mfma16(kf[1][t], qf[t], sc[2]); sc[0] = mfma16(km[t], qf[t], sc[0]); }
          const bool hi = fr >= 8;
          float e[6];
#pragma unroll
          for (int r = 0; r < 4; ++r) e[r] = hi ? sc[2][r] : sc[1][r];
          e[4] = hi ? sc[0][2] : sc[0][0]; e[5] = hi ? sc[0][3] : sc[0][1];
          if (fq >= 2) { e[4] = -INFINITY; e[5] = -INFINITY; }
          float mx = fmaxf(fmaxf(fmaxf(e[0], e[1]), fmaxf(e[2], e[3])), fmaxf(e[4], e[5]));
          mx = fmaxf(mx, __uint_as_float(dpp_ror8(__float_as_uint(mx))));
          mx = red_max_q(mx) * cs;
          float sum = 0.f;
#pragma unroll
          for (int r = 0; r < 6; ++r) { e[r] = __builtin_amdgcn_exp2f(__builtin_fmaf(e[r], cs, -mx)); sum += e[r]; }
          sum += __uint_as_float(dpp_ror8(__float_as_uint(sum)));
          sum = red_sum_q(sum);
          const float inv = __builtin_amdgcn_rcpf(sum);
          const unsigned p0 = canon(pack2(e[0] * inv, e[1] * inv)), p1 = canon(pack2(e[2] * inv, e[3] * inv)), pm = canon(pack2(e[4] * inv, e[5] * inv));
          const unsigned q0 = dpp_ror8(p0), q1 = dpp_ror8(p1), qm = dpp_ror8(pm);
          auto s0 = __builtin_amdgcn_permlane16_swap(qm, qm, false, false);
          auto s1 = __builtin_amdgcn_permlane16_swap(pm, pm, false, false);
          const u32x4 o = hi ? u32x4{qm, pm, s0[1], s1[1]} : u32x4{p0, p1, q0, q1};
          if (fr < 8 || fq == 0) __builtin_amdgcn_raw_buffer_store_b128(o, xr, xoff(s, pst, par), 0, 16);
        }
      } else if (wid == 7) {
        if (s == PS - 1) dep_poll(wr, C_FILL, NFILL);
        if (s + 2 < NSEG) {
          if (!((s + 2) & 3)) { dep_poll(wr, C_K + ((s + 2) >> 2) * 256, 32); dep_poll(wr, C_Q + ((s + 2) >> 2) * 256, 32); }
          kdma(s + 2);
        }
        if (SLT) __builtin_amdgcn_s_sleep(SLT);
        u32x4 km[4];
        const int l7 = ltid() & 63;
#pragma unroll
        for (int t = 0; t < 4; ++t) km[t] = *(const u32x4*)(qk + 2176 + (l7 & 7) * 272 + t * 64 + (l7 >> 4) * 16);
        wave_t1(wr, qring + (s % 3) * 8192, kf, km, s, h, l7);
      }
      TS(5);
      TSOUT(s);
      if (wid < 7) __builtin_amdgcn_s_sleep(SLA);
    }
    WAIT_V(0);
  } else {
    const int b = role - 32;
    const bf16* U = (const bf16*)(ws + WS_U) + (size_t)b * NH * D_ * 8;
    load_wfrags([&](int half) { return U; }, [&](int k, int cc) { return (unsigned)(cc * D_ + k) * 16u; }, 64 * 16, w, sm, tid);
    float* red = (float*)(sm + 10240);
    char* vring = sm + 16384;
    char* pbuf = sm + 16384 + 61440;
    bf16* VO = (bf16*)(ws + WS_VO) + (size_t)b * 20 * 512;
    const int ng = wid < 6 ? 3 : 2;
    auto vdma = [&](int s) {
      const bf16* src = VO + (size_t)s * 80 * 20 * 512;
      const unsigned d = lds_addr(vring) + (s % 3) * 20480;
#pragma unroll
      for (int i = 0; i < 20; ++i) glds16c(src + i * 512, lane * 16, d + i * 1024);
    };
    if (wid == 7) { dep_poll(wr, C_V, 32); vdma(0); vdma(1); }
    if (tid < 128) red[(tid >> 1) * 20 + 14 + (tid & 1)] = 0.f;
    for (int s = 0; s < NSEG; ++s) {
      char* vt = vring + (s % 3) * 20480;
      TSDECL; TS(0);
      if (SLM) __builtin_amdgcn_s_sleep(SLM);
      poll_mem<false>(ws + WS_XCH, s, par, memA, memA, wid, lane);
      if (wid == 7) {
        if (VPOS == 2) { __builtin_amdgcn_s_sleep(SLV); vdma(s + 2 < NSEG ? s + 2 : s + 2 - 3); wait_v<40>(); } else { WAIT_V(20); }
      }
      TS(1);
      LBAR();
      TS(2);
      { f32x4 v = mem_times_w<10>(memA, w, lane);
        const int O = 5 * ((2 * wid + (lane >> 5)) * 2 + (fr >> 3));
        *(u32x2*)(vt + (O >> 3) * 1024 + ((O & 3) * 16 + ((O >> 2) & 1) * 8 + (fr & 7)) * 16 + (fq & 1) * 8) = u32x2{pack2(v[0], v[1]), pack2(v[2], v[3])}; }
      TS(3);
      LBAR();
      TS(4);
      if (wid == 7) {
#pragma unroll
        for (int j = 0; j < 4; ++j) {
          const int i = j * 64 + lane, O = 5 * (i >> 3);
          const int off = (O >> 3) * 1024 + ((O & 3) * 16 + ((O >> 2) & 1) * 8 + (i & 7)) * 16;
          stc(wr, WS_VO, (s * 80 + b) * 20480 + off, canon4(*(const u32x4*)(vt + off)));
        }
        if (s == PS - 1) dep_poll(wr, C_FILL, NFILL);
        if (!((s + 3) & 3) && s + 3 < NSEG) dep_poll(wr, C_V + ((s + 3) >> 2) * 256, 32);
        if (VPOS == 0) { WAIT_L0(); vdma(s + 2 < NSEG ? s + 2 : s + 2 - 3); }
      } else if (s < NSEG - 1) {
        unsigned off[3]; u32x4 vf[3];
#pragma unroll
        for (int i = 0; i < 3; ++i) {
          const int g = i < ng ? wid + 7 * i : wid;
          off[i] = xoff(s, 10240 + g * 1024 + lane * 16, par);
          vf[i] = *(const u32x4*)(vt + g * 1024 + lane * 16);
        }
        __builtin_amdgcn_s_sleep(SLB);
        char* const dst[3] = {pbuf + wid * 3072, pbuf + wid * 3072 + 1024, pbuf + wid * 3072 + 2048};
        u32x4 pv[3];
        if (DL == 1) poll_lds1<3>(ws + WS_XCH, off, dst, lane, pv); else poll_lds<3, 2, STL>(ws + WS_XCH, off, dst, lane, pv);
        TS(5);
        f32x4 acc = ZERO4;
#pragma unroll
        for (int i = 0; i < 3; ++i) if (i < ng) acc = mfma16(pv[i], vf[i], acc);
        if ((fr >> 3) == (fq >> 1)) {
#pragma unroll
          for (int r = 0; r < 4; ++r) red[(((fq & 1) * 4 + r) * 8 + (fr & 7)) * 20 + wid * 2 + (fr >> 3)] = acc[r];
        }
      }
      if (s < NSEG - 1) {
        LBAR();
        TS(6);
        if (VPOS == 1 && wid == 7) { __builtin_amdgcn_s_sleep(SLV); vdma(s + 2 < NSEG ? s + 2 : s + 2 - 3); }
        if (wid == 0) {
          const f32x4* rp = (const f32x4*)(red + lane * 20);
          f32x4 t0 = rp[0] + rp[1], t1 = rp[2] + rp[3]; t0 += t1;
          float v = (t0[0] + t0[1]) + (t0[2] + t0[3]);
          const float nb = __uint_as_float(__builtin_amdgcn_mov_dpp(__float_as_uint(v), 0xB1, 0xf, 0xf, true));
          u32x4 o; o.x = canon(pack2(v, nb));
          o.y = __builtin_amdgcn_mov_dpp(o.x, 0x4E, 0xf, 0xf, true);
          o.z = __builtin_amdgcn_mov_dpp(o.x, 0x104, 0xf, 0xf, true);
          o.w = __builtin_amdgcn_mov_dpp(o.y, 0x104, 0xf, 0xf, true);
          if (!(lane & 7)) {
#pragma unroll
            for (int c = 0; c < NC; ++c) __builtin_amdgcn_raw_buffer_store_b128(o, xr, xoff((s + 1) * NC + c, b * 128 + lane * 2, par), 0, 16);
          }
        }
      }
      TS(7); TSOUT(s);
    }
    WAIT_V(0);
  }
  LBAR();
  }
  const bf16* xn = (const bf16*)(ws + WS_XN);
  unsigned* bc = (unsigned*)(sm + 147456);
  const int g0 = res & 3;
  int qi = slot < 112 ? 4 : 0;
  constexpr unsigned NPQ = 148, NPOST = NFILL + 640 + PSTG * 19 + 94;
  auto deq = [&](int q) { return __hip_atomic_fetch_add((unsigned*)(ws + C_QH + (q < 4 ? ((g0 + q) & 3) : 4) * 256), 1u, __ATOMIC_RELAXED, __HIP_MEMORY_SCOPE_AGENT); };
  unsigned nxt = res < 4 ? bi + 4 : bi;
  if (slot < 112 && ltid() == 0) nxt = deq(qi);
  for (;;) {
    if (ltid() == 0) *bc = nxt;
    LBAR();
    unsigned t = __builtin_amdgcn_readfirstlane(*bc);
    qi = __builtin_amdgcn_readfirstlane(qi);
    if (qi < 4 && t >= NPQ) { ++qi; BAR(); if (ltid() == 0) nxt = deq(qi); continue; }
    if (qi == 4 && t >= NPOST) break;
    const bool pref = qi < 4 && t >= 60 && t + 24 < NPQ;
    if (pref && ltid() == 0) nxt = deq(qi);
    const int tl = ltid();
    unsigned sig, inc = 1;
    if (qi < 4) {
      const int hb = ((g0 + qi) & 3) * 8;
      int rb, j, nv, nh;
      if (t < 32) { rb = t >> 4; j = t & 15; nv = 1; nh = 2; } else if (t < 44) { rb = 2; j = t - 32; nv = 2; nh = 2; }
      else { rb = 3 + ((t - 44) >> 3); j = (t - 44) & 7; nv = 2; nh = 4; }
      rb = __builtin_amdgcn_readfirstlane(rb); j = __builtin_amdgcn_readfirstlane(j); nv = __builtin_amdgcn_readfirstlane(nv); nh = __builtin_amdgcn_readfirstlane(nh);
      if (nv == 1 && j < 8) { task_vvo<1>(xn, Wv, Wo, wr, rb, hb + j, sm, tl); sig = C_V + rb * 256; }
      else if (nv == 2 && j < 4) { task_vvo<2>(xn, Wv, Wo, wr, rb, hb + 2 * j, sm, tl); sig = C_V + rb * 256; inc = 2; }
      else {
        const int hj = (j - (nv == 1 ? 8 : 4)) * nh;
        task_kq(xn, hj < 8 ? Wk : Wq, wr, hj < 8 ? (unsigned)WS_K : (unsigned)WS_Q, rb, hb + (hj & 7), nh, sm, tl);
        sig = (hj < 8 ? C_K : C_Q) + rb * 256; inc = nh;
      }
    } else {
      int stg = 0; unsigned sbase = 0;
      for (;;) {
        const unsigned sz = (stg == 0 ? NFILL : 0) + (stg == PSTG ? 94 : 0) + 40 + (stg >= 1 && stg <= PSTG ? 14 : 0) + (stg >= 2 && stg <= PSTG + 1 ? 5 : 0);
        if (t - sbase < sz) break;
        sbase += sz; ++stg;
      }
      stg = __builtin_amdgcn_readfirstlane(stg); sbase = __builtin_amdgcn_readfirstlane(sbase);
      t -= sbase;
      int ty = -1, ix = 0;
      auto ent = [&](int k, int id, unsigned n) { if (ty < 0) { if (t < n) { ty = k; ix = id; } else t -= n; } };
      ent(6, 0, stg == 0 ? NFILL : 0); ent(4, 0, stg == PSTG ? 54 : 0); ent(5, 0, stg == PSTG ? 40 : 0);
#pragma unroll
      for (int i = 0; i < 4; ++i) ent(1, stg * 4 + i, 10);
      ent(2, stg - 1, stg >= 1 && stg <= PSTG ? 14 : 0); ent(3, stg - 2, stg >= 2 && stg <= PSTG + 1 ? 5 : 0);
      ty = __builtin_amdgcn_readfirstlane(ty); ix = __builtin_amdgcn_readfirstlane(ix); t = __builtin_amdgcn_readfirstlane(t);
      sig = C_FIN + 128;
      if (ty == 1) {
        task_t2(x, wr, Dep{C_V + (ix >> 2) * 256, 32, C_PROG, ix < PS ? ix + 3u : min(ix + T2H, 63u)}, bc, ix, t, sm, tl);
        sig = C_T2 + (ix >> 2) * 256;
      } else if (ty == 2) {
        task_f1(wr, Dep{C_T2 + ix * 256, 40, 0, 0}, (const float*)(ws + WS_CS), Wg, Wu, ix, t * 2, t < 13 ? 2 : 1, sm, tl); sig = C_F1 + ix * 256;
      } else if (ty == 3) {
        dep_wait(wr, Dep{C_F1 + ix * 256, 14, 0, 0}, wid);
        task_f2((const bf16*)(ws + WS_G), Wd, wr, out, ix, t, sm, tl);
      } else if (ty == 4) {
        task_f1p(wr, (const float*)(ws + WS_CS), Wg, Wu, t, sm, tl);
      } else if (ty == 5) {
        task_f2p(wr, Wd, out, t % 20, t / 20, sm, tl);
      } else {
        task_fill(wr, PS + t, tl); sig = C_FILL;
      }
    }
    WAIT_V(0);
    LBAR();
    if (ltid() == 0) {
      __hip_atomic_fetch_add((unsigned*)(ws + sig), inc, __ATOMIC_RELAXED, __HIP_MEMORY_SCOPE_AGENT);
      if (!pref) nxt = deq(qi);
    }
  }
}

extern "C" void kernel_launch(void* const* d_in, const int* in_sizes, int n_in, void* d_out, int out_size, void* d_ws, size_t ws_size, hipStream_t stream) {
  const bf16 *x = (const bf16*)d_in[0], *mem0 = (const bf16*)d_in[1], *Wq = (const bf16*)d_in[2], *Wk = (const bf16*)d_in[3], *Wv = (const bf16*)d_in[4], *Wo = (const bf16*)d_in[5];
  const bf16 *Wg = (const bf16*)d_in[8], *Wu = (const bf16*)d_in[9], *Wd = (const bf16*)d_in[10];
  char* ws = (char*)(((size_t)d_ws + (2 * MB - 1)) & ~(size_t)(2 * MB - 1));
  (void)hipFuncSetAttribute((const void*)k_prologue, hipFuncAttributeMaxDynamicSharedMemorySize, 131072);
  (void)hipFuncSetAttribute((const void*)k_main, hipFuncAttributeMaxDynamicSharedMemorySize, LDS_MAIN);
  k_prologue<<<76 + 161, 512, 131072, stream>>>(x, mem0, Wv, Wo, Wg, Wu, ws);
  k_main<<<256, 512, LDS_MAIN, stream>>>(x, Wq, Wk, Wv, Wo, Wg, Wu, Wd, ws, (bf16*)d_out);
}
```

```cpp
#define NO_KM
#include <hip/hip_runtime.h>
#include <hip/hip_bf16.h>

typedef __hip_bfloat16 bf16;
typedef __bf16 bf16x8 __attribute__((ext_vector_type(8)));
typedef __bf16 bf16x16 __attribute__((ext_vector_type(16)));
typedef float f32x4 __attribute__((ext_vector_type(4)));
typedef unsigned u32x4 __attribute__((ext_vector_type(4)));
typedef unsigned u32x2 __attribute__((ext_vector_type(2)));
typedef unsigned u32x8 __attribute__((ext_vector_type(8)));
typedef short s16x4 __attribute__((ext_vector_type(4)));
typedef __attribute__((address_space(3))) s16x4* lds_s16x4_p;

constexpr int S_ = 2048, D_ = 640, NH = 32, DH = 128, HD = 4096, FF = 1728, NSEG = 64;
constexpr size_t MB = 1u << 20;
#define NC 1
constexpr size_t WS_XCH = 0;
constexpr size_t WS_LP = 4 * MB;
constexpr size_t WS_CTL = 16 * MB;
constexpr size_t WS_Q = 17 * MB;
constexpr size_t WS_K = WS_Q + 16 * MB;
constexpr size_t WS_U = WS_K + 16 * MB;
constexpr size_t WS_VO = WS_U + 25 * MB;
constexpr size_t WS_KM = WS_VO + 100 * MB;
constexpr size_t WS_P = WS_KM + 4 * MB;
constexpr size_t WS_H = WS_P + 5 * MB;
constexpr size_t WS_G = WS_H + 3 * MB;
constexpr size_t WS_XN = WS_G + 7 * MB;
constexpr size_t WS_ST = WS_XN + 3 * MB;
constexpr size_t WS_CS = WS_ST + 1 * MB;
constexpr size_t WS_END = WS_CS + 1 * MB;

#define SLD 8
#define NCAL 8
#define UAUX 0
#define OAUX 0
#define SAUX 16
#define VSPLIT 1
#define PSKIP 0
#define PREF0 60
#define RNG 16
#define SLP 4
#define T2H 0u
constexpr unsigned C_QH = WS_CTL + 256 * 1, C_PROG = WS_CTL + 256 * 6, C_FIN = WS_CTL + 256 * 7, C_K = WS_CTL + 256 * 8, C_V = WS_CTL + 256 * 24,
                   C_Q = WS_CTL + 256 * 40, C_T2 = WS_CTL + 256 * 56, C_F1 = WS_CTL + 256 * 72, C_T1 = WS_CTL + 256 * 88,
                   C_T2S = WS_CTL + 256 * (152 - 48), C_F1S = WS_CTL + 256 * (168 - 48), C_SEG = WS_CTL + 256 * (184 - 48), C_END = WS_CTL + 256 * 200;
constexpr unsigned C_FILL = C_FIN;
#define PSEG 48
constexpr int PS = PSEG, PSTG = PS / 4, NFILL = NSEG - PS;
constexpr unsigned SENT = 0xFFFFFFFFu;
constexpr unsigned SPIN_CAP = 1u << 18;

__device__ __forceinline__ int sslot(int seg) { return seg % (4 * RNG); }
static_assert(RNG == 16, "all row blocks keep their own slot: projection tasks never wait on later work");
__device__ __forceinline__ unsigned pack2(float lo, float hi) {
  typedef __bf16 b2 __attribute__((ext_vector_type(2)));
  typedef float f2 __attribute__((ext_vector_type(2)));
  return __builtin_bit_cast(unsigned, __builtin_convertvector(f2{lo, hi}, b2));
}
__device__ __forceinline__ u32x4 pack8(f32x4 a, f32x4 b) { return u32x4{pack2(a[0], a[1]), pack2(a[2], a[3]), pack2(b[0], b[1]), pack2(b[2], b[3])}; }
__device__ __forceinline__ float bflo(unsigned w) { return __uint_as_float(w << 16); }
__device__ __forceinline__ float bfhi(unsigned w) { return __uint_as_float(w & 0xffff0000u); }
__device__ __forceinline__ unsigned canon(unsigned w) { return min(w, 0xFFFFFFFEu); }
__device__ __forceinline__ u32x4 canon4(u32x4 v) { return u32x4{canon(v.x), canon(v.y), canon(v.z), canon(v.w)}; }
__device__ __forceinline__ unsigned max4(u32x4 v) { return max(max(v.x, v.y), max(v.z, v.w)); }
__device__ __forceinline__ void glds16(const void* base, unsigned voff, unsigned lds) {
  asm volatile("s_mov_b32 m0, %2\n\ts_nop 0\n\tglobal_load_lds_dwordx4 %0, %1" ::"v"(voff), "s"(base), "s"(__builtin_amdgcn_readfirstlane(lds)) : "m0");
}
__device__ __forceinline__ void glds16c(const void* base, unsigned voff, unsigned lds) {
  asm volatile("s_mov_b32 m0, %2\n\ts_nop 0\n\tglobal_load_lds_dwordx4 %0, %1 sc1" ::"v"(voff), "s"(base), "s"(__builtin_amdgcn_readfirstlane(lds)) : "m0");
}
typedef __amdgpu_buffer_rsrc_t rsrc_t;
__device__ __forceinline__ u32x4 ldc(rsrc_t r, unsigned base, unsigned off) { return __builtin_amdgcn_raw_buffer_load_b128(r, off, base, 16); }
__device__ __forceinline__ u32x2 ldc2(rsrc_t r, unsigned base, unsigned off) { return __builtin_amdgcn_raw_buffer_load_b64(r, off, base, 16); }
__device__ __forceinline__ void stc(rsrc_t r, unsigned base, unsigned off, u32x4 v) {
  __builtin_amdgcn_raw_buffer_store_b128(v, r, off, base, SAUX);
  asm volatile("s_nop 1" ::"v"(v));
}
__device__ __forceinline__ void stc2(rsrc_t r, unsigned base, unsigned off, u32x2 v) { __builtin_amdgcn_raw_buffer_store_b64(v, r, off, base, 16); }
template <int N> __device__ __forceinline__ void wait_v() { asm volatile("s_waitcnt vmcnt(%0)" ::"n"(N) : "memory"); }
__device__ __forceinline__ unsigned lds_addr(const void* p) { return (unsigned)(size_t)(const __attribute__((address_space(3))) char*)p; }
__device__ __forceinline__ u32x2 tr_read(const char* p) {
  s16x4 r = __builtin_amdgcn_ds_read_tr16_b64_v4i16((lds_s16x4_p)(p));
  return __builtin_bit_cast(u32x2, r);
}
#define WAIT_V(n) asm volatile("s_waitcnt vmcnt(" #n ")" ::: "memory")
#define WAIT_L0() asm volatile("s_waitcnt lgkmcnt(0)" ::: "memory")
#define BAR() asm volatile("s_barrier" ::: "memory")
#define LBAR() asm volatile("s_waitcnt lgkmcnt(0)\n\ts_barrier" ::: "memory")
__device__ __forceinline__ f32x4 mfma16(u32x4 a, u32x4 b, f32x4 c) {
  return __builtin_amdgcn_mfma_f32_16x16x32_bf16(__builtin_bit_cast(bf16x8, a), __builtin_bit_cast(bf16x8, b), c, 0, 0, 0);
}
__device__ __forceinline__ f32x4 smf(u32x4 a, u32x8 b, f32x4 c, int idx) {
  return __builtin_amdgcn_smfmac_f32_16x16x64_bf16(__builtin_bit_cast(bf16x8, a), __builtin_bit_cast(bf16x16, b), c, idx, 0, 0);
}
__device__ __forceinline__ unsigned xcc_id() { return (unsigned)__builtin_amdgcn_s_getreg(20 | (3 << 11)) & 0xfu; }

__device__ __forceinline__ unsigned dpp_ror8(unsigned v) { return __builtin_amdgcn_mov_dpp(v, 0x128, 0xf, 0xf, true); }
__device__ __forceinline__ float red_max_q(float v) {
  auto a = __builtin_amdgcn_permlane16_swap(__float_as_uint(v), __float_as_uint(v), false, false);
  v = fmaxf(__uint_as_float(a[0]), __uint_as_float(a[1]));
  auto b = __builtin_amdgcn_permlane32_swap(__float_as_uint(v), __float_as_uint(v), false, false);
  return fmaxf(__uint_as_float(b[0]), __uint_as_float(b[1]));
}
__device__ __forceinline__ float red_sum_q(float v) {
  auto a = __builtin_amdgcn_permlane16_swap(__float_as_uint(v), __float_as_uint(v), false, false);
  v = __uint_as_float(a[0]) + __uint_as_float(a[1]);
  auto b = __builtin_amdgcn_permlane32_swap(__float_as_uint(v), __float_as_uint(v), false, false);
  return __uint_as_float(b[0]) + __uint_as_float(b[1]);
}

__device__ __forceinline__ float red_sum8(float v) {
  v += __uint_as_float(__builtin_amdgcn_mov_dpp(__float_as_uint(v), 0xB1, 0xf, 0xf, true));
  v += __uint_as_float(__builtin_amdgcn_mov_dpp(__float_as_uint(v), 0x4E, 0xf, 0xf, true));
  return v + __uint_as_float(__builtin_amdgcn_mov_dpp(__float_as_uint(v), 0x141, 0xf, 0xf, true));
}
__device__ __forceinline__ int bswz(int row) { return (((row >> 2) ^ (row >> 3)) & 1) << 1; }
__device__ __forceinline__ void stageB_rc(int p, int& row, int& ch) {
  row = ((p >> 7) << 3) | ((p >> 2) & 7);
  ch = (((p >> 5) & 3) << 2) | ((p & 3) ^ bswz(row));
}
template <bool PERM>
__device__ __forceinline__ unsigned trB_base(int lane, int j) {
  const int g = lane >> 4, q = (lane & 15) >> 2, p = lane & 3;
  return PERM ? 2048 * (g >> 1) + 64 * (4 * (g & 1) + q) + 8 * p + 32 * ((j ^ g ^ (g >> 1)) & 1) : 2048 * g + 64 * q + 8 * p + 32 * ((j ^ g) & 1);
}
__device__ __forceinline__ u32x2 tr_read_a(unsigned a) {
  s16x4 r = __builtin_amdgcn_ds_read_tr16_b64_v4i16((lds_s16x4_p)(size_t)a);
  return __builtin_bit_cast(u32x2, r);
}

template <bool SW, bool PERM, int CSTR>
__device__ __forceinline__ void mma_tile(const u32x4& a00, const u32x4& a10, const u32x4& a01, const u32x4& a11, unsigned tb0, unsigned tb1, int so, f32x4 (&acc)[2][4]) {
#pragma unroll
  for (int ks = 0; ks < 2; ++ks) {
    u32x4 b[4];
#pragma unroll
    for (int n = 0; n < 4; ++n) {
      const int o = so + 8192 * ks + CSTR * (n >> 1);
      u32x2 lo = tr_read_a(((n & 1) ? tb1 : tb0) + o), hi = tr_read_a((PERM ? ((n & 1) ? tb1 : tb0) + 4096 : ((n & 1) ? tb0 : tb1) + 256) + o);
      b[n] = u32x4{lo.x, lo.y, hi.x, hi.y};
    }
    const u32x4 a0 = ks ? a01 : a00, a1 = ks ? a11 : a10;
#pragma unroll
    for (int n = 0; n < 4; ++n) {
      acc[0][n] = SW ? mfma16(b[n], a0, acc[0][n]) : mfma16(a0, b[n], acc[0][n]);
      acc[1][n] = SW ? mfma16(b[n], a1, acc[1][n]) : mfma16(a1, b[n], acc[1][n]);
    }
  }
}

__device__ __forceinline__ void ln_tile(const bf16* src  , bf16* dst, int lane) {
  const int fr = lane & 15, fq = lane >> 4;
  u32x4 af[20];
#pragma unroll
  for (int s = 0; s < 20; ++s) af[s] = *(const u32x4*)(src + (size_t)fr * D_ + s * 32 + fq * 8);
  float s = 0.f;
#pragma unroll
  for (int i = 0; i < 20; ++i)
#pragma unroll
    for (int e = 0; e < 4; ++e) s += bflo(af[i][e]) + bfhi(af[i][e]);
  s += __shfl_xor(s, 16); s += __shfl_xor(s, 32);
  const float mean = s * (1.f / 640.f);
  float q = 0.f;
#pragma unroll
  for (int i = 0; i < 20; ++i)
#pragma unroll
    for (int e = 0; e < 4; ++e) { float a = bflo(af[i][e]) - mean, b = bfhi(af[i][e]) - mean; q += a * a + b * b; }
  q += __shfl_xor(q, 16); q += __shfl_xor(q, 32);
  const float rstd = 1.f / sqrtf(q * (1.f / 640.f) + 1e-5f);
#pragma unroll
  for (int i = 0; i < 20; ++i) {
    u32x4 o;
#pragma unroll
    for (int e = 0; e < 4; ++e) o[e] = pack2((bflo(af[i][e]) - mean) * rstd, (bfhi(af[i][e]) - mean) * rstd);
    *(u32x4*)(dst + i * 512 + lane * 8) = o;
  }
}
__device__ __forceinline__ void load_af(const bf16* src, u32x4 (&af)[2][20], int lane) {
#pragma unroll
  for (int s = 0; s < 20; ++s)
#pragma unroll
    for (int m = 0; m < 2; ++m) af[m][s] = *(const u32x4*)(src + (m * 20 + s) * 512 + lane * 8);
}

__device__ __forceinline__ void load_afc(rsrc_t r, unsigned off, u32x4 (&af)[2][20], int lane) {
#pragma unroll
  for (int s = 0; s < 20; ++s)
#pragma unroll
    for (int m = 0; m < 2; ++m) af[m][s] = ldc(r, off, (m * 20 + s) * 1024 + lane * 16);
}
__device__ __forceinline__ void dep_poll(rsrc_t r, unsigned off, unsigned target) {
  const unsigned t0 = (unsigned)__builtin_amdgcn_s_memrealtime();
  while ((unsigned)__builtin_amdgcn_readfirstlane(__builtin_amdgcn_raw_buffer_load_b32(r, 0, off, 16)) < target) {
    if ((unsigned)__builtin_amdgcn_s_memrealtime() - t0 > 5000000u) break;
    __builtin_amdgcn_s_sleep(SLD);
  }
}
struct Dep { unsigned o0, t0, o1, t1; };
__device__ __forceinline__ void dep_wait(rsrc_t r, const Dep& d, int wid) {
  if (wid == 0) { dep_poll(r, d.o0, d.t0); if (d.t1) dep_poll(r, d.o1, d.t1); }
  BAR();
}
__device__ __forceinline__ bool dep_first(rsrc_t r, const Dep& d, int wid, unsigned* bc) {
  if (wid == 0) {
    const unsigned a = __builtin_amdgcn_raw_buffer_load_b32(r, 0, d.o0, 16), b = __builtin_amdgcn_raw_buffer_load_b32(r, 0, d.o1, 16);
    if ((unsigned)__builtin_amdgcn_readfirstlane(a) < d.t0) dep_poll(r, d.o0, d.t0);
    bc[1] = b;
  }
  LBAR();
  return (unsigned)__builtin_amdgcn_readfirstlane(bc[1]) >= d.t1;
}
__device__ __forceinline__ void dep_second(rsrc_t r, const Dep& d, int wid) {
  if (wid == 0) dep_poll(r, d.o1, d.t1);
  BAR();
}
constexpr int RING = 5;
#define ZERO4 f32x4{0.f, 0.f, 0.f, 0.f}
__device__ __forceinline__ void ring_wait(int t, int T) {
  const int rem = T - 1 - t;
  if (rem >= 3) { WAIT_V(6); } else if (rem == 2) { WAIT_V(4); } else if (rem == 1) { WAIT_V(2); } else { WAIT_V(0); }
  LBAR();
}

__device__ __forceinline__ void task_kq(const bf16* x, const bf16* W, rsrc_t wr, unsigned out, int rb, int h0, int nh, char* sm, int tid) {
  const int lane = tid & 63, wid = __builtin_amdgcn_readfirstlane(tid >> 6), wm = wid >> 1, wn = wid & 1;
  const unsigned lw = lds_addr(sm) + wid * 1024;
  const unsigned tb0 = lds_addr(sm) + wn * 1024 + trB_base<false>(lane, 0), tb1 = lds_addr(sm) + wn * 1024 + trB_base<false>(lane, 1);
  int r0, c0, r1, c1; stageB_rc(tid, r0, c0); stageB_rc(tid + 512, r1, c1);
  const unsigned o0 = (r0 * HD + c0 * 8) * 2, o1 = (r1 * HD + c1 * 8) * 2;
  const int T = nh * 10;
  auto issue = [&](int hh, int u) {
    const bf16* b = W + (size_t)(u * 64) * HD + (h0 + hh) * DH;
    const unsigned d = lw + (u % RING) * 16384;
    glds16(b, o0, d); glds16(b, o1, d + 8192);
  };
#pragma unroll
  for (int u = 0; u < RING - 1; ++u) issue(0, u);
  u32x4 af[2][20];
  load_af(x + (size_t)(rb * 8 + wm * 2) * 10240, af, lane);
  for (int hh = 0; hh < nh; ++hh) {
    f32x4 acc[2][4] = {};
#pragma unroll
    for (int u = 0; u < 10; ++u) {
      if (u < 4) { WAIT_V(10); LBAR(); } else ring_wait(hh * 10 + u, T);
      if (u + RING - 1 < 10) issue(hh, u + RING - 1); else if (hh + 1 < nh) issue(hh + 1, u + RING - 1 - 10);
      mma_tile<true, false, 512>(af[0][2 * u], af[1][2 * u], af[0][2 * u + 1], af[1][2 * u + 1], tb0, tb1, (u % RING) * 16384, acc);
    }
    const unsigned o = out + (((sslot(rb * 4) + wm) * NH + h0 + hh) * 8 + 2 * wn) * 1024;
#pragma unroll
    for (int m = 0; m < 2; ++m)
#pragma unroll
      for (int t = 0; t < 2; ++t) stc(wr, o, (m * 4 + t) * 1024 + lane * 16, pack8(acc[m][2 * t], acc[m][2 * t + 1]));
  }
}

constexpr int vvo_allow(int u, int T) {
  int n = 0;
  for (int j = u + 1; j <= u + 3 && j < T; ++j) n += 2;
  for (int j = u - 4; j < u; ++j) if (j >= T / 2 && (j & 1)) n += 8;
  return n;
}
template <int NHH>
__device__ __forceinline__ void task_vvo(const bf16* x, const bf16* Wv, const bf16* Wo, rsrc_t wr, int rb, int h0, char* sm, int tid) {
  const int lane = tid & 63, wid = __builtin_amdgcn_readfirstlane(tid >> 6), wm = wid >> 1, wn = wid & 1;
  const unsigned lw = lds_addr(sm) + wid * 1024;
  const unsigned tb0 = lds_addr(sm) + wn * 1024 + trB_base<false>(lane, 0), tb1 = lds_addr(sm) + wn * 1024 + trB_base<false>(lane, 1);
  const unsigned tp0 = lds_addr(sm) + wn * 1024 + trB_base<true>(lane, 0), tp1 = lds_addr(sm) + wn * 1024 + trB_base<true>(lane, 1);
  char* vbuf = sm + RING * 16384;
  int r0, c0, r1, c1; stageB_rc(tid, r0, c0); stageB_rc(tid + 512, r1, c1);
  const unsigned o0 = (r0 * HD + c0 * 8) * 2, o1 = (r1 * HD + c1 * 8) * 2, p0 = (r0 * D_ + c0 * 8) * 2, p1 = (r1 * D_ + c1 * 8) * 2;
  constexpr int T = NHH * 20;
  auto issue = [&](int t) {
    const unsigned d = lw + (t % RING) * 16384;
    if (t < NHH * 10) {
      const bf16* b = Wv + (size_t)((t % 10) * 64) * HD + (h0 + t / 10) * DH;
      glds16(b, o0, d); glds16(b, o1, d + 8192);
    } else {
      const int v = (t - NHH * 10) % 10, h = h0 + (t - NHH * 10) / 10;
      const bf16* b = Wo + (size_t)(h * DH + (v & 1) * 64) * D_ + (v >> 1) * 128;
      glds16(b, p0, d); glds16(b, p1, d + 8192);
    }
  };
#pragma unroll
  for (int u = 0; u < RING - 1; ++u) issue(u);
  {
    u32x4 af[2][20];
    load_af(x + (size_t)(rb * 8 + wm * 2) * 10240, af, lane);
#pragma unroll
    for (int hh = 0; hh < NHH; ++hh) {
      f32x4 acc[2][4] = {};
#pragma unroll
      for (int u = 0; u < 10; ++u) {
        ring_wait(hh * 10 + u, T);
        issue(hh * 10 + u + RING - 1);
        mma_tile<true, false, 512>(af[0][2 * u], af[1][2 * u], af[0][2 * u + 1], af[1][2 * u + 1], tb0, tb1, (u % RING) * 16384, acc);
      }
#pragma unroll
      for (int m = 0; m < 2; ++m)
#pragma unroll
        for (int t = 0; t < 2; ++t) *(u32x4*)(vbuf + (((hh * 4 + wm) * 2 + m) * 4 + 2 * wn + t) * 1024 + lane * 16) = pack8(acc[m][2 * t], acc[m][2 * t + 1]);
    }
  }
#pragma unroll
  for (int hh = 0; hh < NHH; ++hh) {
    const int h = h0 + hh;
    const int seg = sslot(rb * 4) + wm, O = 5 * h + 1 + (lane >> 4);
    const unsigned vo = ((seg * 80 * 20 + (O >> 3)) * 64 + (O & 3) * 16 + ((O >> 2) & 1) * 8 + (lane & 7)) * 16 + ((lane >> 3) & 1) * 20480;
    const unsigned vh = lane < 16 ? ((seg * 80 * 20 + ((5 * h) >> 3)) * 64 + ((5 * h) & 3) * 16 + (((5 * h) >> 2) & 1) * 8 + (lane & 7)) * 16 + (lane >> 3) * 20480 : 0xF0000000u;
    unsigned sv; asm volatile("v_mov_b32 %0, -1" : "=v"(sv));
    u32x4 vf[2][4];
#pragma unroll
    for (int nt = 0; nt < 5; ++nt) {
      f32x4 acc[2][4] = {};
#pragma unroll
      for (int kh = 0; kh < 2; ++kh) {
        const int u = NHH * 10 + hh * 10 + nt * 2 + kh;
        switch (vvo_allow(u, T)) {
          case 22: WAIT_V(22); break; case 20: WAIT_V(20); break; case 18: WAIT_V(18); break; case 16: WAIT_V(16); break; case 14: WAIT_V(14); break;
          default: WAIT_V(6); break;
        }
        LBAR();
        if (u + RING - 1 < T) issue(u + RING - 1);
        if (nt == 0 && kh == 0) {
#pragma unroll
          for (int m = 0; m < 2; ++m)
#pragma unroll
            for (int t = 0; t < 4; ++t) vf[m][t] = *(const u32x4*)(vbuf + (((hh * 4 + wm) * 2 + m) * 4 + t) * 1024 + lane * 16);
        }
        mma_tile<false, true, 512>(vf[0][2 * kh], vf[1][2 * kh], vf[0][2 * kh + 1], vf[1][2 * kh + 1], tp0, tp1, (u % RING) * 16384, acc);
      }
#pragma unroll
      for (int n = 0; n < 4; ++n)
      {
        stc(wr, WS_VO + (nt * 16 + wn * 8 + n * 2) * 20480, vo, pack8(acc[0][n], acc[1][n]));
        stc(wr, WS_VO + (nt * 16 + wn * 8 + n * 2) * 20480, vh, u32x4{sv, sv, sv, sv});
      }
    }
  }
}

__device__ __forceinline__ void task_u(const bf16* Wv, const bf16* Wo, bf16* U, int h, int db, char* sm, int tid) {
  const int lane = tid & 63, wid = __builtin_amdgcn_readfirstlane(tid >> 6), wm = wid >> 1, wn = wid & 1;
  const unsigned lw = lds_addr(sm) + wid * 1024;
  const unsigned tb0 = lds_addr(sm) + wn * 1024 + trB_base<false>(lane, 0), tb1 = lds_addr(sm) + wn * 1024 + trB_base<false>(lane, 1);
  int r0, c0, r1, c1; stageB_rc(tid, r0, c0); stageB_rc(tid + 512, r1, c1);
  const unsigned p0 = (r0 * D_ + c0 * 8) * 2, p1 = (r1 * D_ + c1 * 8) * 2;
  auto issue = [&](int v) {
    const unsigned d = lw + (v % RING) * 16384;
    const bf16* b = Wo + (size_t)(h * DH + (v & 1) * 64) * D_ + (v >> 1) * 128;
    glds16(b, p0, d); glds16(b, p1, d + 8192);
  };
#pragma unroll
  for (int u = 0; u < RING - 1; ++u) issue(u);
  u32x4 vf[2][4];
#pragma unroll
  for (int m = 0; m < 2; ++m)
#pragma unroll
    for (int s = 0; s < 4; ++s) vf[m][s] = *(const u32x4*)(Wv + (size_t)(db * 128 + wm * 32 + m * 16 + (lane & 15)) * HD + h * DH + s * 32 + (lane >> 4) * 8);
#pragma unroll
  for (int nt = 0; nt < 5; ++nt) {
    f32x4 acc[2][4] = {};
#pragma unroll
    for (int kh = 0; kh < 2; ++kh) {
      const int v = nt * 2 + kh;
      ring_wait(v, 10);
      if (v + RING - 1 < 10) issue(v + RING - 1);
      mma_tile<true, false, 512>(vf[0][2 * kh], vf[1][2 * kh], vf[0][2 * kh + 1], vf[1][2 * kh + 1], tb0, tb1, (v % RING) * 16384, acc);
    }
#pragma unroll
    for (int m = 0; m < 2; ++m)
#pragma unroll
      for (int t = 0; t < 2; ++t) {
        const int d1 = db * 128 + wm * 32 + m * 16 + (lane & 15);
        const int c8 = nt * 16 + wn * 8 + t * 4 + ((lane >> 4) & 1) * 2 + (lane >> 5);
        auto s0 = __builtin_amdgcn_permlane16_swap(pack2(acc[m][2 * t][0], acc[m][2 * t][1]), pack2(acc[m][2 * t + 1][0], acc[m][2 * t + 1][1]), false, false);
        auto s1 = __builtin_amdgcn_permlane16_swap(pack2(acc[m][2 * t][2], acc[m][2 * t][3]), pack2(acc[m][2 * t + 1][2], acc[m][2 * t + 1][3]), false, false);
        __builtin_amdgcn_raw_buffer_store_b128(u32x4{s0[0], s1[0], s0[1], s1[1]}, __builtin_amdgcn_make_buffer_rsrc(U, 0, 25 * MB, 0x00020000), ((c8 * NH + h) * D_ + d1) * 16, 0, UAUX);
      }
  }
  LBAR();
}

__device__ __forceinline__ void wave_t1(rsrc_t wr, const char* ql, const u32x4 (&kf)[2][4], const u32x4 (&km)[4], int seg, int h, int lane) {
  const int fr = lane & 15, fq = lane >> 4;
  const float cs = 0.08838834764831845f * 1.4426950408889634f;
#pragma unroll
  for (int i = 0; i < 2; ++i) {
    f32x4 sc[3] = {};
#pragma unroll
    for (int t = 0; t < 4; ++t) {
      const u32x4 qf = *(const u32x4*)(ql + (i * 4 + t) * 1024 + lane * 16);
      sc[0] = mfma16(km[t], qf, sc[0]); sc[1] = mfma16(kf[0][t], qf, sc[1]); sc[2] = mfma16(kf[1][t], qf, sc[2]);
    }
    const int qi = i * 16 + fr;
    float mx = -INFINITY;
#pragma unroll
    for (int r = 0; r < 4; ++r) {
      if (fq >= 2) sc[0][r] = -INFINITY;
      if (fq * 4 + r > qi) sc[1][r] = -INFINITY;
      if (16 + fq * 4 + r > qi) sc[2][r] = -INFINITY;
      mx = fmaxf(mx, fmaxf(sc[0][r], fmaxf(sc[1][r], sc[2][r])));
    }
    mx = red_max_q(mx);
    float sum = 0.f;
#pragma unroll
    for (int k = 0; k < 3; ++k)
#pragma unroll
      for (int r = 0; r < 4; ++r) { sc[k][r] = __builtin_amdgcn_exp2f((sc[k][r] - mx) * cs); sum += sc[k][r]; }
    sum = red_sum_q(sum);
    const float inv = 1.f / sum;
    const unsigned prow = ((seg * 2 + i) * 160 + 5 * h) * 256 + fr * 16;
    stc(wr, WS_P, prow + (1 + fq) * 256, canon4(pack8(sc[1] * inv, sc[2] * inv)));
    if (fq < 2) { u32x2 o2 = {canon(pack2(sc[0][0] * inv, sc[0][1] * inv)), canon(pack2(sc[0][2] * inv, sc[0][3] * inv))}; stc2(wr, WS_P, prow + fq * 8, o2); }
    __builtin_amdgcn_sched_barrier(0);
  }
}

__device__ __forceinline__ void task_t2(const bf16* x, rsrc_t wr, const Dep& dep, unsigned* bc, int seg, int cs, char* sm, int tid) {
  const int lane = tid & 63, wid = __builtin_amdgcn_readfirstlane(tid >> 6), fr = lane & 15, fq = lane >> 4;
  const bool rdy = dep_first(wr, dep, wid, bc);
  f32x4 acc[2][4] = {};
  u32x4 a[5][2], b[5][4];
#pragma unroll
  for (int k = 0; k < 5; ++k) {
    const int O = (wid * 5 + k) * 4 + fq;
#pragma unroll
    for (int n = 0; n < 4; ++n) {
      const int col = cs * 64 + n * 16 + fr;
      b[k][n] = ldc(wr, WS_VO, (((sslot(seg) * 80 + (col >> 3)) * 20 + (O >> 3)) * 64 + (O & 3) * 16 + ((O >> 2) & 1) * 8 + (col & 7)) * 16);
    }
  }
  const u32x4 xv = *(const u32x4*)(x + (size_t)(seg * 32 + ((tid >> 3) & 31)) * D_ + cs * 64 + (tid & 7) * 8);
  if (!rdy) dep_second(wr, dep, wid);
  const unsigned t0 = (unsigned)__builtin_amdgcn_s_memrealtime();
  unsigned pend = rdy ? 0x3ffu : 0x7ffu;
  for (;;) {
    if (pend >> 10) {
#pragma unroll
      for (int k = 0; k < 5; ++k) {
        const int O = (wid * 5 + k) * 4 + fq;
        if (O % 5 == 0) {
#pragma unroll
          for (int n = 0; n < 4; ++n) {
            const int col = cs * 64 + n * 16 + fr;
            b[k][n] = ldc(wr, WS_VO, (((sslot(seg) * 80 + (col >> 3)) * 20 + (O >> 3)) * 64 + (O & 3) * 16 + ((O >> 2) & 1) * 8 + (col & 7)) * 16);
          }
        }
      }
    }
#pragma unroll
    for (int k = 0; k < 5; ++k)
#pragma unroll
      for (int m = 0; m < 2; ++m) if (pend >> (k * 2 + m) & 1) a[k][m] = ldc(wr, WS_P, ((seg * 2 + m) * 40 + wid * 5 + k) * 1024 + lane * 16);
    if (seg < PS) break;
    unsigned mx = 0;
#pragma unroll
    for (int k = 0; k < 5; ++k)
      if (((wid * 5 + k) * 4 + fq) % 5 == 0) mx = max(max(mx, max(max4(b[k][0]), max4(b[k][1]))), max(max4(b[k][2]), max4(b[k][3])));
    pend = __any(mx == SENT) ? 0x400u : 0u;
#pragma unroll
    for (int k = 0; k < 5; ++k)
#pragma unroll
      for (int m = 0; m < 2; ++m) if (__any(max4(a[k][m]) == SENT)) pend |= 1u << (k * 2 + m);
    if (!pend || (unsigned)__builtin_amdgcn_s_memrealtime() - t0 > 5000000u) break;
    if (SLP) __builtin_amdgcn_s_sleep(SLP);
  }
#pragma unroll
  for (int k = 0; k < 5; ++k)
#pragma unroll
    for (int m = 0; m < 2; ++m)
#pragma unroll
      for (int n = 0; n < 4; ++n) acc[m][n] = mfma16(b[k][n], a[k][m], acc[m][n]);
  float* red = (float*)sm;
#pragma unroll
  for (int m = 0; m < 2; ++m)
#pragma unroll
    for (int n = 0; n < 4; ++n) *(f32x4*)(red + (wid * 32 + m * 16 + fr) * 68 + n * 16 + fq * 4) = acc[m][n];
  LBAR();
  if (tid < 256) {
    const int row = tid >> 3, c8 = (tid & 7) * 8;
    const int col = cs * 64 + c8, grow = seg * 32 + row;
    f32x4 s0 = *(const f32x4*)(red + row * 68 + c8), s1 = *(const f32x4*)(red + row * 68 + c8 + 4);
#pragma unroll
    for (int w = 1; w < 8; ++w) { s0 += *(const f32x4*)(red + (w * 32 + row) * 68 + c8); s1 += *(const f32x4*)(red + (w * 32 + row) * 68 + c8 + 4); }
    const u32x4 av = pack8(s0, s1);
    u32x4 o;
#pragma unroll
    for (int e = 0; e < 4; ++e) o[e] = canon(pack2(bflo(xv[e]) + bflo(av[e]), bfhi(xv[e]) + bfhi(av[e])));
    stc(wr, WS_H, ((((grow >> 4) * 20 + (col >> 5)) * 4 + ((col >> 3) & 3)) * 16 + (grow & 15)) * 16, o);
    float sm8 = 0.f;
#pragma unroll
    for (int e = 0; e < 4; ++e) sm8 += bflo(o[e]) + bfhi(o[e]);
    sm8 = red_sum8(sm8);
    const float mu = sm8 * (1.f / 64.f);
    float m2 = 0.f;
#pragma unroll
    for (int e = 0; e < 4; ++e) { const float a = bflo(o[e]) - mu, b = bfhi(o[e]) - mu; m2 += a * a + b * b; }
    m2 = red_sum8(m2);
    if ((tid & 7) == 0) stc2(wr, WS_ST, (grow * 10 + cs) * 8, u32x2{canon(__float_as_uint(sm8)), canon(__float_as_uint(m2))});
  }
}

__device__ __forceinline__ void task_f1(rsrc_t wr, const Dep& dep, const float* CS, const bf16* Wg, const bf16* Wu, int rb, int f0, int nt, char* sm, int tid) {
  const int lane = tid & 63, wid = __builtin_amdgcn_readfirstlane(tid >> 6), wm = wid >> 1, wn = wid & 1;
  const unsigned lw = lds_addr(sm) + wid * 1024;
  const unsigned tb0 = lds_addr(sm) + wn * 512 + trB_base<false>(lane, 0), tb1 = lds_addr(sm) + wn * 512 + trB_base<false>(lane, 1);
  int r0, c0, r1, c1; stageB_rc(tid, r0, c0); stageB_rc(tid + 512, r1, c1);
  const bf16* pw = (wid & 1) ? Wu : Wg;
  const unsigned o0 = (r0 * FF + (c0 & 7) * 8) * 2, o1 = (r1 * FF + (c1 & 7) * 8) * 2;
  const int T = nt * 10;
  auto issue = [&](int ff, int u) {
    const size_t o = (size_t)(u * 64) * FF + (f0 + ff) * 64;
    const unsigned d = lw + (u % RING) * 16384;
    glds16(pw + o, o0, d); glds16(pw + o, o1, d + 8192);
  };
#pragma unroll
  for (int u = 0; u < RING - 1; ++u) issue(0, u);
  u32x4 af[2][20];
  float* lcs = (float*)(sm + RING * 16384);
  if (tid < nt * 128) lcs[tid] = CS[((tid >> 6) & 1) * FF + (f0 + (tid >> 7)) * 64 + (tid & 63)];
  dep_wait(wr, dep, wid);
  load_afc(wr, (unsigned)WS_H + (rb * 8 + wm * 2) * 20480, af, lane);
  float mean[2], rstd[2];
#pragma unroll
  for (int m = 0; m < 2; ++m) {
    const unsigned sp = (rb * 128 + wm * 32 + m * 16 + (lane & 15)) * 80;
    f32x4 p[5];
#pragma unroll
    for (int i = 0; i < 5; ++i) p[i] = __builtin_bit_cast(f32x4, ldc(wr, WS_ST, sp + i * 16));
    float s = 0.f;
#pragma unroll
    for (int i = 0; i < 5; ++i) s += p[i][0] + p[i][2];
    mean[m] = s * (1.f / 640.f);
    float q = 0.f;
#pragma unroll
    for (int i = 0; i < 5; ++i) {
      const float d0 = p[i][0] * (1.f / 64.f) - mean[m], d1 = p[i][2] * (1.f / 64.f) - mean[m];
      q += p[i][1] + p[i][3] + 64.f * (d0 * d0 + d1 * d1);
    }
    rstd[m] = 1.f / sqrtf(q * (1.f / 640.f) + 1e-5f);
  }
  for (int ff = 0; ff < nt; ++ff) {
    f32x4 acc[2][4] = {};
#pragma unroll
    for (int u = 0; u < 10; ++u) {
      if (u < 4) { WAIT_V(8); LBAR(); } else ring_wait(ff * 10 + u, T);
      if (u + RING - 1 < 10) issue(ff, u + RING - 1); else if (ff + 1 < nt) issue(ff + 1, u + RING - 1 - 10);
      mma_tile<true, false, 1024>(af[0][2 * u], af[1][2 * u], af[0][2 * u + 1], af[1][2 * u + 1], tb0, tb1, (u % RING) * 16384, acc);
    }
    f32x4 cs[4];
#pragma unroll
    for (int n = 0; n < 4; ++n) cs[n] = *(const f32x4*)(lcs + ff * 128 + (n >> 1) * 64 + (2 * wn + (n & 1)) * 16 + (lane >> 4) * 4);
#pragma unroll
    for (int m = 0; m < 2; ++m) {
#pragma unroll
      for (int n = 0; n < 4; ++n) acc[m][n] = (acc[m][n] - mean[m] * cs[n]) * rstd[m];
      u32x4 g = pack8(acc[m][0], acc[m][1]), u = pack8(acc[m][2], acc[m][3]), o;
      auto rl = [](float v) { return v > 0.f ? v : 0.f * v; };
#pragma unroll
      for (int e = 0; e < 4; ++e) o[e] = pack2(rl(bflo(g[e])) * bflo(u[e]), rl(bfhi(g[e])) * bfhi(u[e]));
      stc(wr, WS_G, ((rb * 8 + wm * 2 + m) * 54 + (f0 + ff) * 2 + wn) * 1024 + lane * 16, o);
    }
  }
}

__device__ __forceinline__ void task_f2(const bf16* G, const bf16* Wd, rsrc_t wr, bf16* out, int rb, int cs, char* sm, int tid) {
  const int lane = tid & 63, wid = __builtin_amdgcn_readfirstlane(tid >> 6), wm = wid >> 1, wn = wid & 1;
  const unsigned lw = lds_addr(sm) + wid * 1024;
  const unsigned tp0 = lds_addr(sm) + wn * 1024 + trB_base<true>(lane, 0), tp1 = lds_addr(sm) + wn * 1024 + trB_base<true>(lane, 1);
  int r0, c0, r1, c1; stageB_rc(tid, r0, c0); stageB_rc(tid + 512, r1, c1);
  const bf16* pa = G + (size_t)(rb * 8 + wid) * 54 * 512;
  const bf16* pb = Wd + cs * 128;
  const unsigned o0 = (r0 * D_ + c0 * 8) * 2, o1 = (r1 * D_ + c1 * 8) * 2;
  constexpr int R4 = 4;
  auto issue = [&](int t) {
    const unsigned d = lds_addr(sm) + (t % R4) * 32768;
    glds16c(pa + t * 1024, lane * 16, d + wid * 2048); glds16c(pa + t * 1024 + 512, lane * 16, d + wid * 2048 + 1024);
    glds16(pb + (size_t)t * 64 * D_, o0, d + 16384 + wid * 1024); glds16(pb + (size_t)t * 64 * D_, o1, d + 24576 + wid * 1024);
  };
  (void)lw;
  issue(0); issue(1); issue(2);
  f32x4 acc[2][4] = {};
  for (int t = 0; t < 27; ++t) {
    if (t + 2 < 27) { WAIT_V(8); } else if (t + 1 < 27) { WAIT_V(4); } else { WAIT_V(0); }
    LBAR();
    if (t + 3 < 27) issue(t + 3);
    const char* d = sm + (t % R4) * 32768;
    const u32x4 a00 = *(const u32x4*)(d + (wm * 2) * 2048 + lane * 16), a01 = *(const u32x4*)(d + (wm * 2) * 2048 + 1024 + lane * 16);
    const u32x4 a10 = *(const u32x4*)(d + (wm * 2 + 1) * 2048 + lane * 16), a11 = *(const u32x4*)(d + (wm * 2 + 1) * 2048 + 1024 + lane * 16);
    mma_tile<true, true, 512>(a00, a10, a01, a11, tp0 + (t % R4) * 32768, tp1 + (t % R4) * 32768, 16384, acc);
  }
#pragma unroll
  for (int m = 0; m < 2; ++m)
#pragma unroll
    for (int n = 0; n < 4; ++n) {
      const size_t go = (size_t)(rb * 128 + wm * 32 + m * 16 + (lane & 15)) * D_ + cs * 128 + wn * 64 + n * 16 + (lane >> 4) * 4;
      const int col = cs * 128 + wn * 64 + n * 16 + (lane >> 4) * 4;
      u32x2 hv = ldc2(wr, WS_H, ((((rb * 8 + wm * 2 + m) * 20 + (col >> 5)) * 64 + ((col >> 3) & 3) * 16 + (lane & 15)) * 8 + (col & 7)) * 2);
      unsigned a0 = pack2(acc[m][n][0], acc[m][n][1]), a1 = pack2(acc[m][n][2], acc[m][n][3]);
      u32x2 o = {pack2(bflo(hv.x) + bflo(a0), bfhi(hv.x) + bfhi(a0)), pack2(bflo(hv.y) + bflo(a1), bfhi(hv.y) + bfhi(a1))};
      __builtin_amdgcn_raw_buffer_store_b64(o, __builtin_amdgcn_make_buffer_rsrc(out, 0, S_ * D_ * 2, 0x00020000), (unsigned)go * 2, 0, OAUX);
    }
}

__device__ __forceinline__ void task_f1p(rsrc_t wr, const float* CS, const bf16* Wg, const bf16* Wu, int j, char* sm, int tid) {
  const int lane = tid & 63, wid = __builtin_amdgcn_readfirstlane(tid >> 6), kq = wid & 3, ch = wid >> 2, fr = lane & 15, fq = lane >> 4;
  u32x4 w[4][5];
#pragma unroll
  for (int gc = 0; gc < 4; ++gc)
#pragma unroll
    for (int i = 0; i < 5; ++i) {
      const unsigned short* p = (const unsigned short*)((gc >> 1) ? Wu : Wg) + (size_t)((kq * 5 + i) * 32 + fq * 8) * FF + j * 32 + (gc & 1) * 16 + fr;
#pragma unroll
      for (int e = 0; e < 4; ++e) w[gc][i][e] = (unsigned)p[(2 * e) * FF] | ((unsigned)p[(2 * e + 1) * FF] << 16);
    }
  f32x4 cs[2][2];
  dep_wait(wr, Dep{C_FILL, NFILL, 0, 0}, wid);
#pragma unroll
  for (int gu = 0; gu < 2; ++gu)
#pragma unroll
    for (int c = 0; c < 2; ++c) cs[gu][c] = *(const f32x4*)(CS + gu * FF + j * 32 + c * 16 + fq * 4);
  for (int seg = PS; seg < NSEG; seg += 2) {
    const unsigned t0 = (unsigned)__builtin_amdgcn_s_memrealtime();
    u32x4 p_[5] = {};
    const bool last = seg == NSEG - 2;
    if (last ? wid == 7 : wid < 4) {
      for (;;) {
        unsigned mx = 0;
        if (last) mx = max4(ldc(wr, WS_P, (NSEG - 1) * 81920 + lane * 16));
        else {
#pragma unroll
          for (int i = 0; i < 5; ++i) p_[i] = ldc(wr, WS_ST, (seg * 32 + wid * 16 + fr) * 80 + i * 16);
#pragma unroll
          for (int i = 0; i < 5; ++i) mx = max(mx, max4(p_[i]));
        }
        if (!__any(mx == SENT) || (unsigned)__builtin_amdgcn_s_memrealtime() - t0 > 5000000u) break;
        __builtin_amdgcn_s_sleep(SLP);
      }
    }
    BAR();
    u32x4 a[2][5];
    unsigned pend = last && wid < 4 ? 0x7ffu : 0x3ffu;
    for (;;) {
#pragma unroll
      for (int rt = 0; rt < 2; ++rt)
#pragma unroll
        for (int i = 0; i < 5; ++i) if (pend >> (rt * 5 + i) & 1) a[rt][i] = ldc(wr, WS_H, ((seg * 2 + ch * 2 + rt) * 20 + kq * 5 + i) * 1024 + lane * 16);
      if (pend >> 10) {
#pragma unroll
        for (int i = 0; i < 5; ++i) p_[i] = ldc(wr, WS_ST, (seg * 32 + wid * 16 + fr) * 80 + i * 16);
      }
      unsigned mx = 0;
#pragma unroll
      for (int i = 0; i < 5; ++i) mx = max(mx, max4(p_[i]));
      pend = __any(mx == SENT) ? 0x400u : 0u;
#pragma unroll
      for (int rt = 0; rt < 2; ++rt)
#pragma unroll
        for (int i = 0; i < 5; ++i) if (__any(max4(a[rt][i]) == SENT)) pend |= 1u << (rt * 5 + i);
      if (!pend || (unsigned)__builtin_amdgcn_s_memrealtime() - t0 > 5000000u) break;
    }
    f32x4 p[5];
#pragma unroll
    for (int i = 0; i < 5; ++i) p[i] = __builtin_bit_cast(f32x4, p_[i]);
#pragma unroll
    for (int rt = 0; rt < 2; ++rt)
#pragma unroll
      for (int gc = 0; gc < 4; ++gc) {
        f32x4 acc = ZERO4;
#pragma unroll
        for (int i = 0; i < 5; ++i) acc = mfma16(w[gc][i], a[rt][i], acc);
        *(f32x4*)(sm + (((ch * 2 + rt) * 4 + gc) * 4 + kq) * 1024 + lane * 16) = acc;
      }
    LBAR();
    if (wid < 4) {
      float s = 0.f;
#pragma unroll
      for (int i = 0; i < 5; ++i) s += p[i][0] + p[i][2];
      const float mean = s * (1.f / 640.f);
      float q = 0.f;
#pragma unroll
      for (int i = 0; i < 5; ++i) {
        const float d0 = p[i][0] * (1.f / 64.f) - mean, d1 = p[i][2] * (1.f / 64.f) - mean;
        q += p[i][1] + p[i][3] + 64.f * (d0 * d0 + d1 * d1);
      }
      const float rstd = 1.f / sqrtf(q * (1.f / 640.f) + 1e-5f);
      f32x4 v[2][2];
#pragma unroll
      for (int gu = 0; gu < 2; ++gu)
#pragma unroll
        for (int c = 0; c < 2; ++c) {
          const char* b = sm + ((wid * 2 + gu) * 2 + c) * 4096 + lane * 16;
          v[gu][c] = (*(const f32x4*)b + *(const f32x4*)(b + 1024)) + (*(const f32x4*)(b + 2048) + *(const f32x4*)(b + 3072));
          v[gu][c] = (v[gu][c] - mean * cs[gu][c]) * rstd;
        }
      u32x4 g = pack8(v[0][0], v[0][1]), u = pack8(v[1][0], v[1][1]), o;
      auto rl = [](float x) { return x > 0.f ? x : 0.f * x; };
#pragma unroll
      for (int e = 0; e < 4; ++e) o[e] = canon(pack2(rl(bflo(g[e])) * bflo(u[e]), rl(bfhi(g[e])) * bfhi(u[e])));
      stc(wr, WS_G, ((seg * 2 + wid) * 54 + j) * 1024 + lane * 16, o);
    }
  }
}
__device__ __forceinline__ void task_f2p(rsrc_t wr, const bf16* Wd, bf16* out, int j, int par, char* sm, int tid) {
  const int lane = tid & 63, wid = __builtin_amdgcn_readfirstlane(tid >> 6), fr = lane & 15, fq = lane >> 4;
  u32x4 w[2][7];
#pragma unroll
  for (int i = 0; i < 7; ++i) {
    const int ks = min(wid + 8 * i, 53);
#pragma unroll
    for (int ct = 0; ct < 2; ++ct) {
      const unsigned short* p = (const unsigned short*)Wd + (size_t)(ks * 32 + fq * 4) * D_ + j * 32 + (fr >> 2) * 8 + ct * 4 + (fr & 3);
#pragma unroll
      for (int e = 0; e < 4; ++e) {
        const int k0 = (e >> 1) * 16 + (e & 1) * 2;
        w[ct][i][e] = wid + 8 * i < 54 ? (unsigned)p[k0 * D_] | ((unsigned)p[(k0 + 1) * D_] << 16) : 0u;
      }
    }
  }
  dep_wait(wr, Dep{C_FILL, NFILL, 0, 0}, wid);
  for (int seg = PS + par; seg < NSEG; seg += 2) {
    BAR();
    const unsigned t0 = (unsigned)__builtin_amdgcn_s_memrealtime();
    u32x4 a[2][7];
    u32x4 hv;
    for (;;) {
      hv = ldc(wr, WS_H, (((seg * 2 + (wid & 1)) * 20 + j) * 64 + lane) * 16);
      if (!__any(max4(hv) == SENT) || (unsigned)__builtin_amdgcn_s_memrealtime() - t0 > 5000000u) break;
      __builtin_amdgcn_s_sleep(SLP);
    }
    unsigned pend = 0x3fffu;
    for (;;) {
#pragma unroll
      for (int rt = 0; rt < 2; ++rt)
#pragma unroll
        for (int i = 0; i < 7; ++i) if (pend >> (rt * 7 + i) & 1) a[rt][i] = ldc(wr, WS_G, ((seg * 2 + rt) * 54 + min(wid + 8 * i, 53)) * 1024 + lane * 16);
      pend = 0;
#pragma unroll
      for (int rt = 0; rt < 2; ++rt)
#pragma unroll
        for (int i = 0; i < 7; ++i) if (__any(max4(a[rt][i]) == SENT)) pend |= 1u << (rt * 7 + i);
      if (!pend || (unsigned)__builtin_amdgcn_s_memrealtime() - t0 > 5000000u) break;
    }
#pragma unroll
    for (int rt = 0; rt < 2; ++rt)
#pragma unroll
      for (int ct = 0; ct < 2; ++ct) {
        f32x4 acc = ZERO4;
#pragma unroll
        for (int i = 0; i < 7; ++i) acc = mfma16(w[ct][i], a[rt][i], acc);
        *(f32x4*)(sm + ((rt * 2 + ct) * 8 + wid) * 1024 + lane * 16) = acc;
      }
    LBAR();
    if (wid < 2) {
      f32x4 v[2];
#pragma unroll
      for (int ct = 0; ct < 2; ++ct) {
        const char* b = sm + (wid * 2 + ct) * 8192 + lane * 16;
        v[ct] = *(const f32x4*)b;
#pragma unroll
        for (int i = 1; i < 8; ++i) v[ct] += *(const f32x4*)(b + i * 1024);
      }
      const u32x4 av = pack8(v[0], v[1]);
      u32x4 o;
#pragma unroll
      for (int e = 0; e < 4; ++e) o[e] = pack2(bflo(hv[e]) + bflo(av[e]), bfhi(hv[e]) + bfhi(av[e]));
      __builtin_amdgcn_raw_buffer_store_b128(o, __builtin_amdgcn_make_buffer_rsrc(out, 0, S_ * D_ * 2, 0x00020000), ((seg * 32 + wid * 16 + fr) * D_ + j * 32 + fq * 8) * 2, 0, OAUX);
    }
  }
}

__device__ __forceinline__ void task_fill(rsrc_t wr, int seg, int tid) {
  unsigned sv; asm volatile("v_mov_b32 %0, -1" : "=v"(sv));
  const u32x4 S = {sv, sv, sv, sv};
#pragma unroll
  for (int i = 0; i < 10; ++i) stc(wr, WS_P + seg * 81920, (i * 512 + tid) * 16, S);
#pragma unroll
  for (int i = 0; i < 5; ++i) stc(wr, WS_H + seg * 40960, (i * 512 + tid) * 16, S);
  if (tid < 160) stc(wr, WS_ST + seg * 2560, tid * 16, S);
#pragma unroll
  for (int i = 0; i < 14; ++i) if (i * 512 + tid < 6912) stc(wr, WS_G + seg * 110592, (i * 512 + tid) * 16, S);
}

__device__ __forceinline__ unsigned xoff(int step, unsigned o, unsigned par) {
  return (unsigned)step * 65536u + ((o >> 13) << 14) + (o & 8191u) + (((par >> (step >> 5)) & 1u) << 13);
}

__global__ void __launch_bounds__(512, 2) k_prologue(const bf16* x, const bf16* mem0, const bf16* Wv, const bf16* Wo, const bf16* Wg, const bf16* Wu, char* ws) {
  extern __shared__ __attribute__((aligned(16))) char sm[];
  const int tid = threadIdx.x, bid = blockIdx.x;
  if (bid == 0 || bid == 236) {
    const int g = bid ? 1 : 0;
    if (tid < 64) {
      const __amdgpu_buffer_rsrc_t r = __builtin_amdgcn_make_buffer_rsrc(ws + WS_XCH, 0, NC << 22, 0x00020000);
      unsigned best[2] = {~0u, ~0u};
      for (int k = 0; k < NCAL; ++k)
        for (int p = 0; p < 2; ++p) {
          unsigned off = (g << 21) + (k * 4 + 1) * 65536 + 3 * 16384 + p * 8192 + 6144 + 512;
          asm volatile("s_waitcnt vmcnt(0)" ::: "memory");
          unsigned long long t0 = __builtin_amdgcn_s_memtime();
          unsigned v = __builtin_amdgcn_raw_buffer_load_b32(r, off, 0, 16);
          asm volatile("s_waitcnt vmcnt(0)" ::"v"(v) : "memory");
          unsigned long long t1 = __builtin_amdgcn_s_memtime();
          unsigned d = (unsigned)(t1 - t0); if (d < best[p]) best[p] = d;
        }
      const unsigned bit = ((best[1] < best[0]) != (xcc_id() >= 4)) ? 1u << g : 0u;
      if (tid == 0) { ((unsigned*)(ws + WS_CTL))[g * 16] = bit; if (!g) ((unsigned*)(ws + WS_CTL))[1] = xcc_id(); }
    } else if (tid >= 256 && !g) {
      for (unsigned o = C_QH + (tid - 256) * 16; o < C_END; o += 256 * 16) *(u32x4*)(ws + o) = u32x4{o < C_QH + 1024 && !(o & 255) ? 36u : 0u, 0u, 0u, 0u};
    }
    return;
  }
  if (bid <= 16) {
    for (int i = tid; i < 4 * NC * 4096; i += 512) {
      const int step = (bid - 1) * 4 * NC + (i >> 12);
      const unsigned po = (i & 4095) * 16;
      const unsigned lo = ((po >> 14) << 13) + (po & 8191u);
      if (lo >= 30720u) continue;
      u32x4 v = {SENT, SENT, SENT, SENT};
      if (step < NC && lo < 10240u) {
        const int b = lo >> 7, row = (lo >> 4) & 7;
        v = *(const u32x4*)(mem0 + row * D_ + b * 8);
        v.x = canon(v.x); v.y = canon(v.y); v.z = canon(v.z); v.w = canon(v.w);
      }
      *(u32x4*)(ws + WS_XCH + (size_t)step * 65536 + po) = v;
    }
    return;
  }
  if (bid <= 48) {
    if (tid < 256) { const int rt = (bid - 17) * 4 + (tid >> 6); ln_tile(x + (size_t)rt * 16 * D_, (bf16*)(ws + WS_XN) + (size_t)rt * 10240, tid & 63); }
    return;
  }
  if (bid <= 75) {
    const int f0 = (bid - 49) * 64, ch = tid & 7, rg = tid >> 3;
    float* red = (float*)sm;
#pragma unroll
    for (int w = 0; w < 2; ++w) {
      const bf16* W = w ? Wu : Wg;
      float a[8] = {};
#pragma unroll
      for (int i = 0; i < 10; ++i) {
        u32x4 v = *(const u32x4*)(W + (size_t)(rg * 10 + i) * FF + f0 + ch * 8);
#pragma unroll
        for (int e = 0; e < 4; ++e) { a[2 * e] += bflo(v[e]); a[2 * e + 1] += bfhi(v[e]); }
      }
      *(f32x4*)(red + (w * 64 + rg) * 64 + ch * 8) = f32x4{a[0], a[1], a[2], a[3]};
      *(f32x4*)(red + (w * 64 + rg) * 64 + ch * 8 + 4) = f32x4{a[4], a[5], a[6], a[7]};
    }
    LBAR();
    if (tid < 128) {
      float t = 0.f;
      for (int i = 0; i < 64; ++i) t += red[((tid >> 6) * 64 + i) * 64 + (tid & 63)];
      ((float*)(ws + WS_CS))[(tid >> 6) * FF + f0 + (tid & 63)] = t;
    }
    return;
  }
  const int t = bid - 76;
  task_u(Wv, Wo, (bf16*)(ws + WS_U), t / 5, t % 5, sm, tid);
}

#define TS(i)
#define TSDECL
#define TSOUT(s)
#define AWIN 8
#define SLT 0
#define STAG 6
#define SLA 36
#define SLM 20
#define SLR 8
#define STL 6
#define DL 1
#define SLV 6
#define VPOS 2
#define SLB 16
__device__ __forceinline__ bool frag_bad(u32x4 v) { return max(max(v.x, v.y), max(v.z, v.w)) == SENT; }
__device__ __forceinline__ void glds16n(const void* base, unsigned voff, unsigned lds) {
  asm volatile("s_mov_b32 m0, %2\n\ts_nop 0\n\tglobal_load_lds_dwordx4 %0, %1 nt" ::"v"(voff), "s"(base), "s"(__builtin_amdgcn_readfirstlane(lds)) : "m0");
}
template <int NL, int DEPTH, int SG, bool LOCAL = false>
__device__ __forceinline__ void poll_lds(const char* xb, const unsigned (&off)[NL], char* const (&dst)[NL], int lane, u32x4 (&v)[NL]) {
  unsigned spins = 0;
#pragma unroll
  for (int d = 0; d < DEPTH - 1; ++d) {
#pragma unroll
    for (int i = 0; i < NL; ++i) { if (LOCAL) glds16n(xb, off[i], lds_addr(dst[i])); else glds16c(xb, off[i], lds_addr(dst[i])); }
    __builtin_amdgcn_s_sleep(SG);
  }
  for (;;) {
    if (LOCAL && (spins & 15) != 15) {
#pragma unroll
      for (int i = 0; i < NL; ++i) glds16n(xb, off[i], lds_addr(dst[i]));
    } else {
#pragma unroll
      for (int i = 0; i < NL; ++i) glds16c(xb, off[i], lds_addr(dst[i]));
    }
    wait_v<NL * (DEPTH - 1)>();
    bool bad = false;
#pragma unroll
    for (int i = 0; i < NL; ++i) v[i] = *(const u32x4*)(dst[i] + lane * 16);
#pragma unroll
    for (int i = 0; i < NL; ++i) bad |= frag_bad(v[i]);
    if (!__any(bad) || ++spins > SPIN_CAP) break;
  }
}
template <int NL>
__device__ __forceinline__ void poll_lds1(const char* xb, const unsigned (&off)[NL], char* const (&dst)[NL], int lane, u32x4 (&v)[NL]) {
  unsigned spins = 0, pend = (1u << NL) - 1u;
  for (;;) {
#pragma unroll
    for (int i = 0; i < NL; ++i) if (pend & (1u << i)) glds16c(xb, off[i], lds_addr(dst[i]));
    WAIT_V(0);
#pragma unroll
    for (int i = 0; i < NL; ++i) v[i] = *(const u32x4*)(dst[i] + lane * 16);
    pend = 0;
#pragma unroll
    for (int i = 0; i < NL; ++i) if (__any(frag_bad(v[i]))) pend |= 1u << i;
    if (!pend || ++spins > SPIN_CAP) break;
  }
}
template <class FB, class FO>
__device__ __forceinline__ void load_wfrags(FB base, FO voff, int fstride, u32x8 (&w)[2][10], char* sm, int tid) {
  const int lane = tid & 63, wid = __builtin_amdgcn_readfirstlane(tid >> 6);
  const int bq = lane >> 4, q = (lane & 15) >> 2, p = lane & 3;
  const unsigned lw = lds_addr(sm) + wid * 1024;
  unsigned vo[4];
#pragma unroll
  for (int i = 0; i < 4; ++i) {
    const int ci = i * 512 + tid, half = i >> 1, k = (ci >> 4) & 63;
    vo[i] = voff(k, half * 16 + ((ci & 15) ^ (((k & 3) << 1) | (((k >> 2) & 1) << 3))));
  }
  auto issue = [&](int f) {
#pragma unroll
    for (int i = 0; i < 4; ++i) glds16((const char*)base(i >> 1) + f * fstride, vo[i], lw + (f & 3) * 32768 + i * 8192);
  };
#pragma unroll
  for (int f = 0; f < 4; ++f) issue(f);
#pragma unroll
  for (int f = 0; f < 10; ++f) {
    if (f < 7) { WAIT_V(12); } else if (f == 7) { WAIT_V(8); } else if (f == 8) { WAIT_V(4); } else { WAIT_V(0); }
    BAR();
#pragma unroll
    for (int t = 0; t < 2; ++t) {
      u32x2 qd[4];
#pragma unroll
      for (int z = 0; z < 4; ++z) {
        const int k = 16 * (bq >> 1) + 4 * (bq & 1) + (z & 1) * 8 + (z >> 1) * 32 + q;
        const int cc = (2 * wid + t) * 2 + (p >> 1);
        qd[z] = tr_read(sm + (f & 3) * 32768 + ((cc >> 4) * 1024 + k * 16 + ((cc & 15) ^ (((k & 3) << 1) | (((k >> 2) & 1) << 3)))) * 16 + 8 * (p & 1));
      }
      w[t][f] = u32x8{qd[0].x, qd[1].x, qd[0].y, qd[1].y, qd[2].x, qd[3].x, qd[2].y, qd[3].y};
    }
    LBAR();
    if (f + 4 < 10) issue(f + 4);
  }
}

template <bool FAST>
__device__ __forceinline__ void poll_mem(const char* xb, int s, unsigned par, char* memA, char* other, int wid, int lane) {
  if (wid < 3) {
    unsigned off[2] = {xoff(s, wid * 1024 + lane * 16, par), xoff(s, (7 + wid) * 1024 + lane * 16, par)};
    char* const dst[2] = {memA + wid * 1024, memA + (7 + wid) * 1024};
    u32x4 v[2];
    if (FAST) {
      poll_lds<2, 2, STAG>(xb, off, dst, lane, v);
      *(u32x4*)(other + wid * 1024 + lane * 16) = u32x4{SENT, SENT, SENT, SENT};
      *(u32x4*)(other + (7 + wid) * 1024 + lane * 16) = u32x4{SENT, SENT, SENT, SENT};
    } else poll_lds1<2>(xb, off, dst, lane, v);
  } else if (wid < 7) {
    unsigned off[1] = {xoff(s, wid * 1024 + lane * 16, par)};
    char* const dst[1] = {memA + wid * 1024};
    u32x4 v[1];
    if (FAST) {
      poll_lds<1, 2, STAG>(xb, off, dst, lane, v);
      *(u32x4*)(other + wid * 1024 + lane * 16) = u32x4{SENT, SENT, SENT, SENT};
    } else poll_lds1<1>(xb, off, dst, lane, v);
  }
}
template <int WIN>
__device__ __forceinline__ f32x4 mem_times_w(const char* memA, const u32x8 (&w)[2][10], int lane) {
  const int sidx = (lane & 15) < 8 ? 0x4444 : 0xEEEE;
  u32x4 a[10];
#pragma unroll
  for (int f = 0; f < WIN; ++f) a[f] = *(const u32x4*)(memA + f * 1024 + lane * 16);
  f32x4 a0 = ZERO4, a1 = ZERO4;
#pragma unroll
  for (int f = 0; f < 10; ++f) {
    a0 = smf(a[f], w[0][f], a0, sidx); a1 = smf(a[f], w[1][f], a1, sidx);
    if (f + WIN < 10) { __builtin_amdgcn_sched_barrier(0); a[f + WIN] = *(const u32x4*)(memA + (f + WIN) * 1024 + lane * 16); }
  }
  f32x4 v;
#pragma unroll
  for (int r = 0; r < 4; ++r) {
    auto sw = __builtin_amdgcn_permlane32_swap(__float_as_uint(a0[r]), __float_as_uint(a1[r]), false, false);
    v[r] = __uint_as_float(sw[0]) + __uint_as_float(sw[1]);
  }
  return v;
}

constexpr int LDS_MAIN = 147456 + 64;
#define TPREF 0
__global__ void __launch_bounds__(512, 2) k_main(const bf16* x, const bf16* Wq, const bf16* Wk, const bf16* Wv, const bf16* Wo, const bf16* Wg, const bf16* Wu,
                                                 const bf16* Wd, char* ws, bf16* out) {
  extern __shared__ __attribute__((aligned(16))) char sm[];
  const int tid = threadIdx.x;
  const int wid = __builtin_amdgcn_readfirstlane(tid >> 6);
  unsigned* ctl = (unsigned*)(ws + WS_CTL);
  const int res = (blockIdx.x + __builtin_amdgcn_readfirstlane(ctl[1])) & 7, bi = blockIdx.x >> 3;
  const int slot = res < 4 && bi < 28 ? res * 28 + bi : 1000;
  const rsrc_t wr = __builtin_amdgcn_make_buffer_rsrc(ws, 0, (unsigned)(WS_END + 8 * MB), 0x00020000);
  auto ltid = [&]() { int l; asm volatile("v_mbcnt_lo_u32_b32 %0, -1, 0\n\tv_mbcnt_hi_u32_b32 %0, -1, %0" : "=v"(l)); return wid * 64 + l; };
  if (slot < 112) {
  const int tid = ltid(), lane = tid & 63;
  const int grp = slot / 28, tsl = slot % 28;
  const int role = tsl < 8 ? grp * 8 + tsl : 32 + grp * 20 + (tsl - 8);
  const unsigned par = ctl[0] | ctl[16];
  const int cpy = role % NC;
  const __amdgpu_buffer_rsrc_t xr = __builtin_amdgcn_make_buffer_rsrc(ws + WS_XCH, 0, NC << 22, 0x00020000);
  const int fr = lane & 15, fq = lane >> 4;
  u32x8 w[2][10];
  char* memA = sm;
  if (role < 32) {
    const int h = role;
    load_wfrags([&](int half) { return (half ? Wk : Wq) + h * DH; }, [&](int k, int cc) { return (unsigned)(k * HD + (cc & 15) * 8) * 2u; }, 64 * HD * 2, w, sm, tid);
    char* qk = sm + 10240;
    char* kring = sm + 16384;
    char* memB = sm + 40960;
    char* qring = sm + 51200;
    const bf16* K = (const bf16*)(ws + WS_K) + (size_t)h * 4096;
    const float cs = 0.08838834764831845f * 1.4426950408889634f;
    auto kdma = [&](int s) {
      const bf16* src = K + (size_t)sslot(s) * NH * 4096;
      const unsigned d = lds_addr(kring) + (s % 3) * 8192;
#pragma unroll
      for (int i = 0; i < 8; ++i) glds16c(src + i * 512, lane * 16, d + i * 1024);
#pragma unroll
      for (int i = 0; i < 8; ++i) glds16c(src - (WS_K - WS_Q) / 2 + i * 512, lane * 16, d + (51200 - 16384) + i * 1024);
    };
    if (wid == 7) { dep_poll(wr, C_K, 32); dep_poll(wr, C_Q, 32); kdma(0); kdma(1); WAIT_V(16); }
    for (int i = tid; i < 640; i += 512) *(u32x4*)(memA + i * 16) = u32x4{SENT, SENT, SENT, SENT};
    LBAR();
    const int Ost = fr < 8 ? 5 * h + 1 + fq : 5 * h;
    const unsigned pst = 10240 + (Ost >> 3) * 1024 + ((Ost & 3) * 16 + ((Ost >> 2) & 1) * 8 + (fr & 7)) * 16;
    for (int s = 0; s < NSEG; ++s) {
      TSDECL; TS(0);
      char* mcur = (s & 1) ? memB : memA;
      u32x4 kf[2][4];
      if (wid == 0 || wid == 7) {
        const char* kt = kring + (s % 3) * 8192 + lane * 16;
#pragma unroll
        for (int t = 0; t < 4; ++t) { kf[0][t] = *(const u32x4*)(kt + t * 1024); kf[1][t] = *(const u32x4*)(kt + 4096 + t * 1024); }
      }
      poll_mem<true>(ws + WS_XCH, s, par, mcur, (s & 1) ? memA : memB, wid, lane);
      if (wid == 7) { WAIT_V(4); }
      TS(1);
      LBAR();
      TS(2);
      if (role == 0 && tid == 384) __builtin_amdgcn_raw_buffer_store_b32(s, wr, C_PROG, 0, 16);
      { f32x4 v = mem_times_w<AWIN>(mcur, w, lane);
        const int col = (2 * wid + (lane >> 5)) * 16 + fr, e = col & 127;
        char* dst = qk + (col >> 7) * 2176 + (fq & 1) * 4 * 272 + (e >> 5) * 64 + ((e >> 2) & 3) * 16 + ((e >> 4) & 1) * 8 + (e & 3) * 2;
#pragma unroll
        for (int r = 0; r < 4; ++r) *(unsigned short*)(dst + r * 272) = (unsigned short)(pack2(v[r], v[r]) & 0xffffu); }
      TS(3);
      LBAR();
      TS(4);
      if (wid == 0) {
        if (s < NSEG - 1) {
          u32x4 qf[4], km[4];
#pragma unroll
          for (int t = 0; t < 4; ++t) {
            qf[t] = *(const u32x4*)(qk + (fr & 7) * 272 + t * 64 + fq * 16);
            km[t] = *(const u32x4*)(qk + 2176 + (fr & 7) * 272 + t * 64 + fq * 16);
          }
          asm volatile("" : "+v"(qf[0]), "+v"(qf[1]), "+v"(qf[2]), "+v"(qf[3]), "+v"(km[0]), "+v"(km[1]), "+v"(km[2]), "+v"(km[3]));
          f32x4 sc[3] = {};
#pragma unroll
          for (int t = 0; t < 4; ++t) { sc[1] = mfma16(kf[0][t], qf[t], sc[1]); sc[2] = mfma16(kf[1][t], qf[t], sc[2]); sc[0] = mfma16(km[t], qf[t], sc[0]); }
          const bool hi = fr >= 8;
          float e[6];
#pragma unroll
          for (int r = 0; r < 4; ++r) e[r] = hi ? sc[2][r] : sc[1][r];
          e[4] = hi ? sc[0][2] : sc[0][0]; e[5] = hi ? sc[0][3] : sc[0][1];
          if (fq >= 2) { e[4] = -INFINITY; e[5] = -INFINITY; }
          float mx = fmaxf(fmaxf(fmaxf(e[0], e[1]), fmaxf(e[2], e[3])), fmaxf(e[4], e[5]));
          mx = fmaxf(mx, __uint_as_float(dpp_ror8(__float_as_uint(mx))));
          mx = red_max_q(mx) * cs;
          float sum = 0.f;
#pragma unroll
          for (int r = 0; r < 6; ++r) { e[r] = __builtin_amdgcn_exp2f(__builtin_fmaf(e[r], cs, -mx)); sum += e[r]; }
          sum += __uint_as_float(dpp_ror8(__float_as_uint(sum)));
          sum = red_sum_q(sum);
          const float inv = __builtin_amdgcn_rcpf(sum);
          const unsigned p0 = canon(pack2(e[0] * inv, e[1] * inv)), p1 = canon(pack2(e[2] * inv, e[3] * inv)), pm = canon(pack2(e[4] * inv, e[5] * inv));
          const unsigned q0 = dpp_ror8(p0), q1 = dpp_ror8(p1), qm = dpp_ror8(pm);
          auto s0 = __builtin_amdgcn_permlane16_swap(qm, qm, false, false);
          auto s1 = __builtin_amdgcn_permlane16_swap(pm, pm, false, false);
          const u32x4 o = hi ? u32x4{qm, pm, s0[1], s1[1]} : u32x4{p0, p1, q0, q1};
          if (fr < 8 || fq == 0) __builtin_amdgcn_raw_buffer_store_b128(o, xr, xoff(s, pst, par), 0, 16);
        }
      } else if (wid == 7) {
        if (s == PS - 1) dep_poll(wr, C_FILL, NFILL);
        if (s + 2 < NSEG) {
          if (!((s + 2) & 3)) { dep_poll(wr, C_K + ((s + 2) >> 2) * 256, 32); dep_poll(wr, C_Q + ((s + 2) >> 2) * 256, 32); }
          kdma(s + 2);
        }
        if (SLT) __builtin_amdgcn_s_sleep(SLT);
        u32x4 km[4];
        const int l7 = ltid() & 63;
#pragma unroll
        for (int t = 0; t < 4; ++t) km[t] = *(const u32x4*)(qk + 2176 + (l7 & 7) * 272 + t * 64 + (l7 >> 4) * 16);
        wave_t1(wr, qring + (s % 3) * 8192, kf, km, s, h, l7);
      }
      TS(5);
      TSOUT(s);
      if (wid < 7) __builtin_amdgcn_s_sleep(SLA);
    }
    WAIT_V(0);
  } else {
    const int b = role - 32;
    const bf16* U = (const bf16*)(ws + WS_U) + (size_t)b * NH * D_ * 8;
    load_wfrags([&](int half) { return U; }, [&](int k, int cc) { return (unsigned)(cc * D_ + k) * 16u; }, 64 * 16, w, sm, tid);
    float* red = (float*)(sm + 10240);
    char* vring = sm + 16384;
    char* pbuf = sm + 16384 + 61440;
    bf16* VO = (bf16*)(ws + WS_VO) + (size_t)b * 20 * 512;
    const int ng = wid < 6 ? 3 : 2;
    auto vdma = [&](int s) {
      const bf16* src = VO + (size_t)sslot(s) * 80 * 20 * 512;
      const unsigned d = lds_addr(vring) + (s % 3) * 20480;
#pragma unroll
      for (int i = 0; i < 20; ++i) glds16c(src + i * 512, lane * 16, d + i * 1024);
    };
    if (wid == 7) { dep_poll(wr, C_V, 32); vdma(0); vdma(1); }
    if (tid < 128) red[(tid >> 1) * 20 + 14 + (tid & 1)] = 0.f;
    for (int s = 0; s < NSEG; ++s) {
      char* vt = vring + (s % 3) * 20480;
      TSDECL; TS(0);
      if (SLM) __builtin_amdgcn_s_sleep(SLM);
      poll_mem<false>(ws + WS_XCH, s, par, memA, memA, wid, lane);
      if (wid == 7) {
        if (VPOS == 2) { __builtin_amdgcn_s_sleep(SLV); vdma(s + 2 < NSEG ? s + 2 : s + 2 - 3); wait_v<40>(); } else { WAIT_V(20); }
      }
      TS(1);
      LBAR();
      TS(2);
      { f32x4 v = mem_times_w<10>(memA, w, lane);
        const int O = 5 * ((2 * wid + (lane >> 5)) * 2 + (fr >> 3));
        *(u32x2*)(vt + (O >> 3) * 1024 + ((O & 3) * 16 + ((O >> 2) & 1) * 8 + (fr & 7)) * 16 + (fq & 1) * 8) = u32x2{pack2(v[0], v[1]), pack2(v[2], v[3])}; }
      TS(3);
      LBAR();
      TS(4);
      if (wid == 7) {
#pragma unroll
        for (int j = 0; j < 4; ++j) {
          const int i = j * 64 + lane, O = 5 * (i >> 3);
          const int off = (O >> 3) * 1024 + ((O & 3) * 16 + ((O >> 2) & 1) * 8 + (i & 7)) * 16;
          stc(wr, WS_VO, (sslot(s) * 80 + b) * 20480 + off, canon4(*(const u32x4*)(vt + off)));
        }
        if (s == PS - 1) dep_poll(wr, C_FILL, NFILL);
        if (!((s + 3) & 3) && s + 3 < NSEG) dep_poll(wr, C_V + ((s + 3) >> 2) * 256, 32);
        if (VPOS == 0) { WAIT_L0(); vdma(s + 2 < NSEG ? s + 2 : s + 2 - 3); }
      } else if (s < NSEG - 1) {
        unsigned off[3]; u32x4 vf[3];
#pragma unroll
        for (int i = 0; i < 3; ++i) {
          const int g = i < ng ? wid + 7 * i : wid;
          off[i] = xoff(s, 10240 + g * 1024 + lane * 16, par);
          vf[i] = *(const u32x4*)(vt + g * 1024 + lane * 16);
        }
        __builtin_amdgcn_s_sleep(SLB);
        char* const dst[3] = {pbuf + wid * 3072, pbuf + wid * 3072 + 1024, pbuf + wid * 3072 + 2048};
        u32x4 pv[3];
        if (DL == 1) poll_lds1<3>(ws + WS_XCH, off, dst, lane, pv); else poll_lds<3, 2, STL>(ws + WS_XCH, off, dst, lane, pv);
        TS(5);
        f32x4 acc = ZERO4;
#pragma unroll
        for (int i = 0; i < 3; ++i) if (i < ng) acc = mfma16(pv[i], vf[i], acc);
        if ((fr >> 3) == (fq >> 1)) {
#pragma unroll
          for (int r = 0; r < 4; ++r) red[(((fq & 1) * 4 + r) * 8 + (fr & 7)) * 20 + wid * 2 + (fr >> 3)] = acc[r];
        }
      }
      if (s < NSEG - 1) {
        LBAR();
        TS(6);
        if (VPOS == 1 && wid == 7) { __builtin_amdgcn_s_sleep(SLV); vdma(s + 2 < NSEG ? s + 2 : s + 2 - 3); }
        if (wid == 0) {
          const f32x4* rp = (const f32x4*)(red + lane * 20);
          f32x4 t0 = rp[0] + rp[1], t1 = rp[2] + rp[3]; t0 += t1;
          float v = (t0[0] + t0[1]) + (t0[2] + t0[3]);
          const float nb = __uint_as_float(__builtin_amdgcn_mov_dpp(__float_as_uint(v), 0xB1, 0xf, 0xf, true));
          u32x4 o; o.x = canon(pack2(v, nb));
          o.y = __builtin_amdgcn_mov_dpp(o.x, 0x4E, 0xf, 0xf, true);
          o.z = __builtin_amdgcn_mov_dpp(o.x, 0x104, 0xf, 0xf, true);
          o.w = __builtin_amdgcn_mov_dpp(o.y, 0x104, 0xf, 0xf, true);
          if (!(lane & 7)) {
#pragma unroll
            for (int c = 0; c < NC; ++c) __builtin_amdgcn_raw_buffer_store_b128(o, xr, xoff((s + 1) * NC + c, b * 128 + lane * 2, par), 0, 16);
          }
        }
      }
      TS(7); TSOUT(s);
    }
    WAIT_V(0);
  }
  LBAR();
  }
  const bf16* xn = (const bf16*)(ws + WS_XN);
  unsigned* bc = (unsigned*)(sm + 147456);
  const int g0 = res & 3;
  int qi = slot < 112 ? 4 : 0;
  constexpr unsigned NPQ = VSPLIT ? 204 : 148, NPOST = NFILL + 640 + PSTG * 19 + 94;
  auto deq = [&](int q) { return __hip_atomic_fetch_add((unsigned*)(ws + C_QH + (q < 4 ? ((g0 + q) & 3) : 4) * 256), 1u, __ATOMIC_RELAXED, __HIP_MEMORY_SCOPE_AGENT); };
  unsigned nxt = res < 4 ? bi + 4 : bi;
  if (slot < 112 && ltid() == 0) nxt = deq(qi);
  for (;;) {
    if (ltid() == 0) *bc = nxt;
    LBAR();
    unsigned t = __builtin_amdgcn_readfirstlane(*bc);
    qi = __builtin_amdgcn_readfirstlane(qi);
    if (qi < 4 && t >= NPQ) { ++qi; BAR(); if (ltid() == 0) nxt = deq(qi); continue; }
    if (qi == 4 && t >= NPOST) break;
    const bool pref = !(PSKIP && (bi & 7) < PSKIP) && qi < 4 && t >= PREF0 && t + 24 < NPQ;
    if (pref && ltid() == 0) nxt = deq(qi);
    const int tl = ltid();
    unsigned sig, inc = 1;
    if (qi < 4) {
      const int hb = ((g0 + qi) & 3) * 8;
      int rb, j, nv, nh;
      if (VSPLIT) {
        if (t < 48) { rb = t >> 4; j = t & 15; nv = 1; nh = 2; } else { rb = 3 + (t - 48) / 12; j = (t - 48) % 12; nv = 1; nh = 4; }
      } else if (t < 32) { rb = t >> 4; j = t & 15; nv = 1; nh = 2; } else if (t < 44) { rb = 2; j = t - 32; nv = 2; nh = 2; }
      else { rb = 3 + ((t - 44) >> 3); j = (t - 44) & 7; nv = 2; nh = 4; }
      rb = __builtin_amdgcn_readfirstlane(rb); j = __builtin_amdgcn_readfirstlane(j); nv = __builtin_amdgcn_readfirstlane(nv); nh = __builtin_amdgcn_readfirstlane(nh);
      if (rb >= RNG) dep_wait(wr, Dep{C_T2 + (rb - RNG) * 256, 40, 0, 0}, wid);
      if (nv == 1 && j < 8) { task_vvo<1>(xn, Wv, Wo, wr, rb, hb + j, sm, tl); sig = C_V + rb * 256; }
      else if (!VSPLIT && nv == 2 && j < 4) { task_vvo<2>(xn, Wv, Wo, wr, rb, hb + 2 * j, sm, tl); sig = C_V + rb * 256; inc = 2; }
      else {
        const int hj = (j - (nv == 1 ? 8 : 4)) * nh;
        task_kq(xn, hj < 8 ? Wk : Wq, wr, hj < 8 ? (unsigned)WS_K : (unsigned)WS_Q, rb, hb + (hj & 7), nh, sm, tl);
        sig = (hj < 8 ? C_K : C_Q) + rb * 256; inc = nh;
      }
    } else {
      int stg = 0; unsigned sbase = 0;
      for (;;) {
        const unsigned sz = (stg == 0 ? NFILL : 0) + (stg == PSTG ? 94 : 0) + 40 + (stg >= 1 && stg <= PSTG ? 14 : 0) + (stg >= 2 && stg <= PSTG + 1 ? 5 : 0);
        if (t - sbase < sz) break;
        sbase += sz; ++stg;
      }
      stg = __builtin_amdgcn_readfirstlane(stg); sbase = __builtin_amdgcn_readfirstlane(sbase);
      t -= sbase;
      int ty = -1, ix = 0;
      auto ent = [&](int k, int id, unsigned n) { if (ty < 0) { if (t < n) { ty = k; ix = id; } else t -= n; } };
      ent(6, 0, stg == 0 ? NFILL : 0); ent(4, 0, stg == PSTG ? 54 : 0); ent(5, 0, stg == PSTG ? 40 : 0);
#pragma unroll
      for (int i = 0; i < 4; ++i) ent(1, stg * 4 + i, 10);
      ent(2, stg - 1, stg >= 1 && stg <= PSTG ? 14 : 0); ent(3, stg - 2, stg >= 2 && stg <= PSTG + 1 ? 5 : 0);
      ty = __builtin_amdgcn_readfirstlane(ty); ix = __builtin_amdgcn_readfirstlane(ix); t = __builtin_amdgcn_readfirstlane(t);
      sig = C_FIN + 128;
      if (ty == 1) {
        task_t2(x, wr, Dep{C_V + (ix >> 2) * 256, 32, C_PROG, ix < PS ? ix + 3u : min(ix + T2H, 63u)}, bc, ix, t, sm, tl);
        sig = C_T2 + (ix >> 2) * 256;
      } else if (ty == 2) {
        task_f1(wr, Dep{C_T2 + ix * 256, 40, 0, 0}, (const float*)(ws + WS_CS), Wg, Wu, ix, t * 2, t < 13 ? 2 : 1, sm, tl); sig = C_F1 + ix * 256;
      } else if (ty == 3) {
        dep_wait(wr, Dep{C_F1 + ix * 256, 14, 0, 0}, wid);
        task_f2((const bf16*)(ws + WS_G), Wd, wr, out, ix, t, sm, tl);
      } else if (ty == 4) {
        task_f1p(wr, (const float*)(ws + WS_CS), Wg, Wu, t, sm, tl);
      } else if (ty == 5) {
        task_f2p(wr, Wd, out, t % 20, t / 20, sm, tl);
      } else {
        task_fill(wr, PS + t, tl); sig = C_FILL;
      }
    }
    WAIT_V(0);
    LBAR();
    if (ltid() == 0) {
      __hip_atomic_fetch_add((unsigned*)(ws + sig), inc, __ATOMIC_RELAXED, __HIP_MEMORY_SCOPE_AGENT);
      if (!pref) nxt = deq(PSKIP && (bi & 7) < PSKIP ? 4 : qi);
    }
    if (PSKIP && (bi & 7) < PSKIP) qi = 4;
  }
}

extern "C" void kernel_launch(void* const* d_in, const int* in_sizes, int n_in, void* d_out, int out_size, void* d_ws, size_t ws_size, hipStream_t stream) {
  const bf16 *x = (const bf16*)d_in[0], *mem0 = (const bf16*)d_in[1], *Wq = (const bf16*)d_in[2], *Wk = (const bf16*)d_in[3], *Wv = (const bf16*)d_in[4], *Wo = (const bf16*)d_in[5];
  const bf16 *Wg = (const bf16*)d_in[8], *Wu = (const bf16*)d_in[9], *Wd = (const bf16*)d_in[10];
  char* ws = (char*)(((size_t)d_ws + (2 * MB - 1)) & ~(size_t)(2 * MB - 1));
  (void)hipFuncSetAttribute((const void*)k_prologue, hipFuncAttributeMaxDynamicSharedMemorySize, 131072);
  (void)hipFuncSetAttribute((const void*)k_main, hipFuncAttributeMaxDynamicSharedMemorySize, LDS_MAIN);
  k_prologue<<<76 + 161, 512, 131072, stream>>>(x, mem0, Wv, Wo, Wg, Wu, ws);
  k_main<<<256, 512, LDS_MAIN, stream>>>(x, Wq, Wk, Wv, Wo, Wg, Wu, Wd, ws, (bf16*)d_out);
}
```
